```python
import torch

def _softmax(x, axis=-1):
    return torch.softmax(x, dim=axis)
def _gelu(x):
    return torch.nn.functional.gelu(x, approximate="tanh")
def _silu(x):
    return torch.nn.functional.silu(x)
def _sigmoid(x):
    return torch.sigmoid(x)
def _layernorm(x, axis=-1, eps=1e-5):
    m = x.mean(dim=axis, keepdim=True)
    v = x.var(dim=axis, keepdim=True, unbiased=False)
    return (x - m) / torch.sqrt(v + eps)
def _scatter_add(tgt, idx, src, axis=0):
    return tgt.clone().index_add_(axis, idx, src)
def _segment_sum(src, seg, n):
    out = torch.zeros((n,) + src.shape[1:], dtype=src.dtype)
    return out.index_add_(0, seg, src)
def _pad(x, pw):
    flat = tuple(v for pair in reversed(pw) for v in pair)
    return torch.nn.functional.pad(x, flat)

kernel_name = 'sequential_preLN__linear_kernel_hedgehog__glu_gated_swiglu__prompt'

def setup_inputs():

    _w = (lambda a: (lambda t: t.to(torch.bfloat16) if t.is_floating_point() else t)(torch.as_tensor(a)))
    def _A_setup():
        B, S, D, H = 1, 2048, 576, 32
        Dh = D // H
        return {
            "x": _w((lambda *s: torch.randn(*s, dtype=torch.bfloat16) * 0.05)(B, S, D)),
            "wqkv": _w((lambda *s: torch.randn(*s, dtype=torch.bfloat16) * 0.2)(D, 3 * D)),
            "w_phi": _w((lambda *s: torch.randn(*s, dtype=torch.bfloat16) * 0.2)(H, Dh, Dh) * (Dh ** -0.5)),
            "wo": _w((lambda *s: torch.randn(*s, dtype=torch.bfloat16) * 0.05)(D, D)),
        }
    _A_in = _A_setup()

    def _B_setup():
        B, S, D, F = 1, 2048, 576, 4608
        return {
            "x": _w((lambda *s: torch.randn(*s, dtype=torch.bfloat16) * 0.05)(B, S, D)),
            "w1": _w((lambda *s: torch.randn(*s, dtype=torch.bfloat16) * 0.4)(D, F) * (2.0 / D) ** 0.5),
            "w3": _w((lambda *s: torch.randn(*s, dtype=torch.bfloat16) * 0.4)(D, F) * (2.0 / D) ** 0.5),
            "w2": _w((lambda *s: torch.randn(*s, dtype=torch.bfloat16) * 0.4)(F, D) * (2.0 / F) ** 0.5),
        }
    _B_in = _B_setup()

    return {
        "A_x": _A_in["x"],
        "A_wqkv": _A_in["wqkv"],
        "A_w_phi": _A_in["w_phi"],
        "A_wo": _A_in["wo"],
        "B_x": _B_in["x"],
        "B_w1": _B_in["w1"],
        "B_w3": _B_in["w3"],
        "B_w2": _B_in["w2"],
    }

def reference(A_x, A_wqkv, A_w_phi, A_wo, B_x, B_w1, B_w3, B_w2):
    def _A_ref(x, wqkv, w_phi, wo):
        B, S, D = x.shape
        H = 32; Dh = D // H
        qkv = (lambda x, *d: x.permute(*d))((x @ wqkv).reshape(B, S, 3, H, Dh), 2, 0, 3, 1, 4)
        q, k, v = qkv[0], qkv[1], qkv[2]
        pq = torch.einsum("bhsd,hdf->bhsf", q, w_phi)
        pk = torch.einsum("bhsd,hdf->bhsf", k, w_phi)
        phi_q = torch.concatenate([_softmax(pq, axis=-1), _softmax(-pq, axis=-1)], axis=-1)
        phi_k = torch.concatenate([_softmax(pk, axis=-1), _softmax(-pk, axis=-1)], axis=-1)
        kv = torch.einsum("bhsf,bhsd->bhfd", phi_k, v)
        z  = torch.einsum("bhsf->bhf", phi_k)
        num = torch.einsum("bhqf,bhfd->bhqd", phi_q, kv)
        den = torch.einsum("bhqf,bhf->bhq", phi_q, z)[..., None] + 1e-6
        o = num / den
        return (lambda x, *d: x.permute(*d))(o, 0, 2, 1, 3).reshape(B, S, D) @ wo

    def _B_ref(x, w1, w3, w2):
        gate = torch.einsum("bsd,df->bsf", x, w1)
        up = torch.einsum("bsd,df->bsf", x, w3)
        h = _silu(gate) * up
        out = torch.einsum("bsf,fd->bsd", h, w2)
        return out

    x = A_x
    h = x + _A_ref(_layernorm(x, axis=-1), A_wqkv, A_w_phi, A_wo)
    h2 = h + _B_ref(_layernorm(h, axis=-1), B_w1, B_w3, B_w2)
    return h2
```

```cpp
#include <hip/hip_runtime.h>
#include <hip/hip_bf16.h>
typedef unsigned short u16;
__device__ __forceinline__ float bf2f(u16 v){ return __uint_as_float(((unsigned)v)<<16); }
__device__ __forceinline__ u16 f2bf(float f){ unsigned u=__float_as_uint(f); u += 0x7fffu + ((u>>16)&1u); return (u16)(u>>16); }
constexpr int S=2048, D=576, H=32, DH=18, F=4608;

__global__ void k_ln(const u16* x, u16* y){
  int r=blockIdx.x; int t=threadIdx.x; float s=0,q=0;
  for(int i=t;i<D;i+=64){ float v=bf2f(x[r*D+i]); s+=v; }
  for(int o=32;o;o>>=1) s+=__shfl_xor(s,o);
  float m=s/D;
  for(int i=t;i<D;i+=64){ float v=bf2f(x[r*D+i])-m; q+=v*v; }
  for(int o=32;o;o>>=1) q+=__shfl_xor(q,o);
  float rs=rsqrtf(q/D+1e-5f);
  for(int i=t;i<D;i+=64){ y[r*D+i]=f2bf((bf2f(x[r*D+i])-m)*rs); }
}
__global__ void k_gemm(const u16* A,const u16* B,u16* C,const u16* res,int M,int N,int K){
  __shared__ float As[16][17], Bs[16][17];
  int tx=threadIdx.x&15, ty=threadIdx.x>>4; int row=blockIdx.y*16+ty, col=blockIdx.x*16+tx; float acc=0;
  for(int k0=0;k0<K;k0+=16){
    As[ty][tx]=bf2f(A[(size_t)row*K+k0+tx]); Bs[ty][tx]=bf2f(B[(size_t)(k0+ty)*N+col]);
    __syncthreads();
    for(int k=0;k<16;k++) acc+=As[ty][k]*Bs[k][tx];
    __syncthreads();
  }
  if(res) acc = bf2f(f2bf(acc)) + bf2f(res[(size_t)row*N+col]);
  C[(size_t)row*N+col]=f2bf(acc);
}
__global__ void k_phi(const u16* qkv,const u16* wphi,float* phiq,float* phik){
  int idx=blockIdx.x*blockDim.x+threadIdx.x; if(idx>=S*H*2) return;
  int which=idx&1; int h=(idx>>1)%H; int s=idx/(2*H);
  const u16* v=qkv+(size_t)s*3*D+which*D+h*DH; float p[DH]; float mx=-1e30f,mn=1e30f;
  for(int f=0;f<DH;f++){ float a=0; for(int d=0;d<DH;d++) a+=bf2f(v[d])*bf2f(wphi[(h*DH+d)*DH+f]); p[f]=a; mx=fmaxf(mx,a); mn=fminf(mn,a);}
  float s1=0,s2=0; float* o=(which?phik:phiq)+((size_t)s*H+h)*36;
  for(int f=0;f<DH;f++){ s1+=expf(p[f]-mx); s2+=expf(mn-p[f]); }
  for(int f=0;f<DH;f++){ o[f]=expf(p[f]-mx)/s1; o[DH+f]=expf(mn-p[f])/s2; }
}
__global__ void k_kv(const float* phik,const u16* qkv,float* kv){
  int h=blockIdx.x; int t=threadIdx.x; if(t>=36*19) return; int f=t/19,d=t%19; float a=0;
  for(int s=0;s<S;s++){ float pv = d<18? bf2f(qkv[(size_t)s*3*D+2*D+h*DH+d]) : 1.f; a+=phik[((size_t)s*H+h)*36+f]*pv; }
  kv[(h*36+f)*19+d]=a;
}
__global__ void k_o(const float* phiq,const float* kv,u16* o){
  int idx=blockIdx.x*blockDim.x+threadIdx.x; if(idx>=S*D) return; int s=idx/D; int c=idx%D; int h=c/DH,d=c%DH; float n=0,den=0;
  for(int f=0;f<36;f++){ float p=phiq[((size_t)s*H+h)*36+f]; n+=p*kv[(h*36+f)*19+d]; den+=p*kv[(h*36+f)*19+18]; }
  o[idx]=f2bf(n/(den+1e-6f));
}
__global__ void k_glu(const u16* g,const u16* u,u16* o,int n){ int i=blockIdx.x*blockDim.x+threadIdx.x; if(i<n){ float a=bf2f(g[i]); o[i]=f2bf(a/(1.f+expf(-a))*bf2f(u[i])); } }

extern "C" void kernel_launch(void* const* d_in, const int* in_sizes, int n_in, void* d_out, int out_size, void* d_ws, size_t ws_size, hipStream_t stream){
  const u16* x=(const u16*)d_in[0]; const u16* wqkv=(const u16*)d_in[1]; const u16* wphi=(const u16*)d_in[2]; const u16* wo=(const u16*)d_in[3];
  const u16* w1=(const u16*)d_in[5]; const u16* w3=(const u16*)d_in[6]; const u16* w2=(const u16*)d_in[7];
  char* ws=(char*)d_ws; size_t off=0; auto al=[&](size_t b){ void* p=ws+off; off+=(b+255)&~(size_t)255; return p; };
  u16* ln=(u16*)al(S*D*2); u16* qkv=(u16*)al(S*3*D*2); float* phiq=(float*)al((size_t)S*H*36*4); float* phik=(float*)al((size_t)S*H*36*4);
  float* kv=(float*)al(H*36*19*4); u16* o=(u16*)al(S*D*2); u16* h=(u16*)al(S*D*2); u16* ln2=(u16*)al(S*D*2);
  u16* g=(u16*)al((size_t)S*F*2); u16* u=(u16*)al((size_t)S*F*2); u16* hh=(u16*)al((size_t)S*F*2);
  k_ln<<<S,64,0,stream>>>(x,ln);
  k_gemm<<<dim3(3*D/16,S/16),256,0,stream>>>(ln,wqkv,qkv,nullptr,S,3*D,D);
  k_phi<<<(S*H*2+255)/256,256,0,stream>>>(qkv,wphi,phiq,phik);
  k_kv<<<H,704,0,stream>>>(phik,qkv,kv);
  k_o<<<(S*D+255)/256,256,0,stream>>>(phiq,kv,o);
  k_gemm<<<dim3(D/16,S/16),256,0,stream>>>(o,wo,h,x,S,D,D);
  k_ln<<<S,64,0,stream>>>(h,ln2);
  k_gemm<<<dim3(F/16,S/16),256,0,stream>>>(ln2,w1,g,nullptr,S,F,D);
  k_gemm<<<dim3(F/16,S/16),256,0,stream>>>(ln2,w3,u,nullptr,S,F,D);
  k_glu<<<(S*F+255)/256,256,0,stream>>>(g,u,hh,S*F);
  k_gemm<<<dim3(D/16,S/16),256,0,stream>>>(hh,w2,(u16*)d_out,h,S,D,F);
}
```

```cpp
#include <hip/hip_runtime.h>
#include <hip/hip_bf16.h>
typedef unsigned short u16;
typedef unsigned int u32;
typedef u32 u32x4 __attribute__((ext_vector_type(4)));
typedef u32 u32x2 __attribute__((ext_vector_type(2)));
typedef float f32x2 __attribute__((ext_vector_type(2)));
typedef float f32x4 __attribute__((ext_vector_type(4)));
typedef float f32x16 __attribute__((ext_vector_type(16)));
typedef __bf16 bf16x2 __attribute__((ext_vector_type(2)));
typedef __bf16 bf16x8 __attribute__((ext_vector_type(8)));
typedef short s16x4 __attribute__((ext_vector_type(4)));
#define DI __device__ __forceinline__
#define LAS __attribute__((address_space(3)))
DI u32 rfl(u32 x){ return __builtin_amdgcn_readfirstlane(x); }
DI u32 cvtpk(float lo, float hi){ f32x2 v={lo,hi}; bf16x2 b=__builtin_convertvector(v,bf16x2); return __builtin_bit_cast(u32,b); }
DI float bflo(u32 w){ return __uint_as_float(w<<16); }
DI float bfhi(u32 w){ return __uint_as_float(w&0xffff0000u); }
DI float dot2(u32 a, u32 b, float c){ return __builtin_amdgcn_fdot2_f32_bf16(__builtin_bit_cast(bf16x2,a),__builtin_bit_cast(bf16x2,b),c,false); }
DI f32x16 mfma32(u32x4 a, u32x4 b, f32x16 c){ return __builtin_amdgcn_mfma_f32_32x32x16_bf16(__builtin_bit_cast(bf16x8,a),__builtin_bit_cast(bf16x8,b),c,0,0,0); }
DI u32x2 trread(u32 addr){ s16x4 v=__builtin_amdgcn_ds_read_tr16_b64_v4i16((LAS s16x4*)(size_t)addr); return __builtin_bit_cast(u32x2,v); }
DI u32x4 trfrag(u32 a0, u32 a1){ u32x2 lo=trread(a0), hi=trread(a1); return u32x4{lo[0],lo[1],hi[0],hi[1]}; }
DI u32x4 ldsr128(u32 addr){ return *(LAS u32x4*)(size_t)addr; }
DI f32x4 ldsr128f(u32 addr){ return *(LAS f32x4*)(size_t)addr; }
DI u32x2 ldsr64(u32 addr){ return *(LAS u32x2*)(size_t)addr; }
DI float ldsr32f(u32 addr){ return *(LAS float*)(size_t)addr; }
DI void ldsw128(u32 addr, u32x4 v){ *(LAS u32x4*)(size_t)addr=v; }
DI void ldsw128f(u32 addr, f32x4 v){ *(LAS f32x4*)(size_t)addr=v; }
DI void ldsw64(u32 addr, u32x2 v){ *(LAS u32x2*)(size_t)addr=v; }
DI void ldsw32f(u32 addr, float v){ *(LAS float*)(size_t)addr=v; }
DI void ldsw16(u32 addr, u16 v){ *(LAS u16*)(size_t)addr=v; }
DI float swap32(float v){ auto r=__builtin_amdgcn_permlane32_swap(__float_as_uint(v),__float_as_uint(v),false,false);
  return __uint_as_float((threadIdx.x&32)? r[0]: r[1]); }
DI float addhalves(float v){ auto r=__builtin_amdgcn_permlane32_swap(__float_as_uint(v),__float_as_uint(v),false,false); return __uint_as_float(r[0])+__uint_as_float(r[1]); }
DI float maxhalves(float v){ auto r=__builtin_amdgcn_permlane32_swap(__float_as_uint(v),__float_as_uint(v),false,false); return fmaxf(__uint_as_float(r[0]),__uint_as_float(r[1])); }
DI float minhalves(float v){ auto r=__builtin_amdgcn_permlane32_swap(__float_as_uint(v),__float_as_uint(v),false,false); return fminf(__uint_as_float(r[0]),__uint_as_float(r[1])); }
#define DMA16(m0v, voff, rsrc, soff) asm volatile("s_mov_b32 m0, %0\n\ts_nop 0\n\tbuffer_load_dwordx4 %1, %2, %3 offen lds"::"s"(m0v),"v"(voff),"s"(rsrc),"s"(soff):"memory")
#define WAITV(n) asm volatile("s_waitcnt vmcnt(" #n ")":::"memory")
#define BAR() do{ asm volatile("s_waitcnt lgkmcnt(0)\n\ts_barrier":::"memory"); }while(0)
DI __amdgpu_buffer_rsrc_t mkrsrc(const void* p, u32 bytes){ return __builtin_amdgcn_make_buffer_rsrc((void*)p,(short)0,(int)bytes,0x00020000); }

#define STAMP_DECL
#define STAMP(i)
#define STAMP_OUT(k,n)

DI u32 lane_id_opaque(){ u32 l; asm volatile("v_mbcnt_lo_u32_b32 %0, -1, 0\n\tv_mbcnt_hi_u32_b32 %0, -1, %0":"=v"(l)); return l; }
#define K3_ASM_BODY \
  "v_mov_b32 v176, %19\n\t" \
  "v_xor_b32 v177, 64, %19\n\t" \
  "v_add_u32 v178, 49152, v176\n\t" \
  "v_add_u32 v179, 49152, v177\n\t" \
  "v_add_u32 v180, 98304, v176\n\t" \
  "v_add_u32 v181, 98304, v177\n\t" \
  "v_mov_b32 v182, %20\n\t" \
  "v_add_u32 v183, 49152, v182\n\t" \
  "v_add_u32 v184, 98304, v182\n\t" \
  "v_mov_b32 v185, %21\n\t" \
  "v_add_u32 v186, 72704, v185\n\t" \
  "v_add_u32 v187, 145408, v185\n\t" \
  "v_add_u32 v188, 218112, v185\n\t" \
  "v_mov_b32 v189, %22\n\t" \
  "v_add_u32 v190, 146432, v189\n\t" \
  "v_add_u32 v191, 292864, v189\n\t" \
  "v_add_u32 v192, 439296, v189\n\t" \
  "v_mov_b32 v193, %23\n\t" \
  "v_add_u32 v194, 8192, v193\n\t" \
  "v_lshlrev_b32 v195, 4, %24\n\t" \
  "s_add_u32 s40, %4, 0\n\t" \
  "s_add_u32 m0, %14, 0\n\t" \
  "s_nop 0\n\t" \
  "buffer_load_dwordx4 v185, %0, s40 offen lds\n\t" \
  "buffer_load_dwordx4 v186, %0, s40 offen offset:1024 lds\n\t" \
  "buffer_load_dwordx4 v187, %0, s40 offen offset:2048 lds\n\t" \
  "buffer_load_dwordx4 v188, %0, s40 offen offset:3072 lds\n\t" \
  "s_add_u32 s40, %4, 294912\n\t" \
  "s_add_u32 m0, %14, 4096\n\t" \
  "s_nop 0\n\t" \
  "buffer_load_dwordx4 v185, %0, s40 offen lds\n\t" \
  "buffer_load_dwordx4 v186, %0, s40 offen offset:1024 lds\n\t" \
  "buffer_load_dwordx4 v187, %0, s40 offen offset:2048 lds\n\t" \
  "buffer_load_dwordx4 v188, %0, s40 offen offset:3072 lds\n\t" \
  "s_add_u32 s40, %4, 589824\n\t" \
  "s_add_u32 m0, %14, 8192\n\t" \
  "s_nop 0\n\t" \
  "buffer_load_dwordx4 v185, %0, s40 offen lds\n\t" \
  "buffer_load_dwordx4 v186, %0, s40 offen offset:1024 lds\n\t" \
  "buffer_load_dwordx4 v187, %0, s40 offen offset:2048 lds\n\t" \
  "buffer_load_dwordx4 v188, %0, s40 offen offset:3072 lds\n\t" \
  "s_add_u32 s40, %4, 1769472\n\t" \
  "s_add_u32 m0, %14, 49152\n\t" \
  "s_nop 0\n\t" \
  "buffer_load_dwordx4 v185, %0, s40 offen lds\n\t" \
  "buffer_load_dwordx4 v186, %0, s40 offen offset:1024 lds\n\t" \
  "buffer_load_dwordx4 v187, %0, s40 offen offset:2048 lds\n\t" \
  "buffer_load_dwordx4 v188, %0, s40 offen offset:3072 lds\n\t" \
  "s_add_u32 s40, %4, 2064384\n\t" \
  "s_add_u32 m0, %14, 53248\n\t" \
  "s_nop 0\n\t" \
  "buffer_load_dwordx4 v185, %0, s40 offen lds\n\t" \
  "buffer_load_dwordx4 v186, %0, s40 offen offset:1024 lds\n\t" \
  "buffer_load_dwordx4 v187, %0, s40 offen offset:2048 lds\n\t" \
  "buffer_load_dwordx4 v188, %0, s40 offen offset:3072 lds\n\t" \
  "s_add_u32 s40, %4, 2359296\n\t" \
  "s_add_u32 m0, %14, 57344\n\t" \
  "s_nop 0\n\t" \
  "buffer_load_dwordx4 v185, %0, s40 offen lds\n\t" \
  "buffer_load_dwordx4 v186, %0, s40 offen offset:1024 lds\n\t" \
  "buffer_load_dwordx4 v187, %0, s40 offen offset:2048 lds\n\t" \
  "buffer_load_dwordx4 v188, %0, s40 offen offset:3072 lds\n\t" \
  "v_and_b32 v220, 15, %24\n\t" \
  "v_lshlrev_b32 v220, 3, v220\n\t" \
  "s_memrealtime s[44:45]\n\ts_waitcnt lgkmcnt(0)\nKP0_%=:\n\tglobal_load_dwordx2 v[200:201], v220, %27 sc1\n\ts_waitcnt vmcnt(0)\n\tv_cmp_ne_u64 vcc, %28, v[200:201]\n\ts_nop 4\n\ts_cmp_eq_u64 vcc, 0\n\ts_cbranch_scc1 KP1_%=\n\ts_memrealtime s[46:47]\n\ts_waitcnt lgkmcnt(0)\n\ts_sub_u32 s46, s46, s44\n\ts_cmp_gt_u32 s46, 4000000\n\ts_cbranch_scc1 KP1_%=\n\ts_sleep 1\n\ts_branch KP0_%=\nKP1_%=:\n\t" \
  "v_and_b32 v220, 31, %24\n\t" \
  "v_lshlrev_b32 v220, 6, v220\n\t" \
  "global_load_dwordx4 v[200:203], v220, %18 sc1\n\t" \
  "global_load_dwordx4 v[204:207], v220, %18 offset:16 sc1\n\t" \
  "global_load_dwordx4 v[208:211], v220, %18 offset:32 sc1\n\t" \
  "global_load_dwordx4 v[212:215], v220, %18 offset:48 sc1\n\t" \
  "s_mov_b32 s44, 0\n\t" \
  "buffer_load_dwordx4 a[0:3], v195, %2, s44 offen sc1\n\t" \
  "buffer_load_dwordx4 a[4:7], v195, %2, s44 offen offset:1024 sc1\n\t" \
  "buffer_load_dwordx4 a[8:11], v195, %2, s44 offen offset:2048 sc1\n\t" \
  "buffer_load_dwordx4 a[12:15], v195, %2, s44 offen offset:3072 sc1\n\t" \
  "s_mov_b32 s44, 4096\n\t" \
  "buffer_load_dwordx4 a[16:19], v195, %2, s44 offen sc1\n\t" \
  "buffer_load_dwordx4 a[20:23], v195, %2, s44 offen offset:1024 sc1\n\t" \
  "buffer_load_dwordx4 a[24:27], v195, %2, s44 offen offset:2048 sc1\n\t" \
  "buffer_load_dwordx4 a[28:31], v195, %2, s44 offen offset:3072 sc1\n\t" \
  "s_mov_b32 s44, 8192\n\t" \
  "buffer_load_dwordx4 a[32:35], v195, %2, s44 offen sc1\n\t" \
  "buffer_load_dwordx4 a[36:39], v195, %2, s44 offen offset:1024 sc1\n\t" \
  "buffer_load_dwordx4 a[40:43], v195, %2, s44 offen offset:2048 sc1\n\t" \
  "buffer_load_dwordx4 a[44:47], v195, %2, s44 offen offset:3072 sc1\n\t" \
  "s_waitcnt vmcnt(0) lgkmcnt(0)\n\ts_barrier\n\t" \
  "ds_read_b64_tr_b16 v[128:129], v176 offset:0\n\t" \
  "ds_read_b64_tr_b16 v[130:131], v176 offset:1024\n\t" \
  "ds_read_b64_tr_b16 v[132:133], v177 offset:0\n\t" \
  "ds_read_b64_tr_b16 v[134:135], v177 offset:1024\n\t" \
  "ds_read_b64_tr_b16 v[136:137], v176 offset:24576\n\t" \
  "ds_read_b64_tr_b16 v[138:139], v176 offset:25600\n\t" \
  "ds_read_b64_tr_b16 v[140:141], v177 offset:24576\n\t" \
  "ds_read_b64_tr_b16 v[142:143], v177 offset:25600\n\t" \
  "ds_read_b64_tr_b16 v[144:145], v176 offset:2048\n\t" \
  "ds_read_b64_tr_b16 v[146:147], v176 offset:3072\n\t" \
  "ds_read_b64_tr_b16 v[148:149], v177 offset:2048\n\t" \
  "ds_read_b64_tr_b16 v[150:151], v177 offset:3072\n\t" \
  "ds_read_b64_tr_b16 v[152:153], v176 offset:26624\n\t" \
  "ds_read_b64_tr_b16 v[154:155], v176 offset:27648\n\t" \
  "ds_read_b64_tr_b16 v[156:157], v177 offset:26624\n\t" \
  "ds_read_b64_tr_b16 v[158:159], v177 offset:27648\n\t" \
  "s_waitcnt lgkmcnt(8)\n\t" \
  "v_mfma_f32_32x32x16_bf16 v[0:15], v[128:131], a[0:3], 0\n\t" \
  "ds_read_b64_tr_b16 v[160:161], v176 offset:4096\n\t" \
  "ds_read_b64_tr_b16 v[162:163], v176 offset:5120\n\t" \
  "s_mov_b32 s44, 12288\n\t" \
  "buffer_load_dwordx4 a[48:51], v195, %2, s44 offen sc1\n\t" \
  "v_mfma_f32_32x32x16_bf16 v[16:31], v[132:135], a[0:3], 0\n\t" \
  "ds_read_b64_tr_b16 v[164:165], v177 offset:4096\n\t" \
  "ds_read_b64_tr_b16 v[166:167], v177 offset:5120\n\t" \
  "buffer_load_dwordx4 a[52:55], v195, %2, s44 offen offset:1024 sc1\n\t" \
  "v_mfma_f32_32x32x16_bf16 v[32:47], v[136:139], a[0:3], 0\n\t" \
  "ds_read_b64_tr_b16 v[168:169], v176 offset:28672\n\t" \
  "ds_read_b64_tr_b16 v[170:171], v176 offset:29696\n\t" \
  "v_mfma_f32_32x32x16_bf16 v[48:63], v[140:143], a[0:3], 0\n\t" \
  "ds_read_b64_tr_b16 v[172:173], v177 offset:28672\n\t" \
  "ds_read_b64_tr_b16 v[174:175], v177 offset:29696\n\t" \
  "buffer_load_dwordx4 a[56:59], v195, %2, s44 offen offset:2048 sc1\n\t" \
  "s_waitcnt lgkmcnt(8)\n\t" \
  "v_mfma_f32_32x32x16_bf16 v[0:15], v[144:147], a[4:7], v[0:15]\n\t" \
  "ds_read_b64_tr_b16 v[128:129], v176 offset:6144\n\t" \
  "ds_read_b64_tr_b16 v[130:131], v176 offset:7168\n\t" \
  "v_add_f32 v200, v200, v201\n\t" \
  "v_add_f32 v200, v200, v202\n\t" \
  "v_mfma_f32_32x32x16_bf16 v[16:31], v[148:151], a[4:7], v[16:31]\n\t" \
  "ds_read_b64_tr_b16 v[132:133], v177 offset:6144\n\t" \
  "ds_read_b64_tr_b16 v[134:135], v177 offset:7168\n\t" \
  "buffer_load_dwordx4 a[60:63], v195, %2, s44 offen offset:3072 sc1\n\t" \
  "s_mov_b32 s44, 16384\n\t" \
  "v_add_f32 v200, v200, v203\n\t" \
  "v_mfma_f32_32x32x16_bf16 v[32:47], v[152:155], a[4:7], v[32:47]\n\t" \
  "ds_read_b64_tr_b16 v[136:137], v176 offset:30720\n\t" \
  "ds_read_b64_tr_b16 v[138:139], v176 offset:31744\n\t" \
  "buffer_load_dwordx4 a[64:67], v195, %2, s44 offen sc1\n\t" \
  "v_add_f32 v200, v200, v204\n\t" \
  "v_mfma_f32_32x32x16_bf16 v[48:63], v[156:159], a[4:7], v[48:63]\n\t" \
  "ds_read_b64_tr_b16 v[140:141], v177 offset:30720\n\t" \
  "ds_read_b64_tr_b16 v[142:143], v177 offset:31744\n\t" \
  "v_add_f32 v200, v200, v205\n\t" \
  "s_waitcnt lgkmcnt(8)\n\t" \
  "v_mfma_f32_32x32x16_bf16 v[0:15], v[160:163], a[8:11], v[0:15]\n\t" \
  "ds_read_b64_tr_b16 v[144:145], v176 offset:8192\n\t" \
  "ds_read_b64_tr_b16 v[146:147], v176 offset:9216\n\t" \
  "buffer_load_dwordx4 a[68:71], v195, %2, s44 offen offset:1024 sc1\n\t" \
  "v_add_f32 v200, v200, v206\n\t" \
  "v_mfma_f32_32x32x16_bf16 v[16:31], v[164:167], a[8:11], v[16:31]\n\t" \
  "ds_read_b64_tr_b16 v[148:149], v177 offset:8192\n\t" \
  "ds_read_b64_tr_b16 v[150:151], v177 offset:9216\n\t" \
  "v_add_f32 v200, v200, v207\n\t" \
  "v_mfma_f32_32x32x16_bf16 v[32:47], v[168:171], a[8:11], v[32:47]\n\t" \
  "ds_read_b64_tr_b16 v[152:153], v176 offset:32768\n\t" \
  "ds_read_b64_tr_b16 v[154:155], v176 offset:33792\n\t" \
  "buffer_load_dwordx4 a[72:75], v195, %2, s44 offen offset:2048 sc1\n\t" \
  "v_add_f32 v200, v200, v208\n\t" \
  "v_mfma_f32_32x32x16_bf16 v[48:63], v[172:175], a[8:11], v[48:63]\n\t" \
  "ds_read_b64_tr_b16 v[156:157], v177 offset:32768\n\t" \
  "ds_read_b64_tr_b16 v[158:159], v177 offset:33792\n\t" \
  "buffer_load_dwordx4 a[76:79], v195, %2, s44 offen offset:3072 sc1\n\t" \
  "v_add_f32 v200, v200, v209\n\t" \
  "s_waitcnt lgkmcnt(8)\n\t" \
  "v_mfma_f32_32x32x16_bf16 v[0:15], v[128:131], a[12:15], v[0:15]\n\t" \
  "ds_read_b64_tr_b16 v[160:161], v176 offset:10240\n\t" \
  "ds_read_b64_tr_b16 v[162:163], v176 offset:11264\n\t" \
  "s_mov_b32 s44, 20480\n\t" \
  "v_add_f32 v200, v200, v210\n\t" \
  "v_mfma_f32_32x32x16_bf16 v[16:31], v[132:135], a[12:15], v[16:31]\n\t" \
  "ds_read_b64_tr_b16 v[164:165], v177 offset:10240\n\t" \
  "ds_read_b64_tr_b16 v[166:167], v177 offset:11264\n\t" \
  "buffer_load_dwordx4 a[80:83], v195, %2, s44 offen sc1\n\t" \
  "v_add_f32 v200, v200, v211\n\t" \
  "v_mfma_f32_32x32x16_bf16 v[32:47], v[136:139], a[12:15], v[32:47]\n\t" \
  "ds_read_b64_tr_b16 v[168:169], v176 offset:34816\n\t" \
  "ds_read_b64_tr_b16 v[170:171], v176 offset:35840\n\t" \
  "v_add_f32 v200, v200, v212\n\t" \
  "v_add_f32 v200, v200, v213\n\t" \
  "v_mfma_f32_32x32x16_bf16 v[48:63], v[140:143], a[12:15], v[48:63]\n\t" \
  "ds_read_b64_tr_b16 v[172:173], v177 offset:34816\n\t" \
  "ds_read_b64_tr_b16 v[174:175], v177 offset:35840\n\t" \
  "buffer_load_dwordx4 a[84:87], v195, %2, s44 offen offset:1024 sc1\n\t" \
  "v_add_f32 v200, v200, v214\n\t" \
  "s_waitcnt lgkmcnt(8)\n\t" \
  "v_mfma_f32_32x32x16_bf16 v[0:15], v[144:147], a[16:19], v[0:15]\n\t" \
  "ds_read_b64_tr_b16 v[128:129], v176 offset:12288\n\t" \
  "ds_read_b64_tr_b16 v[130:131], v176 offset:13312\n\t" \
  "buffer_load_dwordx4 a[88:91], v195, %2, s44 offen offset:2048 sc1\n\t" \
  "v_add_f32 v200, v200, v215\n\t" \
  "v_mfma_f32_32x32x16_bf16 v[16:31], v[148:151], a[16:19], v[16:31]\n\t" \
  "ds_read_b64_tr_b16 v[132:133], v177 offset:12288\n\t" \
  "ds_read_b64_tr_b16 v[134:135], v177 offset:13312\n\t" \
  "s_mov_b32 s46, 0x3ae38e39\n\t" \
  "v_mfma_f32_32x32x16_bf16 v[32:47], v[152:155], a[16:19], v[32:47]\n\t" \
  "ds_read_b64_tr_b16 v[136:137], v176 offset:36864\n\t" \
  "ds_read_b64_tr_b16 v[138:139], v176 offset:37888\n\t" \
  "buffer_load_dwordx4 a[92:95], v195, %2, s44 offen offset:3072 sc1\n\t" \
  "v_mov_b32 v197, 0x3727c5ac\n\t" \
  "v_mfma_f32_32x32x16_bf16 v[48:63], v[156:159], a[16:19], v[48:63]\n\t" \
  "ds_read_b64_tr_b16 v[140:141], v177 offset:36864\n\t" \
  "ds_read_b64_tr_b16 v[142:143], v177 offset:37888\n\t" \
  "v_fma_f32 v197, v200, s46, v197\n\t" \
  "s_waitcnt lgkmcnt(8)\n\t" \
  "v_mfma_f32_32x32x16_bf16 v[0:15], v[160:163], a[20:23], v[0:15]\n\t" \
  "ds_read_b64_tr_b16 v[144:145], v176 offset:14336\n\t" \
  "ds_read_b64_tr_b16 v[146:147], v176 offset:15360\n\t" \
  "s_mov_b32 s44, 24576\n\t" \
  "buffer_load_dwordx4 a[96:99], v195, %2, s44 offen sc1\n\t" \
  "v_rsq_f32 v196, v197\n\t" \
  "v_mfma_f32_32x32x16_bf16 v[16:31], v[164:167], a[20:23], v[16:31]\n\t" \
  "ds_read_b64_tr_b16 v[148:149], v177 offset:14336\n\t" \
  "ds_read_b64_tr_b16 v[150:151], v177 offset:15360\n\t" \
  "buffer_load_dwordx4 a[100:103], v195, %2, s44 offen offset:1024 sc1\n\t" \
  "v_mov_b32 v201, 0\n\t" \
  "v_mfma_f32_32x32x16_bf16 v[32:47], v[168:171], a[20:23], v[32:47]\n\t" \
  "ds_read_b64_tr_b16 v[152:153], v176 offset:38912\n\t" \
  "ds_read_b64_tr_b16 v[154:155], v176 offset:39936\n\t" \
  "buffer_load_dwordx4 a[104:107], v195, %2, s44 offen offset:2048 sc1\n\t" \
  "v_mul_f32 v196, 0xbfb8aa3b, v196\n\t" \
  "v_mfma_f32_32x32x16_bf16 v[48:63], v[172:175], a[20:23], v[48:63]\n\t" \
  "ds_read_b64_tr_b16 v[156:157], v177 offset:38912\n\t" \
  "ds_read_b64_tr_b16 v[158:159], v177 offset:39936\n\t" \
  "buffer_load_dwordx4 a[108:111], v195, %2, s44 offen offset:3072 sc1\n\t" \
  "s_add_u32 s40, %4, 3538944\n\t" \
  "s_add_u32 m0, %14, 98304\n\t" \
  "s_waitcnt lgkmcnt(8)\n\t" \
  "v_mfma_f32_32x32x16_bf16 v[0:15], v[128:131], a[24:27], v[0:15]\n\t" \
  "ds_read_b64_tr_b16 v[160:161], v176 offset:16384\n\t" \
  "ds_read_b64_tr_b16 v[162:163], v176 offset:17408\n\t" \
  "buffer_load_dwordx4 v185, %0, s40 offen lds\n\t" \
  "v_mfma_f32_32x32x16_bf16 v[16:31], v[132:135], a[24:27], v[16:31]\n\t" \
  "ds_read_b64_tr_b16 v[164:165], v177 offset:16384\n\t" \
  "ds_read_b64_tr_b16 v[166:167], v177 offset:17408\n\t" \
  "buffer_load_dwordx4 v186, %0, s40 offen offset:1024 lds\n\t" \
  "v_mfma_f32_32x32x16_bf16 v[32:47], v[136:139], a[24:27], v[32:47]\n\t" \
  "ds_read_b64_tr_b16 v[168:169], v176 offset:40960\n\t" \
  "ds_read_b64_tr_b16 v[170:171], v176 offset:41984\n\t" \
  "v_mfma_f32_32x32x16_bf16 v[48:63], v[140:143], a[24:27], v[48:63]\n\t" \
  "ds_read_b64_tr_b16 v[172:173], v177 offset:40960\n\t" \
  "ds_read_b64_tr_b16 v[174:175], v177 offset:41984\n\t" \
  "buffer_load_dwordx4 v187, %0, s40 offen offset:2048 lds\n\t" \
  "s_waitcnt lgkmcnt(8)\n\t" \
  "v_mfma_f32_32x32x16_bf16 v[0:15], v[144:147], a[28:31], v[0:15]\n\t" \
  "ds_read_b64_tr_b16 v[128:129], v176 offset:18432\n\t" \
  "ds_read_b64_tr_b16 v[130:131], v176 offset:19456\n\t" \
  "buffer_load_dwordx4 v188, %0, s40 offen offset:3072 lds\n\t" \
  "s_mov_b32 s44, 28672\n\t" \
  "v_mfma_f32_32x32x16_bf16 v[16:31], v[148:151], a[28:31], v[16:31]\n\t" \
  "ds_read_b64_tr_b16 v[132:133], v177 offset:18432\n\t" \
  "ds_read_b64_tr_b16 v[134:135], v177 offset:19456\n\t" \
  "buffer_load_dwordx4 a[112:115], v195, %2, s44 offen sc1\n\t" \
  "v_mfma_f32_32x32x16_bf16 v[32:47], v[152:155], a[28:31], v[32:47]\n\t" \
  "ds_read_b64_tr_b16 v[136:137], v176 offset:43008\n\t" \
  "ds_read_b64_tr_b16 v[138:139], v176 offset:44032\n\t" \
  "buffer_load_dwordx4 a[116:119], v195, %2, s44 offen offset:1024 sc1\n\t" \
  "v_mfma_f32_32x32x16_bf16 v[48:63], v[156:159], a[28:31], v[48:63]\n\t" \
  "ds_read_b64_tr_b16 v[140:141], v177 offset:43008\n\t" \
  "ds_read_b64_tr_b16 v[142:143], v177 offset:44032\n\t" \
  "buffer_load_dwordx4 a[120:123], v195, %2, s44 offen offset:2048 sc1\n\t" \
  "s_waitcnt lgkmcnt(8)\n\t" \
  "v_mfma_f32_32x32x16_bf16 v[0:15], v[160:163], a[32:35], v[0:15]\n\t" \
  "ds_read_b64_tr_b16 v[144:145], v176 offset:20480\n\t" \
  "ds_read_b64_tr_b16 v[146:147], v176 offset:21504\n\t" \
  "buffer_load_dwordx4 a[124:127], v195, %2, s44 offen offset:3072 sc1\n\t" \
  "v_mfma_f32_32x32x16_bf16 v[16:31], v[164:167], a[32:35], v[16:31]\n\t" \
  "ds_read_b64_tr_b16 v[148:149], v177 offset:20480\n\t" \
  "ds_read_b64_tr_b16 v[150:151], v177 offset:21504\n\t" \
  "s_add_u32 s40, %4, 3833856\n\t" \
  "s_add_u32 m0, %14, 102400\n\t" \
  "v_mfma_f32_32x32x16_bf16 v[32:47], v[168:171], a[32:35], v[32:47]\n\t" \
  "ds_read_b64_tr_b16 v[152:153], v176 offset:45056\n\t" \
  "ds_read_b64_tr_b16 v[154:155], v176 offset:46080\n\t" \
  "buffer_load_dwordx4 v185, %0, s40 offen lds\n\t" \
  "v_mfma_f32_32x32x16_bf16 v[48:63], v[172:175], a[32:35], v[48:63]\n\t" \
  "ds_read_b64_tr_b16 v[156:157], v177 offset:45056\n\t" \
  "ds_read_b64_tr_b16 v[158:159], v177 offset:46080\n\t" \
  "buffer_load_dwordx4 v186, %0, s40 offen offset:1024 lds\n\t" \
  "s_waitcnt lgkmcnt(8)\n\t" \
  "v_mfma_f32_32x32x16_bf16 v[0:15], v[128:131], a[36:39], v[0:15]\n\t" \
  "ds_read_b64_tr_b16 v[160:161], v176 offset:22528\n\t" \
  "ds_read_b64_tr_b16 v[162:163], v176 offset:23552\n\t" \
  "ds_read_b64_tr_b16 v[164:165], v177 offset:22528\n\t" \
  "ds_read_b64_tr_b16 v[166:167], v177 offset:23552\n\t" \
  "buffer_load_dwordx4 v187, %0, s40 offen offset:2048 lds\n\t" \
  "v_mfma_f32_32x32x16_bf16 v[16:31], v[132:135], a[36:39], v[16:31]\n\t" \
  "ds_read_b64_tr_b16 v[168:169], v176 offset:47104\n\t" \
  "ds_read_b64_tr_b16 v[170:171], v176 offset:48128\n\t" \
  "ds_read_b64_tr_b16 v[172:173], v177 offset:47104\n\t" \
  "ds_read_b64_tr_b16 v[174:175], v177 offset:48128\n\t" \
  "buffer_load_dwordx4 v188, %0, s40 offen offset:3072 lds\n\t" \
  "s_mov_b32 s44, 32768\n\t" \
  "v_mfma_f32_32x32x16_bf16 v[32:47], v[136:139], a[36:39], v[32:47]\n\t" \
  "buffer_load_dwordx4 a[128:131], v195, %2, s44 offen sc1\n\t" \
  "v_mfma_f32_32x32x16_bf16 v[48:63], v[140:143], a[36:39], v[48:63]\n\t" \
  "buffer_load_dwordx4 a[132:135], v195, %2, s44 offen offset:1024 sc1\n\t" \
  "s_waitcnt lgkmcnt(8)\n\t" \
  "v_mfma_f32_32x32x16_bf16 v[0:15], v[144:147], a[40:43], v[0:15]\n\t" \
  "s_waitcnt vmcnt(18) lgkmcnt(0)\n\ts_barrier\n\t" \
  "ds_read_b64_tr_b16 v[128:129], v178 offset:0\n\t" \
  "ds_read_b64_tr_b16 v[130:131], v178 offset:1024\n\t" \
  "v_mfma_f32_32x32x16_bf16 v[16:31], v[148:151], a[40:43], v[16:31]\n\t" \
  "ds_read_b64_tr_b16 v[132:133], v179 offset:0\n\t" \
  "ds_read_b64_tr_b16 v[134:135], v179 offset:1024\n\t" \
  "buffer_load_dwordx4 a[136:139], v195, %2, s44 offen offset:2048 sc1\n\t" \
  "v_mfma_f32_32x32x16_bf16 v[32:47], v[152:155], a[40:43], v[32:47]\n\t" \
  "ds_read_b64_tr_b16 v[136:137], v178 offset:24576\n\t" \
  "ds_read_b64_tr_b16 v[138:139], v178 offset:25600\n\t" \
  "buffer_load_dwordx4 a[140:143], v195, %2, s44 offen offset:3072 sc1\n\t" \
  "s_add_u32 s40, %4, 4128768\n\t" \
  "s_add_u32 m0, %14, 106496\n\t" \
  "v_mfma_f32_32x32x16_bf16 v[48:63], v[156:159], a[40:43], v[48:63]\n\t" \
  "ds_read_b64_tr_b16 v[140:141], v179 offset:24576\n\t" \
  "ds_read_b64_tr_b16 v[142:143], v179 offset:25600\n\t" \
  "buffer_load_dwordx4 v185, %0, s40 offen lds\n\t" \
  "s_waitcnt lgkmcnt(8)\n\t" \
  "v_mfma_f32_32x32x16_bf16 v[0:15], v[160:163], a[44:47], v[0:15]\n\t" \
  "ds_read_b64_tr_b16 v[144:145], v178 offset:2048\n\t" \
  "ds_read_b64_tr_b16 v[146:147], v178 offset:3072\n\t" \
  "buffer_load_dwordx4 v186, %0, s40 offen offset:1024 lds\n\t" \
  "v_mfma_f32_32x32x16_bf16 v[16:31], v[164:167], a[44:47], v[16:31]\n\t" \
  "ds_read_b64_tr_b16 v[148:149], v179 offset:2048\n\t" \
  "ds_read_b64_tr_b16 v[150:151], v179 offset:3072\n\t" \
  "buffer_load_dwordx4 v187, %0, s40 offen offset:2048 lds\n\t" \
  "v_mfma_f32_32x32x16_bf16 v[32:47], v[168:171], a[44:47], v[32:47]\n\t" \
  "ds_read_b64_tr_b16 v[152:153], v178 offset:26624\n\t" \
  "ds_read_b64_tr_b16 v[154:155], v178 offset:27648\n\t" \
  "buffer_load_dwordx4 v188, %0, s40 offen offset:3072 lds\n\t" \
  "v_mfma_f32_32x32x16_bf16 v[48:63], v[172:175], a[44:47], v[48:63]\n\t" \
  "ds_read_b64_tr_b16 v[156:157], v179 offset:26624\n\t" \
  "ds_read_b64_tr_b16 v[158:159], v179 offset:27648\n\t" \
  "s_waitcnt lgkmcnt(8)\n\t" \
  "v_mfma_f32_32x32x16_bf16 v[0:15], v[128:131], a[48:51], v[0:15]\n\t" \
  "ds_read_b64_tr_b16 v[160:161], v178 offset:4096\n\t" \
  "ds_read_b64_tr_b16 v[162:163], v178 offset:5120\n\t" \
  "s_add_u32 s40, %5, 0\n\t" \
  "s_add_u32 m0, %14, 0\n\t" \
  "v_mfma_f32_32x32x16_bf16 v[16:31], v[132:135], a[48:51], v[16:31]\n\t" \
  "ds_read_b64_tr_b16 v[164:165], v179 offset:4096\n\t" \
  "ds_read_b64_tr_b16 v[166:167], v179 offset:5120\n\t" \
  "buffer_load_dwordx4 v185, %0, s40 offen lds\n\t" \
  "v_mfma_f32_32x32x16_bf16 v[32:47], v[136:139], a[48:51], v[32:47]\n\t" \
  "ds_read_b64_tr_b16 v[168:169], v178 offset:28672\n\t" \
  "ds_read_b64_tr_b16 v[170:171], v178 offset:29696\n\t" \
  "buffer_load_dwordx4 v186, %0, s40 offen offset:1024 lds\n\t" \
  "v_mfma_f32_32x32x16_bf16 v[48:63], v[140:143], a[48:51], v[48:63]\n\t" \
  "ds_read_b64_tr_b16 v[172:173], v179 offset:28672\n\t" \
  "ds_read_b64_tr_b16 v[174:175], v179 offset:29696\n\t" \
  "s_waitcnt lgkmcnt(8)\n\t" \
  "v_mfma_f32_32x32x16_bf16 v[0:15], v[144:147], a[52:55], v[0:15]\n\t" \
  "ds_read_b64_tr_b16 v[128:129], v178 offset:6144\n\t" \
  "ds_read_b64_tr_b16 v[130:131], v178 offset:7168\n\t" \
  "v_mfma_f32_32x32x16_bf16 v[16:31], v[148:151], a[52:55], v[16:31]\n\t" \
  "ds_read_b64_tr_b16 v[132:133], v179 offset:6144\n\t" \
  "ds_read_b64_tr_b16 v[134:135], v179 offset:7168\n\t" \
  "buffer_load_dwordx4 v187, %0, s40 offen offset:2048 lds\n\t" \
  "v_mfma_f32_32x32x16_bf16 v[32:47], v[152:155], a[52:55], v[32:47]\n\t" \
  "ds_read_b64_tr_b16 v[136:137], v178 offset:30720\n\t" \
  "ds_read_b64_tr_b16 v[138:139], v178 offset:31744\n\t" \
  "v_mfma_f32_32x32x16_bf16 v[48:63], v[156:159], a[52:55], v[48:63]\n\t" \
  "ds_read_b64_tr_b16 v[140:141], v179 offset:30720\n\t" \
  "ds_read_b64_tr_b16 v[142:143], v179 offset:31744\n\t" \
  "s_waitcnt lgkmcnt(8)\n\t" \
  "v_mfma_f32_32x32x16_bf16 v[0:15], v[160:163], a[56:59], v[0:15]\n\t" \
  "ds_read_b64_tr_b16 v[144:145], v178 offset:8192\n\t" \
  "ds_read_b64_tr_b16 v[146:147], v178 offset:9216\n\t" \
  "v_mfma_f32_32x32x16_bf16 v[16:31], v[164:167], a[56:59], v[16:31]\n\t" \
  "ds_read_b64_tr_b16 v[148:149], v179 offset:8192\n\t" \
  "ds_read_b64_tr_b16 v[150:151], v179 offset:9216\n\t" \
  "buffer_load_dwordx4 v188, %0, s40 offen offset:3072 lds\n\t" \
  "s_add_u32 s40, %5, 294912\n\t" \
  "s_add_u32 m0, %14, 4096\n\t" \
  "v_mfma_f32_32x32x16_bf16 v[32:47], v[168:171], a[56:59], v[32:47]\n\t" \
  "ds_read_b64_tr_b16 v[152:153], v178 offset:32768\n\t" \
  "ds_read_b64_tr_b16 v[154:155], v178 offset:33792\n\t" \
  "buffer_load_dwordx4 v185, %0, s40 offen lds\n\t" \
  "v_mfma_f32_32x32x16_bf16 v[48:63], v[172:175], a[56:59], v[48:63]\n\t" \
  "ds_read_b64_tr_b16 v[156:157], v179 offset:32768\n\t" \
  "ds_read_b64_tr_b16 v[158:159], v179 offset:33792\n\t" \
  "s_waitcnt lgkmcnt(8)\n\t" \
  "v_mfma_f32_32x32x16_bf16 v[0:15], v[128:131], a[60:63], v[0:15]\n\t" \
  "ds_read_b64_tr_b16 v[160:161], v178 offset:10240\n\t" \
  "ds_read_b64_tr_b16 v[162:163], v178 offset:11264\n\t" \
  "v_mfma_f32_32x32x16_bf16 v[16:31], v[132:135], a[60:63], v[16:31]\n\t" \
  "ds_read_b64_tr_b16 v[164:165], v179 offset:10240\n\t" \
  "ds_read_b64_tr_b16 v[166:167], v179 offset:11264\n\t" \
  "buffer_load_dwordx4 v186, %0, s40 offen offset:1024 lds\n\t" \
  "v_mfma_f32_32x32x16_bf16 v[32:47], v[136:139], a[60:63], v[32:47]\n\t" \
  "ds_read_b64_tr_b16 v[168:169], v178 offset:34816\n\t" \
  "ds_read_b64_tr_b16 v[170:171], v178 offset:35840\n\t" \
  "v_mfma_f32_32x32x16_bf16 v[48:63], v[140:143], a[60:63], v[48:63]\n\t" \
  "ds_read_b64_tr_b16 v[172:173], v179 offset:34816\n\t" \
  "ds_read_b64_tr_b16 v[174:175], v179 offset:35840\n\t" \
  "s_waitcnt lgkmcnt(8)\n\t" \
  "v_mfma_f32_32x32x16_bf16 v[0:15], v[144:147], a[64:67], v[0:15]\n\t" \
  "ds_read_b64_tr_b16 v[128:129], v178 offset:12288\n\t" \
  "ds_read_b64_tr_b16 v[130:131], v178 offset:13312\n\t" \
  "v_mfma_f32_32x32x16_bf16 v[16:31], v[148:151], a[64:67], v[16:31]\n\t" \
  "ds_read_b64_tr_b16 v[132:133], v179 offset:12288\n\t" \
  "ds_read_b64_tr_b16 v[134:135], v179 offset:13312\n\t" \
  "buffer_load_dwordx4 v187, %0, s40 offen offset:2048 lds\n\t" \
  "v_mfma_f32_32x32x16_bf16 v[32:47], v[152:155], a[64:67], v[32:47]\n\t" \
  "ds_read_b64_tr_b16 v[136:137], v178 offset:36864\n\t" \
  "ds_read_b64_tr_b16 v[138:139], v178 offset:37888\n\t" \
  "buffer_load_dwordx4 v188, %0, s40 offen offset:3072 lds\n\t" \
  "v_mfma_f32_32x32x16_bf16 v[48:63], v[156:159], a[64:67], v[48:63]\n\t" \
  "ds_read_b64_tr_b16 v[140:141], v179 offset:36864\n\t" \
  "ds_read_b64_tr_b16 v[142:143], v179 offset:37888\n\t" \
  "s_waitcnt lgkmcnt(8)\n\t" \
  "v_mfma_f32_32x32x16_bf16 v[0:15], v[160:163], a[68:71], v[0:15]\n\t" \
  "ds_read_b64_tr_b16 v[144:145], v178 offset:14336\n\t" \
  "ds_read_b64_tr_b16 v[146:147], v178 offset:15360\n\t" \
  "s_add_u32 s40, %5, 589824\n\t" \
  "s_add_u32 m0, %14, 8192\n\t" \
  "v_mfma_f32_32x32x16_bf16 v[16:31], v[164:167], a[68:71], v[16:31]\n\t" \
  "ds_read_b64_tr_b16 v[148:149], v179 offset:14336\n\t" \
  "ds_read_b64_tr_b16 v[150:151], v179 offset:15360\n\t" \
  "buffer_load_dwordx4 v185, %0, s40 offen lds\n\t" \
  "v_mfma_f32_32x32x16_bf16 v[32:47], v[168:171], a[68:71], v[32:47]\n\t" \
  "ds_read_b64_tr_b16 v[152:153], v178 offset:38912\n\t" \
  "ds_read_b64_tr_b16 v[154:155], v178 offset:39936\n\t" \
  "v_mfma_f32_32x32x16_bf16 v[48:63], v[172:175], a[68:71], v[48:63]\n\t" \
  "ds_read_b64_tr_b16 v[156:157], v179 offset:38912\n\t" \
  "ds_read_b64_tr_b16 v[158:159], v179 offset:39936\n\t" \
  "s_waitcnt lgkmcnt(8)\n\t" \
  "v_mfma_f32_32x32x16_bf16 v[0:15], v[128:131], a[72:75], v[0:15]\n\t" \
  "ds_read_b64_tr_b16 v[160:161], v178 offset:16384\n\t" \
  "ds_read_b64_tr_b16 v[162:163], v178 offset:17408\n\t" \
  "v_mfma_f32_32x32x16_bf16 v[16:31], v[132:135], a[72:75], v[16:31]\n\t" \
  "ds_read_b64_tr_b16 v[164:165], v179 offset:16384\n\t" \
  "ds_read_b64_tr_b16 v[166:167], v179 offset:17408\n\t" \
  "buffer_load_dwordx4 v186, %0, s40 offen offset:1024 lds\n\t" \
  "v_mfma_f32_32x32x16_bf16 v[32:47], v[136:139], a[72:75], v[32:47]\n\t" \
  "ds_read_b64_tr_b16 v[168:169], v178 offset:40960\n\t" \
  "ds_read_b64_tr_b16 v[170:171], v178 offset:41984\n\t" \
  "buffer_load_dwordx4 v187, %0, s40 offen offset:2048 lds\n\t" \
  "v_mfma_f32_32x32x16_bf16 v[48:63], v[140:143], a[72:75], v[48:63]\n\t" \
  "ds_read_b64_tr_b16 v[172:173], v179 offset:40960\n\t" \
  "ds_read_b64_tr_b16 v[174:175], v179 offset:41984\n\t" \
  "s_waitcnt lgkmcnt(8)\n\t" \
  "v_mfma_f32_32x32x16_bf16 v[0:15], v[144:147], a[76:79], v[0:15]\n\t" \
  "ds_read_b64_tr_b16 v[128:129], v178 offset:18432\n\t" \
  "ds_read_b64_tr_b16 v[130:131], v178 offset:19456\n\t" \
  "v_mfma_f32_32x32x16_bf16 v[16:31], v[148:151], a[76:79], v[16:31]\n\t" \
  "ds_read_b64_tr_b16 v[132:133], v179 offset:18432\n\t" \
  "ds_read_b64_tr_b16 v[134:135], v179 offset:19456\n\t" \
  "buffer_load_dwordx4 v188, %0, s40 offen offset:3072 lds\n\t" \
  "v_mfma_f32_32x32x16_bf16 v[32:47], v[152:155], a[76:79], v[32:47]\n\t" \
  "ds_read_b64_tr_b16 v[136:137], v178 offset:43008\n\t" \
  "ds_read_b64_tr_b16 v[138:139], v178 offset:44032\n\t" \
  "v_mfma_f32_32x32x16_bf16 v[48:63], v[156:159], a[76:79], v[48:63]\n\t" \
  "ds_read_b64_tr_b16 v[140:141], v179 offset:43008\n\t" \
  "ds_read_b64_tr_b16 v[142:143], v179 offset:44032\n\t" \
  "s_waitcnt lgkmcnt(8)\n\t" \
  "v_mfma_f32_32x32x16_bf16 v[0:15], v[160:163], a[80:83], v[0:15]\n\t" \
  "ds_read_b64_tr_b16 v[144:145], v178 offset:20480\n\t" \
  "ds_read_b64_tr_b16 v[146:147], v178 offset:21504\n\t" \
  "v_mfma_f32_32x32x16_bf16 v[16:31], v[164:167], a[80:83], v[16:31]\n\t" \
  "ds_read_b64_tr_b16 v[148:149], v179 offset:20480\n\t" \
  "ds_read_b64_tr_b16 v[150:151], v179 offset:21504\n\t" \
  "v_mfma_f32_32x32x16_bf16 v[32:47], v[168:171], a[80:83], v[32:47]\n\t" \
  "ds_read_b64_tr_b16 v[152:153], v178 offset:45056\n\t" \
  "ds_read_b64_tr_b16 v[154:155], v178 offset:46080\n\t" \
  "v_mfma_f32_32x32x16_bf16 v[48:63], v[172:175], a[80:83], v[48:63]\n\t" \
  "ds_read_b64_tr_b16 v[156:157], v179 offset:45056\n\t" \
  "ds_read_b64_tr_b16 v[158:159], v179 offset:46080\n\t" \
  "s_waitcnt lgkmcnt(8)\n\t" \
  "v_mfma_f32_32x32x16_bf16 v[0:15], v[128:131], a[84:87], v[0:15]\n\t" \
  "ds_read_b64_tr_b16 v[160:161], v178 offset:22528\n\t" \
  "ds_read_b64_tr_b16 v[162:163], v178 offset:23552\n\t" \
  "ds_read_b64_tr_b16 v[164:165], v179 offset:22528\n\t" \
  "ds_read_b64_tr_b16 v[166:167], v179 offset:23552\n\t" \
  "v_mfma_f32_32x32x16_bf16 v[16:31], v[132:135], a[84:87], v[16:31]\n\t" \
  "ds_read_b64_tr_b16 v[168:169], v178 offset:47104\n\t" \
  "ds_read_b64_tr_b16 v[170:171], v178 offset:48128\n\t" \
  "ds_read_b64_tr_b16 v[172:173], v179 offset:47104\n\t" \
  "ds_read_b64_tr_b16 v[174:175], v179 offset:48128\n\t" \
  "v_mfma_f32_32x32x16_bf16 v[32:47], v[136:139], a[84:87], v[32:47]\n\t" \
  "v_mfma_f32_32x32x16_bf16 v[48:63], v[140:143], a[84:87], v[48:63]\n\t" \
  "s_waitcnt lgkmcnt(8)\n\t" \
  "v_mfma_f32_32x32x16_bf16 v[0:15], v[144:147], a[88:91], v[0:15]\n\t" \
  "s_waitcnt vmcnt(12) lgkmcnt(0)\n\ts_barrier\n\t" \
  "ds_read_b64_tr_b16 v[128:129], v180 offset:0\n\t" \
  "ds_read_b64_tr_b16 v[130:131], v180 offset:1024\n\t" \
  "v_mfma_f32_32x32x16_bf16 v[16:31], v[148:151], a[88:91], v[16:31]\n\t" \
  "ds_read_b64_tr_b16 v[132:133], v181 offset:0\n\t" \
  "ds_read_b64_tr_b16 v[134:135], v181 offset:1024\n\t" \
  "s_add_u32 s40, %5, 1769472\n\t" \
  "s_add_u32 m0, %14, 49152\n\t" \
  "s_nop 0\n\t" \
  "buffer_load_dwordx4 v185, %0, s40 offen lds\n\t" \
  "v_mfma_f32_32x32x16_bf16 v[32:47], v[152:155], a[88:91], v[32:47]\n\t" \
  "ds_read_b64_tr_b16 v[136:137], v180 offset:24576\n\t" \
  "ds_read_b64_tr_b16 v[138:139], v180 offset:25600\n\t" \
  "buffer_load_dwordx4 v186, %0, s40 offen offset:1024 lds\n\t" \
  "v_mfma_f32_32x32x16_bf16 v[48:63], v[156:159], a[88:91], v[48:63]\n\t" \
  "ds_read_b64_tr_b16 v[140:141], v181 offset:24576\n\t" \
  "ds_read_b64_tr_b16 v[142:143], v181 offset:25600\n\t" \
  "s_waitcnt lgkmcnt(8)\n\t" \
  "v_mfma_f32_32x32x16_bf16 v[0:15], v[160:163], a[92:95], v[0:15]\n\t" \
  "ds_read_b64_tr_b16 v[144:145], v180 offset:2048\n\t" \
  "ds_read_b64_tr_b16 v[146:147], v180 offset:3072\n\t" \
  "v_mfma_f32_32x32x16_bf16 v[16:31], v[164:167], a[92:95], v[16:31]\n\t" \
  "ds_read_b64_tr_b16 v[148:149], v181 offset:2048\n\t" \
  "ds_read_b64_tr_b16 v[150:151], v181 offset:3072\n\t" \
  "v_mfma_f32_32x32x16_bf16 v[32:47], v[168:171], a[92:95], v[32:47]\n\t" \
  "ds_read_b64_tr_b16 v[152:153], v180 offset:26624\n\t" \
  "ds_read_b64_tr_b16 v[154:155], v180 offset:27648\n\t" \
  "buffer_load_dwordx4 v187, %0, s40 offen offset:2048 lds\n\t" \
  "v_mfma_f32_32x32x16_bf16 v[48:63], v[172:175], a[92:95], v[48:63]\n\t" \
  "ds_read_b64_tr_b16 v[156:157], v181 offset:26624\n\t" \
  "ds_read_b64_tr_b16 v[158:159], v181 offset:27648\n\t" \
  "s_waitcnt lgkmcnt(8)\n\t" \
  "v_mfma_f32_32x32x16_bf16 v[0:15], v[128:131], a[96:99], v[0:15]\n\t" \
  "ds_read_b64_tr_b16 v[160:161], v180 offset:4096\n\t" \
  "ds_read_b64_tr_b16 v[162:163], v180 offset:5120\n\t" \
  "v_mfma_f32_32x32x16_bf16 v[16:31], v[132:135], a[96:99], v[16:31]\n\t" \
  "ds_read_b64_tr_b16 v[164:165], v181 offset:4096\n\t" \
  "ds_read_b64_tr_b16 v[166:167], v181 offset:5120\n\t" \
  "v_mfma_f32_32x32x16_bf16 v[32:47], v[136:139], a[96:99], v[32:47]\n\t" \
  "ds_read_b64_tr_b16 v[168:169], v180 offset:28672\n\t" \
  "ds_read_b64_tr_b16 v[170:171], v180 offset:29696\n\t" \
  "buffer_load_dwordx4 v188, %0, s40 offen offset:3072 lds\n\t" \
  "v_mfma_f32_32x32x16_bf16 v[48:63], v[140:143], a[96:99], v[48:63]\n\t" \
  "ds_read_b64_tr_b16 v[172:173], v181 offset:28672\n\t" \
  "ds_read_b64_tr_b16 v[174:175], v181 offset:29696\n\t" \
  "s_waitcnt lgkmcnt(8)\n\t" \
  "v_mfma_f32_32x32x16_bf16 v[0:15], v[144:147], a[100:103], v[0:15]\n\t" \
  "ds_read_b64_tr_b16 v[128:129], v180 offset:6144\n\t" \
  "ds_read_b64_tr_b16 v[130:131], v180 offset:7168\n\t" \
  "s_add_u32 s40, %5, 2064384\n\t" \
  "s_add_u32 m0, %14, 53248\n\t" \
  "v_mfma_f32_32x32x16_bf16 v[16:31], v[148:151], a[100:103], v[16:31]\n\t" \
  "ds_read_b64_tr_b16 v[132:133], v181 offset:6144\n\t" \
  "ds_read_b64_tr_b16 v[134:135], v181 offset:7168\n\t" \
  "buffer_load_dwordx4 v185, %0, s40 offen lds\n\t" \
  "v_mfma_f32_32x32x16_bf16 v[32:47], v[152:155], a[100:103], v[32:47]\n\t" \
  "ds_read_b64_tr_b16 v[136:137], v180 offset:30720\n\t" \
  "ds_read_b64_tr_b16 v[138:139], v180 offset:31744\n\t" \
  "v_mfma_f32_32x32x16_bf16 v[48:63], v[156:159], a[100:103], v[48:63]\n\t" \
  "ds_read_b64_tr_b16 v[140:141], v181 offset:30720\n\t" \
  "ds_read_b64_tr_b16 v[142:143], v181 offset:31744\n\t" \
  "s_waitcnt lgkmcnt(8)\n\t" \
  "v_mfma_f32_32x32x16_bf16 v[0:15], v[160:163], a[104:107], v[0:15]\n\t" \
  "ds_read_b64_tr_b16 v[144:145], v180 offset:8192\n\t" \
  "ds_read_b64_tr_b16 v[146:147], v180 offset:9216\n\t" \
  "v_mfma_f32_32x32x16_bf16 v[16:31], v[164:167], a[104:107], v[16:31]\n\t" \
  "ds_read_b64_tr_b16 v[148:149], v181 offset:8192\n\t" \
  "ds_read_b64_tr_b16 v[150:151], v181 offset:9216\n\t" \
  "buffer_load_dwordx4 v186, %0, s40 offen offset:1024 lds\n\t" \
  "v_mfma_f32_32x32x16_bf16 v[32:47], v[168:171], a[104:107], v[32:47]\n\t" \
  "ds_read_b64_tr_b16 v[152:153], v180 offset:32768\n\t" \
  "ds_read_b64_tr_b16 v[154:155], v180 offset:33792\n\t" \
  "v_mfma_f32_32x32x16_bf16 v[48:63], v[172:175], a[104:107], v[48:63]\n\t" \
  "ds_read_b64_tr_b16 v[156:157], v181 offset:32768\n\t" \
  "ds_read_b64_tr_b16 v[158:159], v181 offset:33792\n\t" \
  "s_waitcnt lgkmcnt(8)\n\t" \
  "v_mfma_f32_32x32x16_bf16 v[0:15], v[128:131], a[108:111], v[0:15]\n\t" \
  "ds_read_b64_tr_b16 v[160:161], v180 offset:10240\n\t" \
  "ds_read_b64_tr_b16 v[162:163], v180 offset:11264\n\t" \
  "v_mfma_f32_32x32x16_bf16 v[16:31], v[132:135], a[108:111], v[16:31]\n\t" \
  "ds_read_b64_tr_b16 v[164:165], v181 offset:10240\n\t" \
  "ds_read_b64_tr_b16 v[166:167], v181 offset:11264\n\t" \
  "buffer_load_dwordx4 v187, %0, s40 offen offset:2048 lds\n\t" \
  "v_mfma_f32_32x32x16_bf16 v[32:47], v[136:139], a[108:111], v[32:47]\n\t" \
  "ds_read_b64_tr_b16 v[168:169], v180 offset:34816\n\t" \
  "ds_read_b64_tr_b16 v[170:171], v180 offset:35840\n\t" \
  "buffer_load_dwordx4 v188, %0, s40 offen offset:3072 lds\n\t" \
  "v_mfma_f32_32x32x16_bf16 v[48:63], v[140:143], a[108:111], v[48:63]\n\t" \
  "ds_read_b64_tr_b16 v[172:173], v181 offset:34816\n\t" \
  "ds_read_b64_tr_b16 v[174:175], v181 offset:35840\n\t" \
  "s_waitcnt lgkmcnt(8)\n\t" \
  "v_mfma_f32_32x32x16_bf16 v[0:15], v[144:147], a[112:115], v[0:15]\n\t" \
  "ds_read_b64_tr_b16 v[128:129], v180 offset:12288\n\t" \
  "ds_read_b64_tr_b16 v[130:131], v180 offset:13312\n\t" \
  "v_mfma_f32_32x32x16_bf16 v[16:31], v[148:151], a[112:115], v[16:31]\n\t" \
  "ds_read_b64_tr_b16 v[132:133], v181 offset:12288\n\t" \
  "ds_read_b64_tr_b16 v[134:135], v181 offset:13312\n\t" \
  "s_add_u32 s40, %5, 2359296\n\t" \
  "s_add_u32 m0, %14, 57344\n\t" \
  "v_mfma_f32_32x32x16_bf16 v[32:47], v[152:155], a[112:115], v[32:47]\n\t" \
  "ds_read_b64_tr_b16 v[136:137], v180 offset:36864\n\t" \
  "ds_read_b64_tr_b16 v[138:139], v180 offset:37888\n\t" \
  "buffer_load_dwordx4 v185, %0, s40 offen lds\n\t" \
  "v_mfma_f32_32x32x16_bf16 v[48:63], v[156:159], a[112:115], v[48:63]\n\t" \
  "ds_read_b64_tr_b16 v[140:141], v181 offset:36864\n\t" \
  "ds_read_b64_tr_b16 v[142:143], v181 offset:37888\n\t" \
  "s_waitcnt lgkmcnt(8)\n\t" \
  "v_mfma_f32_32x32x16_bf16 v[0:15], v[160:163], a[116:119], v[0:15]\n\t" \
  "ds_read_b64_tr_b16 v[144:145], v180 offset:14336\n\t" \
  "ds_read_b64_tr_b16 v[146:147], v180 offset:15360\n\t" \
  "v_mfma_f32_32x32x16_bf16 v[16:31], v[164:167], a[116:119], v[16:31]\n\t" \
  "ds_read_b64_tr_b16 v[148:149], v181 offset:14336\n\t" \
  "ds_read_b64_tr_b16 v[150:151], v181 offset:15360\n\t" \
  "v_mfma_f32_32x32x16_bf16 v[32:47], v[168:171], a[116:119], v[32:47]\n\t" \
  "ds_read_b64_tr_b16 v[152:153], v180 offset:38912\n\t" \
  "ds_read_b64_tr_b16 v[154:155], v180 offset:39936\n\t" \
  "buffer_load_dwordx4 v186, %0, s40 offen offset:1024 lds\n\t" \
  "v_mfma_f32_32x32x16_bf16 v[48:63], v[172:175], a[116:119], v[48:63]\n\t" \
  "ds_read_b64_tr_b16 v[156:157], v181 offset:38912\n\t" \
  "ds_read_b64_tr_b16 v[158:159], v181 offset:39936\n\t" \
  "s_waitcnt lgkmcnt(8)\n\t" \
  "v_mfma_f32_32x32x16_bf16 v[0:15], v[128:131], a[120:123], v[0:15]\n\t" \
  "ds_read_b64_tr_b16 v[160:161], v180 offset:16384\n\t" \
  "ds_read_b64_tr_b16 v[162:163], v180 offset:17408\n\t" \
  "v_mfma_f32_32x32x16_bf16 v[16:31], v[132:135], a[120:123], v[16:31]\n\t" \
  "ds_read_b64_tr_b16 v[164:165], v181 offset:16384\n\t" \
  "ds_read_b64_tr_b16 v[166:167], v181 offset:17408\n\t" \
  "buffer_load_dwordx4 v187, %0, s40 offen offset:2048 lds\n\t" \
  "v_mfma_f32_32x32x16_bf16 v[32:47], v[136:139], a[120:123], v[32:47]\n\t" \
  "ds_read_b64_tr_b16 v[168:169], v180 offset:40960\n\t" \
  "ds_read_b64_tr_b16 v[170:171], v180 offset:41984\n\t" \
  "v_mfma_f32_32x32x16_bf16 v[48:63], v[140:143], a[120:123], v[48:63]\n\t" \
  "ds_read_b64_tr_b16 v[172:173], v181 offset:40960\n\t" \
  "ds_read_b64_tr_b16 v[174:175], v181 offset:41984\n\t" \
  "s_waitcnt lgkmcnt(8)\n\t" \
  "v_mfma_f32_32x32x16_bf16 v[0:15], v[144:147], a[124:127], v[0:15]\n\t" \
  "ds_read_b64_tr_b16 v[128:129], v180 offset:18432\n\t" \
  "ds_read_b64_tr_b16 v[130:131], v180 offset:19456\n\t" \
  "v_mfma_f32_32x32x16_bf16 v[16:31], v[148:151], a[124:127], v[16:31]\n\t" \
  "ds_read_b64_tr_b16 v[132:133], v181 offset:18432\n\t" \
  "ds_read_b64_tr_b16 v[134:135], v181 offset:19456\n\t" \
  "buffer_load_dwordx4 v188, %0, s40 offen offset:3072 lds\n\t" \
  "v_mfma_f32_32x32x16_bf16 v[32:47], v[152:155], a[124:127], v[32:47]\n\t" \
  "ds_read_b64_tr_b16 v[136:137], v180 offset:43008\n\t" \
  "ds_read_b64_tr_b16 v[138:139], v180 offset:44032\n\t" \
  "v_mfma_f32_32x32x16_bf16 v[48:63], v[156:159], a[124:127], v[48:63]\n\t" \
  "ds_read_b64_tr_b16 v[140:141], v181 offset:43008\n\t" \
  "ds_read_b64_tr_b16 v[142:143], v181 offset:44032\n\t" \
  "s_waitcnt lgkmcnt(8)\n\t" \
  "v_mfma_f32_32x32x16_bf16 v[0:15], v[160:163], a[128:131], v[0:15]\n\t" \
  "ds_read_b64_tr_b16 v[144:145], v180 offset:20480\n\t" \
  "ds_read_b64_tr_b16 v[146:147], v180 offset:21504\n\t" \
  "v_mfma_f32_32x32x16_bf16 v[16:31], v[164:167], a[128:131], v[16:31]\n\t" \
  "ds_read_b64_tr_b16 v[148:149], v181 offset:20480\n\t" \
  "ds_read_b64_tr_b16 v[150:151], v181 offset:21504\n\t" \
  "v_mfma_f32_32x32x16_bf16 v[32:47], v[168:171], a[128:131], v[32:47]\n\t" \
  "ds_read_b64_tr_b16 v[152:153], v180 offset:45056\n\t" \
  "ds_read_b64_tr_b16 v[154:155], v180 offset:46080\n\t" \
  "v_mfma_f32_32x32x16_bf16 v[48:63], v[172:175], a[128:131], v[48:63]\n\t" \
  "ds_read_b64_tr_b16 v[156:157], v181 offset:45056\n\t" \
  "ds_read_b64_tr_b16 v[158:159], v181 offset:46080\n\t" \
  "s_waitcnt lgkmcnt(8)\n\t" \
  "v_mfma_f32_32x32x16_bf16 v[0:15], v[128:131], a[132:135], v[0:15]\n\t" \
  "ds_read_b64_tr_b16 v[160:161], v180 offset:22528\n\t" \
  "ds_read_b64_tr_b16 v[162:163], v180 offset:23552\n\t" \
  "ds_read_b64_tr_b16 v[164:165], v181 offset:22528\n\t" \
  "ds_read_b64_tr_b16 v[166:167], v181 offset:23552\n\t" \
  "v_mfma_f32_32x32x16_bf16 v[16:31], v[132:135], a[132:135], v[16:31]\n\t" \
  "ds_read_b64_tr_b16 v[168:169], v180 offset:47104\n\t" \
  "ds_read_b64_tr_b16 v[170:171], v180 offset:48128\n\t" \
  "ds_read_b64_tr_b16 v[172:173], v181 offset:47104\n\t" \
  "ds_read_b64_tr_b16 v[174:175], v181 offset:48128\n\t" \
  "v_mfma_f32_32x32x16_bf16 v[32:47], v[136:139], a[132:135], v[32:47]\n\t" \
  "v_mfma_f32_32x32x16_bf16 v[48:63], v[140:143], a[132:135], v[48:63]\n\t" \
  "s_waitcnt lgkmcnt(8)\n\t" \
  "v_mfma_f32_32x32x16_bf16 v[0:15], v[144:147], a[136:139], v[0:15]\n\t" \
  "s_waitcnt vmcnt(12) lgkmcnt(0)\n\ts_barrier\n\t" \
  "ds_read_b64_tr_b16 v[128:129], v176 offset:0\n\t" \
  "ds_read_b64_tr_b16 v[130:131], v176 offset:1024\n\t" \
  "v_mfma_f32_32x32x16_bf16 v[16:31], v[148:151], a[136:139], v[16:31]\n\t" \
  "ds_read_b64_tr_b16 v[132:133], v177 offset:0\n\t" \
  "ds_read_b64_tr_b16 v[134:135], v177 offset:1024\n\t" \
  "s_add_u32 s40, %5, 3538944\n\t" \
  "s_add_u32 m0, %14, 98304\n\t" \
  "s_nop 0\n\t" \
  "buffer_load_dwordx4 v185, %0, s40 offen lds\n\t" \
  "v_mfma_f32_32x32x16_bf16 v[32:47], v[152:155], a[136:139], v[32:47]\n\t" \
  "ds_read_b64_tr_b16 v[136:137], v176 offset:24576\n\t" \
  "ds_read_b64_tr_b16 v[138:139], v176 offset:25600\n\t" \
  "buffer_load_dwordx4 v186, %0, s40 offen offset:1024 lds\n\t" \
  "v_mfma_f32_32x32x16_bf16 v[48:63], v[156:159], a[136:139], v[48:63]\n\t" \
  "ds_read_b64_tr_b16 v[140:141], v177 offset:24576\n\t" \
  "ds_read_b64_tr_b16 v[142:143], v177 offset:25600\n\t" \
  "s_waitcnt lgkmcnt(8)\n\t" \
  "v_mfma_f32_32x32x16_bf16 v[0:15], v[160:163], a[140:143], v[0:15]\n\t" \
  "ds_read_b64_tr_b16 v[144:145], v176 offset:2048\n\t" \
  "ds_read_b64_tr_b16 v[146:147], v176 offset:3072\n\t" \
  "v_mfma_f32_32x32x16_bf16 v[16:31], v[164:167], a[140:143], v[16:31]\n\t" \
  "ds_read_b64_tr_b16 v[148:149], v177 offset:2048\n\t" \
  "ds_read_b64_tr_b16 v[150:151], v177 offset:3072\n\t" \
  "v_mfma_f32_32x32x16_bf16 v[32:47], v[168:171], a[140:143], v[32:47]\n\t" \
  "ds_read_b64_tr_b16 v[152:153], v176 offset:26624\n\t" \
  "ds_read_b64_tr_b16 v[154:155], v176 offset:27648\n\t" \
  "buffer_load_dwordx4 v187, %0, s40 offen offset:2048 lds\n\t" \
  "v_mfma_f32_32x32x16_bf16 v[48:63], v[172:175], a[140:143], v[48:63]\n\t" \
  "ds_read_b64_tr_b16 v[156:157], v177 offset:26624\n\t" \
  "ds_read_b64_tr_b16 v[158:159], v177 offset:27648\n\t" \
  "s_waitcnt lgkmcnt(8)\n\t" \
  "v_mfma_f32_32x32x16_bf16 v[64:79], v[128:131], a[0:3], 0\n\t" \
  "ds_read_b64_tr_b16 v[160:161], v176 offset:4096\n\t" \
  "ds_read_b64_tr_b16 v[162:163], v176 offset:5120\n\t" \
  "v_mfma_f32_32x32x16_bf16 v[80:95], v[132:135], a[0:3], 0\n\t" \
  "ds_read_b64_tr_b16 v[164:165], v177 offset:4096\n\t" \
  "ds_read_b64_tr_b16 v[166:167], v177 offset:5120\n\t" \
  "v_mfma_f32_32x32x16_bf16 v[96:111], v[136:139], a[0:3], 0\n\t" \
  "ds_read_b64_tr_b16 v[168:169], v176 offset:28672\n\t" \
  "ds_read_b64_tr_b16 v[170:171], v176 offset:29696\n\t" \
  "buffer_load_dwordx4 v188, %0, s40 offen offset:3072 lds\n\t" \
  "v_mfma_f32_32x32x16_bf16 v[112:127], v[140:143], a[0:3], 0\n\t" \
  "ds_read_b64_tr_b16 v[172:173], v177 offset:28672\n\t" \
  "ds_read_b64_tr_b16 v[174:175], v177 offset:29696\n\t" \
  "v_mul_f32 v32, v0, v32\n\t" \
  "v_mul_f32 v33, v1, v33\n\t" \
  "v_mul_f32 v34, v2, v34\n\t" \
  "s_waitcnt lgkmcnt(8)\n\t" \
  "v_mfma_f32_32x32x16_bf16 v[64:79], v[144:147], a[4:7], v[64:79]\n\t" \
  "ds_read_b64_tr_b16 v[128:129], v176 offset:6144\n\t" \
  "ds_read_b64_tr_b16 v[130:131], v176 offset:7168\n\t" \
  "s_add_u32 s40, %5, 3833856\n\t" \
  "s_add_u32 m0, %14, 102400\n\t" \
  "v_mul_f32 v35, v3, v35\n\t" \
  "v_mfma_f32_32x32x16_bf16 v[80:95], v[148:151], a[4:7], v[80:95]\n\t" \
  "ds_read_b64_tr_b16 v[132:133], v177 offset:6144\n\t" \
  "ds_read_b64_tr_b16 v[134:135], v177 offset:7168\n\t" \
  "buffer_load_dwordx4 v185, %0, s40 offen lds\n\t" \
  "v_mul_f32 v0, v196, v0\n\t" \
  "v_mul_f32 v1, v196, v1\n\t" \
  "v_mfma_f32_32x32x16_bf16 v[96:111], v[152:155], a[4:7], v[96:111]\n\t" \
  "ds_read_b64_tr_b16 v[136:137], v176 offset:30720\n\t" \
  "ds_read_b64_tr_b16 v[138:139], v176 offset:31744\n\t" \
  "v_mul_f32 v2, v196, v2\n\t" \
  "v_mfma_f32_32x32x16_bf16 v[112:127], v[156:159], a[4:7], v[112:127]\n\t" \
  "ds_read_b64_tr_b16 v[140:141], v177 offset:30720\n\t" \
  "ds_read_b64_tr_b16 v[142:143], v177 offset:31744\n\t" \
  "v_mul_f32 v3, v196, v3\n\t" \
  "v_exp_f32 v0, v0\n\t" \
  "s_waitcnt lgkmcnt(8)\n\t" \
  "v_mfma_f32_32x32x16_bf16 v[64:79], v[160:163], a[8:11], v[64:79]\n\t" \
  "ds_read_b64_tr_b16 v[144:145], v176 offset:8192\n\t" \
  "ds_read_b64_tr_b16 v[146:147], v176 offset:9216\n\t" \
  "v_exp_f32 v1, v1\n\t" \
  "v_exp_f32 v2, v2\n\t" \
  "v_mfma_f32_32x32x16_bf16 v[80:95], v[164:167], a[8:11], v[80:95]\n\t" \
  "ds_read_b64_tr_b16 v[148:149], v177 offset:8192\n\t" \
  "ds_read_b64_tr_b16 v[150:151], v177 offset:9216\n\t" \
  "buffer_load_dwordx4 v186, %0, s40 offen offset:1024 lds\n\t" \
  "v_exp_f32 v3, v3\n\t" \
  "v_mfma_f32_32x32x16_bf16 v[96:111], v[168:171], a[8:11], v[96:111]\n\t" \
  "ds_read_b64_tr_b16 v[152:153], v176 offset:32768\n\t" \
  "ds_read_b64_tr_b16 v[154:155], v176 offset:33792\n\t" \
  "v_fma_f32 v0, v0, v197, v197\n\t" \
  "v_fma_f32 v1, v1, v197, v197\n\t" \
  "v_mfma_f32_32x32x16_bf16 v[112:127], v[172:175], a[8:11], v[112:127]\n\t" \
  "ds_read_b64_tr_b16 v[156:157], v177 offset:32768\n\t" \
  "ds_read_b64_tr_b16 v[158:159], v177 offset:33792\n\t" \
  "v_fma_f32 v2, v2, v197, v197\n\t" \
  "s_waitcnt lgkmcnt(8)\n\t" \
  "v_mfma_f32_32x32x16_bf16 v[64:79], v[128:131], a[12:15], v[64:79]\n\t" \
  "ds_read_b64_tr_b16 v[160:161], v176 offset:10240\n\t" \
  "ds_read_b64_tr_b16 v[162:163], v176 offset:11264\n\t" \
  "v_fma_f32 v3, v3, v197, v197\n\t" \
  "v_rcp_f32 v0, v0\n\t" \
  "v_mfma_f32_32x32x16_bf16 v[80:95], v[132:135], a[12:15], v[80:95]\n\t" \
  "ds_read_b64_tr_b16 v[164:165], v177 offset:10240\n\t" \
  "ds_read_b64_tr_b16 v[166:167], v177 offset:11264\n\t" \
  "buffer_load_dwordx4 v187, %0, s40 offen offset:2048 lds\n\t" \
  "v_rcp_f32 v1, v1\n\t" \
  "v_mfma_f32_32x32x16_bf16 v[96:111], v[136:139], a[12:15], v[96:111]\n\t" \
  "ds_read_b64_tr_b16 v[168:169], v176 offset:34816\n\t" \
  "ds_read_b64_tr_b16 v[170:171], v176 offset:35840\n\t" \
  "buffer_load_dwordx4 v188, %0, s40 offen offset:3072 lds\n\t" \
  "v_rcp_f32 v2, v2\n\t" \
  "v_rcp_f32 v3, v3\n\t" \
  "v_mfma_f32_32x32x16_bf16 v[112:127], v[140:143], a[12:15], v[112:127]\n\t" \
  "ds_read_b64_tr_b16 v[172:173], v177 offset:34816\n\t" \
  "ds_read_b64_tr_b16 v[174:175], v177 offset:35840\n\t" \
  "v_mul_f32 v32, v32, v0\n\t" \
  "v_mul_f32 v33, v33, v1\n\t" \
  "s_waitcnt lgkmcnt(8)\n\t" \
  "v_mfma_f32_32x32x16_bf16 v[64:79], v[144:147], a[16:19], v[64:79]\n\t" \
  "ds_read_b64_tr_b16 v[128:129], v176 offset:12288\n\t" \
  "ds_read_b64_tr_b16 v[130:131], v176 offset:13312\n\t" \
  "v_mul_f32 v34, v34, v2\n\t" \
  "v_mfma_f32_32x32x16_bf16 v[80:95], v[148:151], a[16:19], v[80:95]\n\t" \
  "ds_read_b64_tr_b16 v[132:133], v177 offset:12288\n\t" \
  "ds_read_b64_tr_b16 v[134:135], v177 offset:13312\n\t" \
  "s_add_u32 s40, %5, 4128768\n\t" \
  "s_add_u32 m0, %14, 106496\n\t" \
  "v_mul_f32 v35, v35, v3\n\t" \
  "v_cvt_pk_bf16_f32 v0, v32, v33\n\t" \
  "v_mfma_f32_32x32x16_bf16 v[96:111], v[152:155], a[16:19], v[96:111]\n\t" \
  "ds_read_b64_tr_b16 v[136:137], v176 offset:36864\n\t" \
  "ds_read_b64_tr_b16 v[138:139], v176 offset:37888\n\t" \
  "buffer_load_dwordx4 v185, %0, s40 offen lds\n\t" \
  "v_cvt_pk_bf16_f32 v1, v34, v35\n\t" \
  "v_mfma_f32_32x32x16_bf16 v[112:127], v[156:159], a[16:19], v[112:127]\n\t" \
  "ds_read_b64_tr_b16 v[140:141], v177 offset:36864\n\t" \
  "ds_read_b64_tr_b16 v[142:143], v177 offset:37888\n\t" \
  "v_accvgpr_write_b32 a144, v0\n\t" \
  "v_accvgpr_write_b32 a145, v1\n\t" \
  "s_waitcnt lgkmcnt(8)\n\t" \
  "v_mfma_f32_32x32x16_bf16 v[64:79], v[160:163], a[20:23], v[64:79]\n\t" \
  "ds_read_b64_tr_b16 v[144:145], v176 offset:14336\n\t" \
  "ds_read_b64_tr_b16 v[146:147], v176 offset:15360\n\t" \
  "v_mul_f32 v36, v4, v36\n\t" \
  "v_mul_f32 v37, v5, v37\n\t" \
  "v_mfma_f32_32x32x16_bf16 v[80:95], v[164:167], a[20:23], v[80:95]\n\t" \
  "ds_read_b64_tr_b16 v[148:149], v177 offset:14336\n\t" \
  "ds_read_b64_tr_b16 v[150:151], v177 offset:15360\n\t" \
  "v_mul_f32 v38, v6, v38\n\t" \
  "v_mfma_f32_32x32x16_bf16 v[96:111], v[168:171], a[20:23], v[96:111]\n\t" \
  "ds_read_b64_tr_b16 v[152:153], v176 offset:38912\n\t" \
  "ds_read_b64_tr_b16 v[154:155], v176 offset:39936\n\t" \
  "buffer_load_dwordx4 v186, %0, s40 offen offset:1024 lds\n\t" \
  "v_mul_f32 v39, v7, v39\n\t" \
  "v_mul_f32 v4, v196, v4\n\t" \
  "v_mfma_f32_32x32x16_bf16 v[112:127], v[172:175], a[20:23], v[112:127]\n\t" \
  "ds_read_b64_tr_b16 v[156:157], v177 offset:38912\n\t" \
  "ds_read_b64_tr_b16 v[158:159], v177 offset:39936\n\t" \
  "v_mul_f32 v5, v196, v5\n\t" \
  "s_waitcnt lgkmcnt(8)\n\t" \
  "v_mfma_f32_32x32x16_bf16 v[64:79], v[128:131], a[24:27], v[64:79]\n\t" \
  "ds_read_b64_tr_b16 v[160:161], v176 offset:16384\n\t" \
  "ds_read_b64_tr_b16 v[162:163], v176 offset:17408\n\t" \
  "v_mul_f32 v6, v196, v6\n\t" \
  "v_mul_f32 v7, v196, v7\n\t" \
  "v_mfma_f32_32x32x16_bf16 v[80:95], v[132:135], a[24:27], v[80:95]\n\t" \
  "ds_read_b64_tr_b16 v[164:165], v177 offset:16384\n\t" \
  "ds_read_b64_tr_b16 v[166:167], v177 offset:17408\n\t" \
  "buffer_load_dwordx4 v187, %0, s40 offen offset:2048 lds\n\t" \
  "v_exp_f32 v4, v4\n\t" \
  "v_exp_f32 v5, v5\n\t" \
  "v_mfma_f32_32x32x16_bf16 v[96:111], v[136:139], a[24:27], v[96:111]\n\t" \
  "ds_read_b64_tr_b16 v[168:169], v176 offset:40960\n\t" \
  "ds_read_b64_tr_b16 v[170:171], v176 offset:41984\n\t" \
  "v_exp_f32 v6, v6\n\t" \
  "v_mfma_f32_32x32x16_bf16 v[112:127], v[140:143], a[24:27], v[112:127]\n\t" \
  "ds_read_b64_tr_b16 v[172:173], v177 offset:40960\n\t" \
  "ds_read_b64_tr_b16 v[174:175], v177 offset:41984\n\t" \
  "v_exp_f32 v7, v7\n\t" \
  "v_fma_f32 v4, v4, v197, v197\n\t" \
  "s_waitcnt lgkmcnt(8)\n\t" \
  "v_mfma_f32_32x32x16_bf16 v[64:79], v[144:147], a[28:31], v[64:79]\n\t" \
  "ds_read_b64_tr_b16 v[128:129], v176 offset:18432\n\t" \
  "ds_read_b64_tr_b16 v[130:131], v176 offset:19456\n\t" \
  "v_fma_f32 v5, v5, v197, v197\n\t" \
  "v_mfma_f32_32x32x16_bf16 v[80:95], v[148:151], a[28:31], v[80:95]\n\t" \
  "ds_read_b64_tr_b16 v[132:133], v177 offset:18432\n\t" \
  "ds_read_b64_tr_b16 v[134:135], v177 offset:19456\n\t" \
  "buffer_load_dwordx4 v188, %0, s40 offen offset:3072 lds\n\t" \
  "v_fma_f32 v6, v6, v197, v197\n\t" \
  "v_fma_f32 v7, v7, v197, v197\n\t" \
  "v_mfma_f32_32x32x16_bf16 v[96:111], v[152:155], a[28:31], v[96:111]\n\t" \
  "ds_read_b64_tr_b16 v[136:137], v176 offset:43008\n\t" \
  "ds_read_b64_tr_b16 v[138:139], v176 offset:44032\n\t" \
  "v_rcp_f32 v4, v4\n\t" \
  "v_mfma_f32_32x32x16_bf16 v[112:127], v[156:159], a[28:31], v[112:127]\n\t" \
  "ds_read_b64_tr_b16 v[140:141], v177 offset:43008\n\t" \
  "ds_read_b64_tr_b16 v[142:143], v177 offset:44032\n\t" \
  "v_rcp_f32 v5, v5\n\t" \
  "v_rcp_f32 v6, v6\n\t" \
  "s_waitcnt lgkmcnt(8)\n\t" \
  "v_mfma_f32_32x32x16_bf16 v[64:79], v[160:163], a[32:35], v[64:79]\n\t" \
  "ds_read_b64_tr_b16 v[144:145], v176 offset:20480\n\t" \
  "ds_read_b64_tr_b16 v[146:147], v176 offset:21504\n\t" \
  "v_rcp_f32 v7, v7\n\t" \
  "v_mul_f32 v36, v36, v4\n\t" \
  "v_mfma_f32_32x32x16_bf16 v[80:95], v[164:167], a[32:35], v[80:95]\n\t" \
  "ds_read_b64_tr_b16 v[148:149], v177 offset:20480\n\t" \
  "ds_read_b64_tr_b16 v[150:151], v177 offset:21504\n\t" \
  "v_mul_f32 v37, v37, v5\n\t" \
  "v_mfma_f32_32x32x16_bf16 v[96:111], v[168:171], a[32:35], v[96:111]\n\t" \
  "ds_read_b64_tr_b16 v[152:153], v176 offset:45056\n\t" \
  "ds_read_b64_tr_b16 v[154:155], v176 offset:46080\n\t" \
  "v_mul_f32 v38, v38, v6\n\t" \
  "v_mul_f32 v39, v39, v7\n\t" \
  "v_mfma_f32_32x32x16_bf16 v[112:127], v[172:175], a[32:35], v[112:127]\n\t" \
  "ds_read_b64_tr_b16 v[156:157], v177 offset:45056\n\t" \
  "ds_read_b64_tr_b16 v[158:159], v177 offset:46080\n\t" \
  "v_cvt_pk_bf16_f32 v4, v36, v37\n\t" \
  "s_waitcnt lgkmcnt(8)\n\t" \
  "v_mfma_f32_32x32x16_bf16 v[64:79], v[128:131], a[36:39], v[64:79]\n\t" \
  "ds_read_b64_tr_b16 v[160:161], v176 offset:22528\n\t" \
  "ds_read_b64_tr_b16 v[162:163], v176 offset:23552\n\t" \
  "ds_read_b64_tr_b16 v[164:165], v177 offset:22528\n\t" \
  "ds_read_b64_tr_b16 v[166:167], v177 offset:23552\n\t" \
  "v_cvt_pk_bf16_f32 v5, v38, v39\n\t" \
  "v_accvgpr_write_b32 a146, v4\n\t" \
  "v_mfma_f32_32x32x16_bf16 v[80:95], v[132:135], a[36:39], v[80:95]\n\t" \
  "ds_read_b64_tr_b16 v[168:169], v176 offset:47104\n\t" \
  "ds_read_b64_tr_b16 v[170:171], v176 offset:48128\n\t" \
  "ds_read_b64_tr_b16 v[172:173], v177 offset:47104\n\t" \
  "ds_read_b64_tr_b16 v[174:175], v177 offset:48128\n\t" \
  "v_accvgpr_write_b32 a147, v5\n\t" \
  "v_mul_f32 v40, v8, v40\n\t" \
  "v_mfma_f32_32x32x16_bf16 v[96:111], v[136:139], a[36:39], v[96:111]\n\t" \
  "v_mul_f32 v41, v9, v41\n\t" \
  "v_mfma_f32_32x32x16_bf16 v[112:127], v[140:143], a[36:39], v[112:127]\n\t" \
  "v_mul_f32 v42, v10, v42\n\t" \
  "v_mul_f32 v43, v11, v43\n\t" \
  "s_waitcnt lgkmcnt(8)\n\t" \
  "v_mfma_f32_32x32x16_bf16 v[64:79], v[144:147], a[40:43], v[64:79]\n\t" \
  "s_waitcnt vmcnt(12) lgkmcnt(0)\n\ts_barrier\n\t" \
  "ds_read_b64_tr_b16 v[128:129], v178 offset:0\n\t" \
  "ds_read_b64_tr_b16 v[130:131], v178 offset:1024\n\t" \
  "v_mul_f32 v8, v196, v8\n\t" \
  "v_mfma_f32_32x32x16_bf16 v[80:95], v[148:151], a[40:43], v[80:95]\n\t" \
  "ds_read_b64_tr_b16 v[132:133], v179 offset:0\n\t" \
  "ds_read_b64_tr_b16 v[134:135], v179 offset:1024\n\t" \
  "s_add_u32 s40, %6, 0\n\t" \
  "s_add_u32 m0, %14, 0\n\t" \
  "s_nop 0\n\t" \
  "buffer_load_dwordx4 v185, %0, s40 offen lds\n\t" \
  "v_mul_f32 v9, v196, v9\n\t" \
  "v_mul_f32 v10, v196, v10\n\t" \
  "v_mfma_f32_32x32x16_bf16 v[96:111], v[152:155], a[40:43], v[96:111]\n\t" \
  "ds_read_b64_tr_b16 v[136:137], v178 offset:24576\n\t" \
  "ds_read_b64_tr_b16 v[138:139], v178 offset:25600\n\t" \
  "buffer_load_dwordx4 v186, %0, s40 offen offset:1024 lds\n\t" \
  "v_mul_f32 v11, v196, v11\n\t" \
  "v_exp_f32 v8, v8\n\t" \
  "v_mfma_f32_32x32x16_bf16 v[112:127], v[156:159], a[40:43], v[112:127]\n\t" \
  "ds_read_b64_tr_b16 v[140:141], v179 offset:24576\n\t" \
  "ds_read_b64_tr_b16 v[142:143], v179 offset:25600\n\t" \
  "v_exp_f32 v9, v9\n\t" \
  "s_waitcnt lgkmcnt(8)\n\t" \
  "v_mfma_f32_32x32x16_bf16 v[64:79], v[160:163], a[44:47], v[64:79]\n\t" \
  "ds_read_b64_tr_b16 v[144:145], v178 offset:2048\n\t" \
  "ds_read_b64_tr_b16 v[146:147], v178 offset:3072\n\t" \
  "v_exp_f32 v10, v10\n\t" \
  "v_exp_f32 v11, v11\n\t" \
  "v_mfma_f32_32x32x16_bf16 v[80:95], v[164:167], a[44:47], v[80:95]\n\t" \
  "ds_read_b64_tr_b16 v[148:149], v179 offset:2048\n\t" \
  "ds_read_b64_tr_b16 v[150:151], v179 offset:3072\n\t" \
  "v_fma_f32 v8, v8, v197, v197\n\t" \
  "v_mfma_f32_32x32x16_bf16 v[96:111], v[168:171], a[44:47], v[96:111]\n\t" \
  "ds_read_b64_tr_b16 v[152:153], v178 offset:26624\n\t" \
  "ds_read_b64_tr_b16 v[154:155], v178 offset:27648\n\t" \
  "buffer_load_dwordx4 v187, %0, s40 offen offset:2048 lds\n\t" \
  "v_fma_f32 v9, v9, v197, v197\n\t" \
  "v_fma_f32 v10, v10, v197, v197\n\t" \
  "v_mfma_f32_32x32x16_bf16 v[112:127], v[172:175], a[44:47], v[112:127]\n\t" \
  "ds_read_b64_tr_b16 v[156:157], v179 offset:26624\n\t" \
  "ds_read_b64_tr_b16 v[158:159], v179 offset:27648\n\t" \
  "v_fma_f32 v11, v11, v197, v197\n\t" \
  "s_waitcnt lgkmcnt(8)\n\t" \
  "v_mfma_f32_32x32x16_bf16 v[64:79], v[128:131], a[48:51], v[64:79]\n\t" \
  "ds_read_b64_tr_b16 v[160:161], v178 offset:4096\n\t" \
  "ds_read_b64_tr_b16 v[162:163], v178 offset:5120\n\t" \
  "v_rcp_f32 v8, v8\n\t" \
  "v_rcp_f32 v9, v9\n\t" \
  "v_mfma_f32_32x32x16_bf16 v[80:95], v[132:135], a[48:51], v[80:95]\n\t" \
  "ds_read_b64_tr_b16 v[164:165], v179 offset:4096\n\t" \
  "ds_read_b64_tr_b16 v[166:167], v179 offset:5120\n\t" \
  "v_rcp_f32 v10, v10\n\t" \
  "v_rcp_f32 v11, v11\n\t" \
  "v_mfma_f32_32x32x16_bf16 v[96:111], v[136:139], a[48:51], v[96:111]\n\t" \
  "ds_read_b64_tr_b16 v[168:169], v178 offset:28672\n\t" \
  "ds_read_b64_tr_b16 v[170:171], v178 offset:29696\n\t" \
  "buffer_load_dwordx4 v188, %0, s40 offen offset:3072 lds\n\t" \
  "v_mul_f32 v40, v40, v8\n\t" \
  "v_mfma_f32_32x32x16_bf16 v[112:127], v[140:143], a[48:51], v[112:127]\n\t" \
  "ds_read_b64_tr_b16 v[172:173], v179 offset:28672\n\t" \
  "ds_read_b64_tr_b16 v[174:175], v179 offset:29696\n\t" \
  "v_mul_f32 v41, v41, v9\n\t" \
  "v_mul_f32 v42, v42, v10\n\t" \
  "s_waitcnt lgkmcnt(8)\n\t" \
  "v_mfma_f32_32x32x16_bf16 v[64:79], v[144:147], a[52:55], v[64:79]\n\t" \
  "ds_read_b64_tr_b16 v[128:129], v178 offset:6144\n\t" \
  "ds_read_b64_tr_b16 v[130:131], v178 offset:7168\n\t" \
  "s_add_u32 s40, %6, 294912\n\t" \
  "s_add_u32 m0, %14, 4096\n\t" \
  "v_mul_f32 v43, v43, v11\n\t" \
  "v_mfma_f32_32x32x16_bf16 v[80:95], v[148:151], a[52:55], v[80:95]\n\t" \
  "ds_read_b64_tr_b16 v[132:133], v179 offset:6144\n\t" \
  "ds_read_b64_tr_b16 v[134:135], v179 offset:7168\n\t" \
  "buffer_load_dwordx4 v185, %0, s40 offen lds\n\t" \
  "v_cvt_pk_bf16_f32 v8, v40, v41\n\t" \
  "v_cvt_pk_bf16_f32 v9, v42, v43\n\t" \
  "v_mfma_f32_32x32x16_bf16 v[96:111], v[152:155], a[52:55], v[96:111]\n\t" \
  "ds_read_b64_tr_b16 v[136:137], v178 offset:30720\n\t" \
  "ds_read_b64_tr_b16 v[138:139], v178 offset:31744\n\t" \
  "v_accvgpr_write_b32 a148, v8\n\t" \
  "v_accvgpr_write_b32 a149, v9\n\t" \
  "v_mfma_f32_32x32x16_bf16 v[112:127], v[156:159], a[52:55], v[112:127]\n\t" \
  "ds_read_b64_tr_b16 v[140:141], v179 offset:30720\n\t" \
  "ds_read_b64_tr_b16 v[142:143], v179 offset:31744\n\t" \
  "v_mul_f32 v44, v12, v44\n\t" \
  "s_waitcnt lgkmcnt(8)\n\t" \
  "v_mfma_f32_32x32x16_bf16 v[64:79], v[160:163], a[56:59], v[64:79]\n\t" \
  "ds_read_b64_tr_b16 v[144:145], v178 offset:8192\n\t" \
  "ds_read_b64_tr_b16 v[146:147], v178 offset:9216\n\t" \
  "v_mul_f32 v45, v13, v45\n\t" \
  "v_mul_f32 v46, v14, v46\n\t" \
  "v_mfma_f32_32x32x16_bf16 v[80:95], v[164:167], a[56:59], v[80:95]\n\t" \
  "ds_read_b64_tr_b16 v[148:149], v179 offset:8192\n\t" \
  "ds_read_b64_tr_b16 v[150:151], v179 offset:9216\n\t" \
  "buffer_load_dwordx4 v186, %0, s40 offen offset:1024 lds\n\t" \
  "v_mul_f32 v47, v15, v47\n\t" \
  "v_mfma_f32_32x32x16_bf16 v[96:111], v[168:171], a[56:59], v[96:111]\n\t" \
  "ds_read_b64_tr_b16 v[152:153], v178 offset:32768\n\t" \
  "ds_read_b64_tr_b16 v[154:155], v178 offset:33792\n\t" \
  "v_mul_f32 v12, v196, v12\n\t" \
  "v_mul_f32 v13, v196, v13\n\t" \
  "v_mfma_f32_32x32x16_bf16 v[112:127], v[172:175], a[56:59], v[112:127]\n\t" \
  "ds_read_b64_tr_b16 v[156:157], v179 offset:32768\n\t" \
  "ds_read_b64_tr_b16 v[158:159], v179 offset:33792\n\t" \
  "v_mul_f32 v14, v196, v14\n\t" \
  "v_mul_f32 v15, v196, v15\n\t" \
  "s_waitcnt lgkmcnt(8)\n\t" \
  "v_mfma_f32_32x32x16_bf16 v[64:79], v[128:131], a[60:63], v[64:79]\n\t" \
  "ds_read_b64_tr_b16 v[160:161], v178 offset:10240\n\t" \
  "ds_read_b64_tr_b16 v[162:163], v178 offset:11264\n\t" \
  "v_exp_f32 v12, v12\n\t" \
  "v_mfma_f32_32x32x16_bf16 v[80:95], v[132:135], a[60:63], v[80:95]\n\t" \
  "ds_read_b64_tr_b16 v[164:165], v179 offset:10240\n\t" \
  "ds_read_b64_tr_b16 v[166:167], v179 offset:11264\n\t" \
  "buffer_load_dwordx4 v187, %0, s40 offen offset:2048 lds\n\t" \
  "v_exp_f32 v13, v13\n\t" \
  "v_exp_f32 v14, v14\n\t" \
  "v_mfma_f32_32x32x16_bf16 v[96:111], v[136:139], a[60:63], v[96:111]\n\t" \
  "ds_read_b64_tr_b16 v[168:169], v178 offset:34816\n\t" \
  "ds_read_b64_tr_b16 v[170:171], v178 offset:35840\n\t" \
  "buffer_load_dwordx4 v188, %0, s40 offen offset:3072 lds\n\t" \
  "v_exp_f32 v15, v15\n\t" \
  "v_mfma_f32_32x32x16_bf16 v[112:127], v[140:143], a[60:63], v[112:127]\n\t" \
  "ds_read_b64_tr_b16 v[172:173], v179 offset:34816\n\t" \
  "ds_read_b64_tr_b16 v[174:175], v179 offset:35840\n\t" \
  "v_fma_f32 v12, v12, v197, v197\n\t" \
  "v_fma_f32 v13, v13, v197, v197\n\t" \
  "s_waitcnt lgkmcnt(8)\n\t" \
  "v_mfma_f32_32x32x16_bf16 v[64:79], v[144:147], a[64:67], v[64:79]\n\t" \
  "ds_read_b64_tr_b16 v[128:129], v178 offset:12288\n\t" \
  "ds_read_b64_tr_b16 v[130:131], v178 offset:13312\n\t" \
  "v_fma_f32 v14, v14, v197, v197\n\t" \
  "v_mfma_f32_32x32x16_bf16 v[80:95], v[148:151], a[64:67], v[80:95]\n\t" \
  "ds_read_b64_tr_b16 v[132:133], v179 offset:12288\n\t" \
  "ds_read_b64_tr_b16 v[134:135], v179 offset:13312\n\t" \
  "s_add_u32 s40, %6, 589824\n\t" \
  "s_add_u32 m0, %14, 8192\n\t" \
  "v_fma_f32 v15, v15, v197, v197\n\t" \
  "v_rcp_f32 v12, v12\n\t" \
  "v_mfma_f32_32x32x16_bf16 v[96:111], v[152:155], a[64:67], v[96:111]\n\t" \
  "ds_read_b64_tr_b16 v[136:137], v178 offset:36864\n\t" \
  "ds_read_b64_tr_b16 v[138:139], v178 offset:37888\n\t" \
  "buffer_load_dwordx4 v185, %0, s40 offen lds\n\t" \
  "v_rcp_f32 v13, v13\n\t" \
  "v_rcp_f32 v14, v14\n\t" \
  "v_mfma_f32_32x32x16_bf16 v[112:127], v[156:159], a[64:67], v[112:127]\n\t" \
  "ds_read_b64_tr_b16 v[140:141], v179 offset:36864\n\t" \
  "ds_read_b64_tr_b16 v[142:143], v179 offset:37888\n\t" \
  "v_rcp_f32 v15, v15\n\t" \
  "s_waitcnt lgkmcnt(8)\n\t" \
  "v_mfma_f32_32x32x16_bf16 v[64:79], v[160:163], a[68:71], v[64:79]\n\t" \
  "ds_read_b64_tr_b16 v[144:145], v178 offset:14336\n\t" \
  "ds_read_b64_tr_b16 v[146:147], v178 offset:15360\n\t" \
  "v_mul_f32 v44, v44, v12\n\t" \
  "v_mul_f32 v45, v45, v13\n\t" \
  "v_mfma_f32_32x32x16_bf16 v[80:95], v[164:167], a[68:71], v[80:95]\n\t" \
  "ds_read_b64_tr_b16 v[148:149], v179 offset:14336\n\t" \
  "ds_read_b64_tr_b16 v[150:151], v179 offset:15360\n\t" \
  "v_mul_f32 v46, v46, v14\n\t" \
  "v_mfma_f32_32x32x16_bf16 v[96:111], v[168:171], a[68:71], v[96:111]\n\t" \
  "ds_read_b64_tr_b16 v[152:153], v178 offset:38912\n\t" \
  "ds_read_b64_tr_b16 v[154:155], v178 offset:39936\n\t" \
  "buffer_load_dwordx4 v186, %0, s40 offen offset:1024 lds\n\t" \
  "v_mul_f32 v47, v47, v15\n\t" \
  "v_cvt_pk_bf16_f32 v12, v44, v45\n\t" \
  "v_mfma_f32_32x32x16_bf16 v[112:127], v[172:175], a[68:71], v[112:127]\n\t" \
  "ds_read_b64_tr_b16 v[156:157], v179 offset:38912\n\t" \
  "ds_read_b64_tr_b16 v[158:159], v179 offset:39936\n\t" \
  "v_cvt_pk_bf16_f32 v13, v46, v47\n\t" \
  "v_accvgpr_write_b32 a150, v12\n\t" \
  "s_waitcnt lgkmcnt(8)\n\t" \
  "v_mfma_f32_32x32x16_bf16 v[64:79], v[128:131], a[72:75], v[64:79]\n\t" \
  "ds_read_b64_tr_b16 v[160:161], v178 offset:16384\n\t" \
  "ds_read_b64_tr_b16 v[162:163], v178 offset:17408\n\t" \
  "v_accvgpr_write_b32 a151, v13\n\t" \
  "v_mfma_f32_32x32x16_bf16 v[80:95], v[132:135], a[72:75], v[80:95]\n\t" \
  "ds_read_b64_tr_b16 v[164:165], v179 offset:16384\n\t" \
  "ds_read_b64_tr_b16 v[166:167], v179 offset:17408\n\t" \
  "buffer_load_dwordx4 v187, %0, s40 offen offset:2048 lds\n\t" \
  "v_mul_f32 v48, v16, v48\n\t" \
  "v_mul_f32 v49, v17, v49\n\t" \
  "v_mfma_f32_32x32x16_bf16 v[96:111], v[136:139], a[72:75], v[96:111]\n\t" \
  "ds_read_b64_tr_b16 v[168:169], v178 offset:40960\n\t" \
  "ds_read_b64_tr_b16 v[170:171], v178 offset:41984\n\t" \
  "v_mul_f32 v50, v18, v50\n\t" \
  "v_mfma_f32_32x32x16_bf16 v[112:127], v[140:143], a[72:75], v[112:127]\n\t" \
  "ds_read_b64_tr_b16 v[172:173], v179 offset:40960\n\t" \
  "ds_read_b64_tr_b16 v[174:175], v179 offset:41984\n\t" \
  "v_mul_f32 v51, v19, v51\n\t" \
  "v_mul_f32 v16, v196, v16\n\t" \
  "s_waitcnt lgkmcnt(8)\n\t" \
  "v_mfma_f32_32x32x16_bf16 v[64:79], v[144:147], a[76:79], v[64:79]\n\t" \
  "ds_read_b64_tr_b16 v[128:129], v178 offset:18432\n\t" \
  "ds_read_b64_tr_b16 v[130:131], v178 offset:19456\n\t" \
  "v_mul_f32 v17, v196, v17\n\t" \
  "v_mul_f32 v18, v196, v18\n\t" \
  "v_mfma_f32_32x32x16_bf16 v[80:95], v[148:151], a[76:79], v[80:95]\n\t" \
  "ds_read_b64_tr_b16 v[132:133], v179 offset:18432\n\t" \
  "ds_read_b64_tr_b16 v[134:135], v179 offset:19456\n\t" \
  "buffer_load_dwordx4 v188, %0, s40 offen offset:3072 lds\n\t" \
  "v_mul_f32 v19, v196, v19\n\t" \
  "v_mfma_f32_32x32x16_bf16 v[96:111], v[152:155], a[76:79], v[96:111]\n\t" \
  "ds_read_b64_tr_b16 v[136:137], v178 offset:43008\n\t" \
  "ds_read_b64_tr_b16 v[138:139], v178 offset:44032\n\t" \
  "v_exp_f32 v16, v16\n\t" \
  "v_exp_f32 v17, v17\n\t" \
  "v_mfma_f32_32x32x16_bf16 v[112:127], v[156:159], a[76:79], v[112:127]\n\t" \
  "ds_read_b64_tr_b16 v[140:141], v179 offset:43008\n\t" \
  "ds_read_b64_tr_b16 v[142:143], v179 offset:44032\n\t" \
  "v_exp_f32 v18, v18\n\t" \
  "s_waitcnt lgkmcnt(8)\n\t" \
  "v_mfma_f32_32x32x16_bf16 v[64:79], v[160:163], a[80:83], v[64:79]\n\t" \
  "ds_read_b64_tr_b16 v[144:145], v178 offset:20480\n\t" \
  "ds_read_b64_tr_b16 v[146:147], v178 offset:21504\n\t" \
  "v_exp_f32 v19, v19\n\t" \
  "v_fma_f32 v16, v16, v197, v197\n\t" \
  "v_mfma_f32_32x32x16_bf16 v[80:95], v[164:167], a[80:83], v[80:95]\n\t" \
  "ds_read_b64_tr_b16 v[148:149], v179 offset:20480\n\t" \
  "ds_read_b64_tr_b16 v[150:151], v179 offset:21504\n\t" \
  "v_fma_f32 v17, v17, v197, v197\n\t" \
  "v_fma_f32 v18, v18, v197, v197\n\t" \
  "v_mfma_f32_32x32x16_bf16 v[96:111], v[168:171], a[80:83], v[96:111]\n\t" \
  "ds_read_b64_tr_b16 v[152:153], v178 offset:45056\n\t" \
  "ds_read_b64_tr_b16 v[154:155], v178 offset:46080\n\t" \
  "v_fma_f32 v19, v19, v197, v197\n\t" \
  "v_mfma_f32_32x32x16_bf16 v[112:127], v[172:175], a[80:83], v[112:127]\n\t" \
  "ds_read_b64_tr_b16 v[156:157], v179 offset:45056\n\t" \
  "ds_read_b64_tr_b16 v[158:159], v179 offset:46080\n\t" \
  "v_rcp_f32 v16, v16\n\t" \
  "v_rcp_f32 v17, v17\n\t" \
  "s_waitcnt lgkmcnt(8)\n\t" \
  "v_mfma_f32_32x32x16_bf16 v[64:79], v[128:131], a[84:87], v[64:79]\n\t" \
  "ds_read_b64_tr_b16 v[160:161], v178 offset:22528\n\t" \
  "ds_read_b64_tr_b16 v[162:163], v178 offset:23552\n\t" \
  "ds_read_b64_tr_b16 v[164:165], v179 offset:22528\n\t" \
  "ds_read_b64_tr_b16 v[166:167], v179 offset:23552\n\t" \
  "v_rcp_f32 v18, v18\n\t" \
  "v_mfma_f32_32x32x16_bf16 v[80:95], v[132:135], a[84:87], v[80:95]\n\t" \
  "ds_read_b64_tr_b16 v[168:169], v178 offset:47104\n\t" \
  "ds_read_b64_tr_b16 v[170:171], v178 offset:48128\n\t" \
  "ds_read_b64_tr_b16 v[172:173], v179 offset:47104\n\t" \
  "ds_read_b64_tr_b16 v[174:175], v179 offset:48128\n\t" \
  "v_rcp_f32 v19, v19\n\t" \
  "v_mul_f32 v48, v48, v16\n\t" \
  "v_mfma_f32_32x32x16_bf16 v[96:111], v[136:139], a[84:87], v[96:111]\n\t" \
  "v_mul_f32 v49, v49, v17\n\t" \
  "v_mfma_f32_32x32x16_bf16 v[112:127], v[140:143], a[84:87], v[112:127]\n\t" \
  "v_mul_f32 v50, v50, v18\n\t" \
  "v_mul_f32 v51, v51, v19\n\t" \
  "s_waitcnt lgkmcnt(8)\n\t" \
  "v_mfma_f32_32x32x16_bf16 v[64:79], v[144:147], a[88:91], v[64:79]\n\t" \
  "s_waitcnt vmcnt(12) lgkmcnt(0)\n\ts_barrier\n\t" \
  "ds_read_b64_tr_b16 v[128:129], v180 offset:0\n\t" \
  "ds_read_b64_tr_b16 v[130:131], v180 offset:1024\n\t" \
  "v_cvt_pk_bf16_f32 v16, v48, v49\n\t" \
  "v_cvt_pk_bf16_f32 v17, v50, v51\n\t" \
  "v_mfma_f32_32x32x16_bf16 v[80:95], v[148:151], a[88:91], v[80:95]\n\t" \
  "ds_read_b64_tr_b16 v[132:133], v181 offset:0\n\t" \
  "ds_read_b64_tr_b16 v[134:135], v181 offset:1024\n\t" \
  "s_add_u32 s40, %6, 1769472\n\t" \
  "s_add_u32 m0, %14, 49152\n\t" \
  "s_nop 0\n\t" \
  "buffer_load_dwordx4 v185, %0, s40 offen lds\n\t" \
  "v_accvgpr_write_b32 a152, v16\n\t" \
  "v_mfma_f32_32x32x16_bf16 v[96:111], v[152:155], a[88:91], v[96:111]\n\t" \
  "ds_read_b64_tr_b16 v[136:137], v180 offset:24576\n\t" \
  "ds_read_b64_tr_b16 v[138:139], v180 offset:25600\n\t" \
  "buffer_load_dwordx4 v186, %0, s40 offen offset:1024 lds\n\t" \
  "v_accvgpr_write_b32 a153, v17\n\t" \
  "v_mul_f32 v52, v20, v52\n\t" \
  "v_mfma_f32_32x32x16_bf16 v[112:127], v[156:159], a[88:91], v[112:127]\n\t" \
  "ds_read_b64_tr_b16 v[140:141], v181 offset:24576\n\t" \
  "ds_read_b64_tr_b16 v[142:143], v181 offset:25600\n\t" \
  "v_mul_f32 v53, v21, v53\n\t" \
  "s_waitcnt lgkmcnt(8)\n\t" \
  "v_mfma_f32_32x32x16_bf16 v[64:79], v[160:163], a[92:95], v[64:79]\n\t" \
  "ds_read_b64_tr_b16 v[144:145], v180 offset:2048\n\t" \
  "ds_read_b64_tr_b16 v[146:147], v180 offset:3072\n\t" \
  "v_mul_f32 v54, v22, v54\n\t" \
  "v_mul_f32 v55, v23, v55\n\t" \
  "v_mfma_f32_32x32x16_bf16 v[80:95], v[164:167], a[92:95], v[80:95]\n\t" \
  "ds_read_b64_tr_b16 v[148:149], v181 offset:2048\n\t" \
  "ds_read_b64_tr_b16 v[150:151], v181 offset:3072\n\t" \
  "v_mul_f32 v20, v196, v20\n\t" \
  "v_mul_f32 v21, v196, v21\n\t" \
  "v_mfma_f32_32x32x16_bf16 v[96:111], v[168:171], a[92:95], v[96:111]\n\t" \
  "ds_read_b64_tr_b16 v[152:153], v180 offset:26624\n\t" \
  "ds_read_b64_tr_b16 v[154:155], v180 offset:27648\n\t" \
  "buffer_load_dwordx4 v187, %0, s40 offen offset:2048 lds\n\t" \
  "v_mul_f32 v22, v196, v22\n\t" \
  "v_mfma_f32_32x32x16_bf16 v[112:127], v[172:175], a[92:95], v[112:127]\n\t" \
  "ds_read_b64_tr_b16 v[156:157], v181 offset:26624\n\t" \
  "ds_read_b64_tr_b16 v[158:159], v181 offset:27648\n\t" \
  "v_mul_f32 v23, v196, v23\n\t" \
  "v_exp_f32 v20, v20\n\t" \
  "s_waitcnt lgkmcnt(8)\n\t" \
  "v_mfma_f32_32x32x16_bf16 v[64:79], v[128:131], a[96:99], v[64:79]\n\t" \
  "ds_read_b64_tr_b16 v[160:161], v180 offset:4096\n\t" \
  "ds_read_b64_tr_b16 v[162:163], v180 offset:5120\n\t" \
  "v_exp_f32 v21, v21\n\t" \
  "v_mfma_f32_32x32x16_bf16 v[80:95], v[132:135], a[96:99], v[80:95]\n\t" \
  "ds_read_b64_tr_b16 v[164:165], v181 offset:4096\n\t" \
  "ds_read_b64_tr_b16 v[166:167], v181 offset:5120\n\t" \
  "v_exp_f32 v22, v22\n\t" \
  "v_exp_f32 v23, v23\n\t" \
  "v_mfma_f32_32x32x16_bf16 v[96:111], v[136:139], a[96:99], v[96:111]\n\t" \
  "ds_read_b64_tr_b16 v[168:169], v180 offset:28672\n\t" \
  "ds_read_b64_tr_b16 v[170:171], v180 offset:29696\n\t" \
  "buffer_load_dwordx4 v188, %0, s40 offen offset:3072 lds\n\t" \
  "v_fma_f32 v20, v20, v197, v197\n\t" \
  "v_fma_f32 v21, v21, v197, v197\n\t" \
  "v_mfma_f32_32x32x16_bf16 v[112:127], v[140:143], a[96:99], v[112:127]\n\t" \
  "ds_read_b64_tr_b16 v[172:173], v181 offset:28672\n\t" \
  "ds_read_b64_tr_b16 v[174:175], v181 offset:29696\n\t" \
  "v_fma_f32 v22, v22, v197, v197\n\t" \
  "s_waitcnt lgkmcnt(8)\n\t" \
  "v_mfma_f32_32x32x16_bf16 v[64:79], v[144:147], a[100:103], v[64:79]\n\t" \
  "ds_read_b64_tr_b16 v[128:129], v180 offset:6144\n\t" \
  "ds_read_b64_tr_b16 v[130:131], v180 offset:7168\n\t" \
  "s_add_u32 s40, %6, 2064384\n\t" \
  "s_add_u32 m0, %14, 53248\n\t" \
  "v_fma_f32 v23, v23, v197, v197\n\t" \
  "v_rcp_f32 v20, v20\n\t" \
  "v_mfma_f32_32x32x16_bf16 v[80:95], v[148:151], a[100:103], v[80:95]\n\t" \
  "ds_read_b64_tr_b16 v[132:133], v181 offset:6144\n\t" \
  "ds_read_b64_tr_b16 v[134:135], v181 offset:7168\n\t" \
  "buffer_load_dwordx4 v185, %0, s40 offen lds\n\t" \
  "v_rcp_f32 v21, v21\n\t" \
  "v_mfma_f32_32x32x16_bf16 v[96:111], v[152:155], a[100:103], v[96:111]\n\t" \
  "ds_read_b64_tr_b16 v[136:137], v180 offset:30720\n\t" \
  "ds_read_b64_tr_b16 v[138:139], v180 offset:31744\n\t" \
  "v_rcp_f32 v22, v22\n\t" \
  "v_rcp_f32 v23, v23\n\t" \
  "v_mfma_f32_32x32x16_bf16 v[112:127], v[156:159], a[100:103], v[112:127]\n\t" \
  "ds_read_b64_tr_b16 v[140:141], v181 offset:30720\n\t" \
  "ds_read_b64_tr_b16 v[142:143], v181 offset:31744\n\t" \
  "v_mul_f32 v52, v52, v20\n\t" \
  "s_waitcnt lgkmcnt(8)\n\t" \
  "v_mfma_f32_32x32x16_bf16 v[64:79], v[160:163], a[104:107], v[64:79]\n\t" \
  "ds_read_b64_tr_b16 v[144:145], v180 offset:8192\n\t" \
  "ds_read_b64_tr_b16 v[146:147], v180 offset:9216\n\t" \
  "v_mul_f32 v53, v53, v21\n\t" \
  "v_mul_f32 v54, v54, v22\n\t" \
  "v_mfma_f32_32x32x16_bf16 v[80:95], v[164:167], a[104:107], v[80:95]\n\t" \
  "ds_read_b64_tr_b16 v[148:149], v181 offset:8192\n\t" \
  "ds_read_b64_tr_b16 v[150:151], v181 offset:9216\n\t" \
  "buffer_load_dwordx4 v186, %0, s40 offen offset:1024 lds\n\t" \
  "v_mul_f32 v55, v55, v23\n\t" \
  "v_cvt_pk_bf16_f32 v20, v52, v53\n\t" \
  "v_mfma_f32_32x32x16_bf16 v[96:111], v[168:171], a[104:107], v[96:111]\n\t" \
  "ds_read_b64_tr_b16 v[152:153], v180 offset:32768\n\t" \
  "ds_read_b64_tr_b16 v[154:155], v180 offset:33792\n\t" \
  "v_cvt_pk_bf16_f32 v21, v54, v55\n\t" \
  "v_mfma_f32_32x32x16_bf16 v[112:127], v[172:175], a[104:107], v[112:127]\n\t" \
  "ds_read_b64_tr_b16 v[156:157], v181 offset:32768\n\t" \
  "ds_read_b64_tr_b16 v[158:159], v181 offset:33792\n\t" \
  "v_accvgpr_write_b32 a154, v20\n\t" \
  "v_accvgpr_write_b32 a155, v21\n\t" \
  "s_waitcnt lgkmcnt(8)\n\t" \
  "v_mfma_f32_32x32x16_bf16 v[64:79], v[128:131], a[108:111], v[64:79]\n\t" \
  "ds_read_b64_tr_b16 v[160:161], v180 offset:10240\n\t" \
  "ds_read_b64_tr_b16 v[162:163], v180 offset:11264\n\t" \
  "v_mul_f32 v56, v24, v56\n\t" \
  "v_mfma_f32_32x32x16_bf16 v[80:95], v[132:135], a[108:111], v[80:95]\n\t" \
  "ds_read_b64_tr_b16 v[164:165], v181 offset:10240\n\t" \
  "ds_read_b64_tr_b16 v[166:167], v181 offset:11264\n\t" \
  "buffer_load_dwordx4 v187, %0, s40 offen offset:2048 lds\n\t" \
  "v_mul_f32 v57, v25, v57\n\t" \
  "v_mul_f32 v58, v26, v58\n\t" \
  "v_mfma_f32_32x32x16_bf16 v[96:111], v[136:139], a[108:111], v[96:111]\n\t" \
  "ds_read_b64_tr_b16 v[168:169], v180 offset:34816\n\t" \
  "ds_read_b64_tr_b16 v[170:171], v180 offset:35840\n\t" \
  "buffer_load_dwordx4 v188, %0, s40 offen offset:3072 lds\n\t" \
  "v_mul_f32 v59, v27, v59\n\t" \
  "v_mul_f32 v24, v196, v24\n\t" \
  "v_mfma_f32_32x32x16_bf16 v[112:127], v[140:143], a[108:111], v[112:127]\n\t" \
  "ds_read_b64_tr_b16 v[172:173], v181 offset:34816\n\t" \
  "ds_read_b64_tr_b16 v[174:175], v181 offset:35840\n\t" \
  "v_mul_f32 v25, v196, v25\n\t" \
  "s_waitcnt lgkmcnt(8)\n\t" \
  "v_mfma_f32_32x32x16_bf16 v[64:79], v[144:147], a[112:115], v[64:79]\n\t" \
  "ds_read_b64_tr_b16 v[128:129], v180 offset:12288\n\t" \
  "ds_read_b64_tr_b16 v[130:131], v180 offset:13312\n\t" \
  "v_mul_f32 v26, v196, v26\n\t" \
  "v_mul_f32 v27, v196, v27\n\t" \
  "v_mfma_f32_32x32x16_bf16 v[80:95], v[148:151], a[112:115], v[80:95]\n\t" \
  "ds_read_b64_tr_b16 v[132:133], v181 offset:12288\n\t" \
  "ds_read_b64_tr_b16 v[134:135], v181 offset:13312\n\t" \
  "s_add_u32 s40, %6, 2359296\n\t" \
  "s_add_u32 m0, %14, 57344\n\t" \
  "v_exp_f32 v24, v24\n\t" \
  "v_mfma_f32_32x32x16_bf16 v[96:111], v[152:155], a[112:115], v[96:111]\n\t" \
  "ds_read_b64_tr_b16 v[136:137], v180 offset:36864\n\t" \
  "ds_read_b64_tr_b16 v[138:139], v180 offset:37888\n\t" \
  "buffer_load_dwordx4 v185, %0, s40 offen lds\n\t" \
  "v_exp_f32 v25, v25\n\t" \
  "v_exp_f32 v26, v26\n\t" \
  "v_mfma_f32_32x32x16_bf16 v[112:127], v[156:159], a[112:115], v[112:127]\n\t" \
  "ds_read_b64_tr_b16 v[140:141], v181 offset:36864\n\t" \
  "ds_read_b64_tr_b16 v[142:143], v181 offset:37888\n\t" \
  "v_exp_f32 v27, v27\n\t" \
  "v_fma_f32 v24, v24, v197, v197\n\t" \
  "s_waitcnt lgkmcnt(8)\n\t" \
  "v_mfma_f32_32x32x16_bf16 v[64:79], v[160:163], a[116:119], v[64:79]\n\t" \
  "ds_read_b64_tr_b16 v[144:145], v180 offset:14336\n\t" \
  "ds_read_b64_tr_b16 v[146:147], v180 offset:15360\n\t" \
  "v_fma_f32 v25, v25, v197, v197\n\t" \
  "v_mfma_f32_32x32x16_bf16 v[80:95], v[164:167], a[116:119], v[80:95]\n\t" \
  "ds_read_b64_tr_b16 v[148:149], v181 offset:14336\n\t" \
  "ds_read_b64_tr_b16 v[150:151], v181 offset:15360\n\t" \
  "v_fma_f32 v26, v26, v197, v197\n\t" \
  "v_fma_f32 v27, v27, v197, v197\n\t" \
  "v_mfma_f32_32x32x16_bf16 v[96:111], v[168:171], a[116:119], v[96:111]\n\t" \
  "ds_read_b64_tr_b16 v[152:153], v180 offset:38912\n\t" \
  "ds_read_b64_tr_b16 v[154:155], v180 offset:39936\n\t" \
  "buffer_load_dwordx4 v186, %0, s40 offen offset:1024 lds\n\t" \
  "v_rcp_f32 v24, v24\n\t" \
  "v_mfma_f32_32x32x16_bf16 v[112:127], v[172:175], a[116:119], v[112:127]\n\t" \
  "ds_read_b64_tr_b16 v[156:157], v181 offset:38912\n\t" \
  "ds_read_b64_tr_b16 v[158:159], v181 offset:39936\n\t" \
  "v_rcp_f32 v25, v25\n\t" \
  "v_rcp_f32 v26, v26\n\t" \
  "s_waitcnt lgkmcnt(8)\n\t" \
  "v_mfma_f32_32x32x16_bf16 v[64:79], v[128:131], a[120:123], v[64:79]\n\t" \
  "ds_read_b64_tr_b16 v[160:161], v180 offset:16384\n\t" \
  "ds_read_b64_tr_b16 v[162:163], v180 offset:17408\n\t" \
  "v_rcp_f32 v27, v27\n\t" \
  "v_mfma_f32_32x32x16_bf16 v[80:95], v[132:135], a[120:123], v[80:95]\n\t" \
  "ds_read_b64_tr_b16 v[164:165], v181 offset:16384\n\t" \
  "ds_read_b64_tr_b16 v[166:167], v181 offset:17408\n\t" \
  "buffer_load_dwordx4 v187, %0, s40 offen offset:2048 lds\n\t" \
  "v_mul_f32 v56, v56, v24\n\t" \
  "v_mul_f32 v57, v57, v25\n\t" \
  "v_mfma_f32_32x32x16_bf16 v[96:111], v[136:139], a[120:123], v[96:111]\n\t" \
  "ds_read_b64_tr_b16 v[168:169], v180 offset:40960\n\t" \
  "ds_read_b64_tr_b16 v[170:171], v180 offset:41984\n\t" \
  "v_mul_f32 v58, v58, v26\n\t" \
  "v_mul_f32 v59, v59, v27\n\t" \
  "v_mfma_f32_32x32x16_bf16 v[112:127], v[140:143], a[120:123], v[112:127]\n\t" \
  "ds_read_b64_tr_b16 v[172:173], v181 offset:40960\n\t" \
  "ds_read_b64_tr_b16 v[174:175], v181 offset:41984\n\t" \
  "v_cvt_pk_bf16_f32 v24, v56, v57\n\t" \
  "s_waitcnt lgkmcnt(8)\n\t" \
  "v_mfma_f32_32x32x16_bf16 v[64:79], v[144:147], a[124:127], v[64:79]\n\t" \
  "ds_read_b64_tr_b16 v[128:129], v180 offset:18432\n\t" \
  "ds_read_b64_tr_b16 v[130:131], v180 offset:19456\n\t" \
  "v_cvt_pk_bf16_f32 v25, v58, v59\n\t" \
  "v_accvgpr_write_b32 a156, v24\n\t" \
  "v_mfma_f32_32x32x16_bf16 v[80:95], v[148:151], a[124:127], v[80:95]\n\t" \
  "ds_read_b64_tr_b16 v[132:133], v181 offset:18432\n\t" \
  "ds_read_b64_tr_b16 v[134:135], v181 offset:19456\n\t" \
  "buffer_load_dwordx4 v188, %0, s40 offen offset:3072 lds\n\t" \
  "v_accvgpr_write_b32 a157, v25\n\t" \
  "v_mfma_f32_32x32x16_bf16 v[96:111], v[152:155], a[124:127], v[96:111]\n\t" \
  "ds_read_b64_tr_b16 v[136:137], v180 offset:43008\n\t" \
  "ds_read_b64_tr_b16 v[138:139], v180 offset:44032\n\t" \
  "v_mul_f32 v60, v28, v60\n\t" \
  "v_mul_f32 v61, v29, v61\n\t" \
  "v_mfma_f32_32x32x16_bf16 v[112:127], v[156:159], a[124:127], v[112:127]\n\t" \
  "ds_read_b64_tr_b16 v[140:141], v181 offset:43008\n\t" \
  "ds_read_b64_tr_b16 v[142:143], v181 offset:44032\n\t" \
  "v_mul_f32 v62, v30, v62\n\t" \
  "v_mul_f32 v63, v31, v63\n\t" \
  "s_waitcnt lgkmcnt(8)\n\t" \
  "v_mfma_f32_32x32x16_bf16 v[64:79], v[160:163], a[128:131], v[64:79]\n\t" \
  "ds_read_b64_tr_b16 v[144:145], v180 offset:20480\n\t" \
  "ds_read_b64_tr_b16 v[146:147], v180 offset:21504\n\t" \
  "v_mul_f32 v28, v196, v28\n\t" \
  "v_mfma_f32_32x32x16_bf16 v[80:95], v[164:167], a[128:131], v[80:95]\n\t" \
  "ds_read_b64_tr_b16 v[148:149], v181 offset:20480\n\t" \
  "ds_read_b64_tr_b16 v[150:151], v181 offset:21504\n\t" \
  "v_mul_f32 v29, v196, v29\n\t" \
  "v_mul_f32 v30, v196, v30\n\t" \
  "v_mfma_f32_32x32x16_bf16 v[96:111], v[168:171], a[128:131], v[96:111]\n\t" \
  "ds_read_b64_tr_b16 v[152:153], v180 offset:45056\n\t" \
  "ds_read_b64_tr_b16 v[154:155], v180 offset:46080\n\t" \
  "v_mul_f32 v31, v196, v31\n\t" \
  "v_mfma_f32_32x32x16_bf16 v[112:127], v[172:175], a[128:131], v[112:127]\n\t" \
  "ds_read_b64_tr_b16 v[156:157], v181 offset:45056\n\t" \
  "ds_read_b64_tr_b16 v[158:159], v181 offset:46080\n\t" \
  "v_exp_f32 v28, v28\n\t" \
  "v_exp_f32 v29, v29\n\t" \
  "s_waitcnt lgkmcnt(8)\n\t" \
  "v_mfma_f32_32x32x16_bf16 v[64:79], v[128:131], a[132:135], v[64:79]\n\t" \
  "ds_read_b64_tr_b16 v[160:161], v180 offset:22528\n\t" \
  "ds_read_b64_tr_b16 v[162:163], v180 offset:23552\n\t" \
  "ds_read_b64_tr_b16 v[164:165], v181 offset:22528\n\t" \
  "ds_read_b64_tr_b16 v[166:167], v181 offset:23552\n\t" \
  "v_exp_f32 v30, v30\n\t" \
  "v_exp_f32 v31, v31\n\t" \
  "v_mfma_f32_32x32x16_bf16 v[80:95], v[132:135], a[132:135], v[80:95]\n\t" \
  "ds_read_b64_tr_b16 v[168:169], v180 offset:47104\n\t" \
  "ds_read_b64_tr_b16 v[170:171], v180 offset:48128\n\t" \
  "ds_read_b64_tr_b16 v[172:173], v181 offset:47104\n\t" \
  "ds_read_b64_tr_b16 v[174:175], v181 offset:48128\n\t" \
  "v_fma_f32 v28, v28, v197, v197\n\t" \
  "v_mfma_f32_32x32x16_bf16 v[96:111], v[136:139], a[132:135], v[96:111]\n\t" \
  "v_fma_f32 v29, v29, v197, v197\n\t" \
  "v_fma_f32 v30, v30, v197, v197\n\t" \
  "v_mfma_f32_32x32x16_bf16 v[112:127], v[140:143], a[132:135], v[112:127]\n\t" \
  "v_fma_f32 v31, v31, v197, v197\n\t" \
  "s_waitcnt lgkmcnt(8)\n\t" \
  "v_mfma_f32_32x32x16_bf16 v[64:79], v[144:147], a[136:139], v[64:79]\n\t" \
  "s_waitcnt vmcnt(12) lgkmcnt(0)\n\ts_barrier\n\t" \
  "ds_read_b64_tr_b16 v[128:129], v176 offset:0\n\t" \
  "ds_read_b64_tr_b16 v[130:131], v176 offset:1024\n\t" \
  "v_rcp_f32 v28, v28\n\t" \
  "v_rcp_f32 v29, v29\n\t" \
  "v_mfma_f32_32x32x16_bf16 v[80:95], v[148:151], a[136:139], v[80:95]\n\t" \
  "ds_read_b64_tr_b16 v[132:133], v177 offset:0\n\t" \
  "ds_read_b64_tr_b16 v[134:135], v177 offset:1024\n\t" \
  "s_add_u32 s40, %6, 3538944\n\t" \
  "s_add_u32 m0, %14, 98304\n\t" \
  "s_nop 0\n\t" \
  "buffer_load_dwordx4 v185, %0, s40 offen lds\n\t" \
  "v_rcp_f32 v30, v30\n\t" \
  "v_mfma_f32_32x32x16_bf16 v[96:111], v[152:155], a[136:139], v[96:111]\n\t" \
  "ds_read_b64_tr_b16 v[136:137], v176 offset:24576\n\t" \
  "ds_read_b64_tr_b16 v[138:139], v176 offset:25600\n\t" \
  "buffer_load_dwordx4 v186, %0, s40 offen offset:1024 lds\n\t" \
  "v_rcp_f32 v31, v31\n\t" \
  "v_mul_f32 v60, v60, v28\n\t" \
  "v_mfma_f32_32x32x16_bf16 v[112:127], v[156:159], a[136:139], v[112:127]\n\t" \
  "ds_read_b64_tr_b16 v[140:141], v177 offset:24576\n\t" \
  "ds_read_b64_tr_b16 v[142:143], v177 offset:25600\n\t" \
  "v_mul_f32 v61, v61, v29\n\t" \
  "v_mul_f32 v62, v62, v30\n\t" \
  "s_waitcnt lgkmcnt(8)\n\t" \
  "v_mfma_f32_32x32x16_bf16 v[64:79], v[160:163], a[140:143], v[64:79]\n\t" \
  "ds_read_b64_tr_b16 v[144:145], v176 offset:2048\n\t" \
  "ds_read_b64_tr_b16 v[146:147], v176 offset:3072\n\t" \
  "v_mul_f32 v63, v63, v31\n\t" \
  "v_mfma_f32_32x32x16_bf16 v[80:95], v[164:167], a[140:143], v[80:95]\n\t" \
  "ds_read_b64_tr_b16 v[148:149], v177 offset:2048\n\t" \
  "ds_read_b64_tr_b16 v[150:151], v177 offset:3072\n\t" \
  "v_cvt_pk_bf16_f32 v28, v60, v61\n\t" \
  "v_cvt_pk_bf16_f32 v29, v62, v63\n\t" \
  "v_mfma_f32_32x32x16_bf16 v[96:111], v[168:171], a[140:143], v[96:111]\n\t" \
  "ds_read_b64_tr_b16 v[152:153], v176 offset:26624\n\t" \
  "ds_read_b64_tr_b16 v[154:155], v176 offset:27648\n\t" \
  "buffer_load_dwordx4 v187, %0, s40 offen offset:2048 lds\n\t" \
  "v_accvgpr_write_b32 a158, v28\n\t" \
  "v_mfma_f32_32x32x16_bf16 v[112:127], v[172:175], a[140:143], v[112:127]\n\t" \
  "ds_read_b64_tr_b16 v[156:157], v177 offset:26624\n\t" \
  "ds_read_b64_tr_b16 v[158:159], v177 offset:27648\n\t" \
  "v_accvgpr_write_b32 a159, v29\n\t" \
  "s_waitcnt lgkmcnt(8)\n\t" \
  "v_mfma_f32_32x32x16_bf16 v[0:15], v[128:131], a[0:3], 0\n\t" \
  "ds_read_b64_tr_b16 v[160:161], v176 offset:4096\n\t" \
  "ds_read_b64_tr_b16 v[162:163], v176 offset:5120\n\t" \
  "v_mfma_f32_32x32x16_bf16 v[16:31], v[132:135], a[0:3], 0\n\t" \
  "ds_read_b64_tr_b16 v[164:165], v177 offset:4096\n\t" \
  "ds_read_b64_tr_b16 v[166:167], v177 offset:5120\n\t" \
  "v_mfma_f32_32x32x16_bf16 v[32:47], v[136:139], a[0:3], 0\n\t" \
  "ds_read_b64_tr_b16 v[168:169], v176 offset:28672\n\t" \
  "ds_read_b64_tr_b16 v[170:171], v176 offset:29696\n\t" \
  "buffer_load_dwordx4 v188, %0, s40 offen offset:3072 lds\n\t" \
  "v_mfma_f32_32x32x16_bf16 v[48:63], v[140:143], a[0:3], 0\n\t" \
  "ds_read_b64_tr_b16 v[172:173], v177 offset:28672\n\t" \
  "ds_read_b64_tr_b16 v[174:175], v177 offset:29696\n\t" \
  "v_mul_f32 v96, v64, v96\n\t" \
  "v_mul_f32 v97, v65, v97\n\t" \
  "v_mul_f32 v98, v66, v98\n\t" \
  "s_waitcnt lgkmcnt(8)\n\t" \
  "v_mfma_f32_32x32x16_bf16 v[0:15], v[144:147], a[4:7], v[0:15]\n\t" \
  "ds_read_b64_tr_b16 v[128:129], v176 offset:6144\n\t" \
  "ds_read_b64_tr_b16 v[130:131], v176 offset:7168\n\t" \
  "s_add_u32 s40, %6, 3833856\n\t" \
  "s_add_u32 m0, %14, 102400\n\t" \
  "v_mul_f32 v99, v67, v99\n\t" \
  "v_mfma_f32_32x32x16_bf16 v[16:31], v[148:151], a[4:7], v[16:31]\n\t" \
  "ds_read_b64_tr_b16 v[132:133], v177 offset:6144\n\t" \
  "ds_read_b64_tr_b16 v[134:135], v177 offset:7168\n\t" \
  "buffer_load_dwordx4 v185, %0, s40 offen lds\n\t" \
  "v_mul_f32 v64, v196, v64\n\t" \
  "v_mul_f32 v65, v196, v65\n\t" \
  "v_mfma_f32_32x32x16_bf16 v[32:47], v[152:155], a[4:7], v[32:47]\n\t" \
  "ds_read_b64_tr_b16 v[136:137], v176 offset:30720\n\t" \
  "ds_read_b64_tr_b16 v[138:139], v176 offset:31744\n\t" \
  "v_mul_f32 v66, v196, v66\n\t" \
  "v_mfma_f32_32x32x16_bf16 v[48:63], v[156:159], a[4:7], v[48:63]\n\t" \
  "ds_read_b64_tr_b16 v[140:141], v177 offset:30720\n\t" \
  "ds_read_b64_tr_b16 v[142:143], v177 offset:31744\n\t" \
  "v_mul_f32 v67, v196, v67\n\t" \
  "v_exp_f32 v64, v64\n\t" \
  "s_waitcnt lgkmcnt(8)\n\t" \
  "v_mfma_f32_32x32x16_bf16 v[0:15], v[160:163], a[8:11], v[0:15]\n\t" \
  "ds_read_b64_tr_b16 v[144:145], v176 offset:8192\n\t" \
  "ds_read_b64_tr_b16 v[146:147], v176 offset:9216\n\t" \
  "v_exp_f32 v65, v65\n\t" \
  "v_exp_f32 v66, v66\n\t" \
  "v_mfma_f32_32x32x16_bf16 v[16:31], v[164:167], a[8:11], v[16:31]\n\t" \
  "ds_read_b64_tr_b16 v[148:149], v177 offset:8192\n\t" \
  "ds_read_b64_tr_b16 v[150:151], v177 offset:9216\n\t" \
  "buffer_load_dwordx4 v186, %0, s40 offen offset:1024 lds\n\t" \
  "v_exp_f32 v67, v67\n\t" \
  "v_mfma_f32_32x32x16_bf16 v[32:47], v[168:171], a[8:11], v[32:47]\n\t" \
  "ds_read_b64_tr_b16 v[152:153], v176 offset:32768\n\t" \
  "ds_read_b64_tr_b16 v[154:155], v176 offset:33792\n\t" \
  "v_fma_f32 v64, v64, v197, v197\n\t" \
  "v_fma_f32 v65, v65, v197, v197\n\t" \
  "v_mfma_f32_32x32x16_bf16 v[48:63], v[172:175], a[8:11], v[48:63]\n\t" \
  "ds_read_b64_tr_b16 v[156:157], v177 offset:32768\n\t" \
  "ds_read_b64_tr_b16 v[158:159], v177 offset:33792\n\t" \
  "v_fma_f32 v66, v66, v197, v197\n\t" \
  "s_waitcnt lgkmcnt(8)\n\t" \
  "v_mfma_f32_32x32x16_bf16 v[0:15], v[128:131], a[12:15], v[0:15]\n\t" \
  "ds_read_b64_tr_b16 v[160:161], v176 offset:10240\n\t" \
  "ds_read_b64_tr_b16 v[162:163], v176 offset:11264\n\t" \
  "v_fma_f32 v67, v67, v197, v197\n\t" \
  "v_rcp_f32 v64, v64\n\t" \
  "v_mfma_f32_32x32x16_bf16 v[16:31], v[132:135], a[12:15], v[16:31]\n\t" \
  "ds_read_b64_tr_b16 v[164:165], v177 offset:10240\n\t" \
  "ds_read_b64_tr_b16 v[166:167], v177 offset:11264\n\t" \
  "buffer_load_dwordx4 v187, %0, s40 offen offset:2048 lds\n\t" \
  "v_rcp_f32 v65, v65\n\t" \
  "v_mfma_f32_32x32x16_bf16 v[32:47], v[136:139], a[12:15], v[32:47]\n\t" \
  "ds_read_b64_tr_b16 v[168:169], v176 offset:34816\n\t" \
  "ds_read_b64_tr_b16 v[170:171], v176 offset:35840\n\t" \
  "buffer_load_dwordx4 v188, %0, s40 offen offset:3072 lds\n\t" \
  "v_rcp_f32 v66, v66\n\t" \
  "v_rcp_f32 v67, v67\n\t" \
  "v_mfma_f32_32x32x16_bf16 v[48:63], v[140:143], a[12:15], v[48:63]\n\t" \
  "ds_read_b64_tr_b16 v[172:173], v177 offset:34816\n\t" \
  "ds_read_b64_tr_b16 v[174:175], v177 offset:35840\n\t" \
  "v_mul_f32 v96, v96, v64\n\t" \
  "v_mul_f32 v97, v97, v65\n\t" \
  "s_waitcnt lgkmcnt(8)\n\t" \
  "v_mfma_f32_32x32x16_bf16 v[0:15], v[144:147], a[16:19], v[0:15]\n\t" \
  "ds_read_b64_tr_b16 v[128:129], v176 offset:12288\n\t" \
  "ds_read_b64_tr_b16 v[130:131], v176 offset:13312\n\t" \
  "v_mul_f32 v98, v98, v66\n\t" \
  "v_mfma_f32_32x32x16_bf16 v[16:31], v[148:151], a[16:19], v[16:31]\n\t" \
  "ds_read_b64_tr_b16 v[132:133], v177 offset:12288\n\t" \
  "ds_read_b64_tr_b16 v[134:135], v177 offset:13312\n\t" \
  "s_add_u32 s40, %6, 4128768\n\t" \
  "s_add_u32 m0, %14, 106496\n\t" \
  "v_mul_f32 v99, v99, v67\n\t" \
  "v_cvt_pk_bf16_f32 v64, v96, v97\n\t" \
  "v_mfma_f32_32x32x16_bf16 v[32:47], v[152:155], a[16:19], v[32:47]\n\t" \
  "ds_read_b64_tr_b16 v[136:137], v176 offset:36864\n\t" \
  "ds_read_b64_tr_b16 v[138:139], v176 offset:37888\n\t" \
  "buffer_load_dwordx4 v185, %0, s40 offen lds\n\t" \
  "v_cvt_pk_bf16_f32 v65, v98, v99\n\t" \
  "v_mfma_f32_32x32x16_bf16 v[48:63], v[156:159], a[16:19], v[48:63]\n\t" \
  "ds_read_b64_tr_b16 v[140:141], v177 offset:36864\n\t" \
  "ds_read_b64_tr_b16 v[142:143], v177 offset:37888\n\t" \
  "v_accvgpr_write_b32 a160, v64\n\t" \
  "v_accvgpr_write_b32 a161, v65\n\t" \
  "s_waitcnt lgkmcnt(8)\n\t" \
  "v_mfma_f32_32x32x16_bf16 v[0:15], v[160:163], a[20:23], v[0:15]\n\t" \
  "ds_read_b64_tr_b16 v[144:145], v176 offset:14336\n\t" \
  "ds_read_b64_tr_b16 v[146:147], v176 offset:15360\n\t" \
  "v_mul_f32 v100, v68, v100\n\t" \
  "v_mul_f32 v101, v69, v101\n\t" \
  "v_mfma_f32_32x32x16_bf16 v[16:31], v[164:167], a[20:23], v[16:31]\n\t" \
  "ds_read_b64_tr_b16 v[148:149], v177 offset:14336\n\t" \
  "ds_read_b64_tr_b16 v[150:151], v177 offset:15360\n\t" \
  "v_mul_f32 v102, v70, v102\n\t" \
  "v_mfma_f32_32x32x16_bf16 v[32:47], v[168:171], a[20:23], v[32:47]\n\t" \
  "ds_read_b64_tr_b16 v[152:153], v176 offset:38912\n\t" \
  "ds_read_b64_tr_b16 v[154:155], v176 offset:39936\n\t" \
  "buffer_load_dwordx4 v186, %0, s40 offen offset:1024 lds\n\t" \
  "v_mul_f32 v103, v71, v103\n\t" \
  "v_mul_f32 v68, v196, v68\n\t" \
  "v_mfma_f32_32x32x16_bf16 v[48:63], v[172:175], a[20:23], v[48:63]\n\t" \
  "ds_read_b64_tr_b16 v[156:157], v177 offset:38912\n\t" \
  "ds_read_b64_tr_b16 v[158:159], v177 offset:39936\n\t" \
  "v_mul_f32 v69, v196, v69\n\t" \
  "s_waitcnt lgkmcnt(8)\n\t" \
  "v_mfma_f32_32x32x16_bf16 v[0:15], v[128:131], a[24:27], v[0:15]\n\t" \
  "ds_read_b64_tr_b16 v[160:161], v176 offset:16384\n\t" \
  "ds_read_b64_tr_b16 v[162:163], v176 offset:17408\n\t" \
  "v_mul_f32 v70, v196, v70\n\t" \
  "v_mul_f32 v71, v196, v71\n\t" \
  "v_mfma_f32_32x32x16_bf16 v[16:31], v[132:135], a[24:27], v[16:31]\n\t" \
  "ds_read_b64_tr_b16 v[164:165], v177 offset:16384\n\t" \
  "ds_read_b64_tr_b16 v[166:167], v177 offset:17408\n\t" \
  "buffer_load_dwordx4 v187, %0, s40 offen offset:2048 lds\n\t" \
  "v_exp_f32 v68, v68\n\t" \
  "v_exp_f32 v69, v69\n\t" \
  "v_mfma_f32_32x32x16_bf16 v[32:47], v[136:139], a[24:27], v[32:47]\n\t" \
  "ds_read_b64_tr_b16 v[168:169], v176 offset:40960\n\t" \
  "ds_read_b64_tr_b16 v[170:171], v176 offset:41984\n\t" \
  "v_exp_f32 v70, v70\n\t" \
  "v_mfma_f32_32x32x16_bf16 v[48:63], v[140:143], a[24:27], v[48:63]\n\t" \
  "ds_read_b64_tr_b16 v[172:173], v177 offset:40960\n\t" \
  "ds_read_b64_tr_b16 v[174:175], v177 offset:41984\n\t" \
  "v_exp_f32 v71, v71\n\t" \
  "v_fma_f32 v68, v68, v197, v197\n\t" \
  "s_waitcnt lgkmcnt(8)\n\t" \
  "v_mfma_f32_32x32x16_bf16 v[0:15], v[144:147], a[28:31], v[0:15]\n\t" \
  "ds_read_b64_tr_b16 v[128:129], v176 offset:18432\n\t" \
  "ds_read_b64_tr_b16 v[130:131], v176 offset:19456\n\t" \
  "v_fma_f32 v69, v69, v197, v197\n\t" \
  "v_mfma_f32_32x32x16_bf16 v[16:31], v[148:151], a[28:31], v[16:31]\n\t" \
  "ds_read_b64_tr_b16 v[132:133], v177 offset:18432\n\t" \
  "ds_read_b64_tr_b16 v[134:135], v177 offset:19456\n\t" \
  "buffer_load_dwordx4 v188, %0, s40 offen offset:3072 lds\n\t" \
  "v_fma_f32 v70, v70, v197, v197\n\t" \
  "v_fma_f32 v71, v71, v197, v197\n\t" \
  "v_mfma_f32_32x32x16_bf16 v[32:47], v[152:155], a[28:31], v[32:47]\n\t" \
  "ds_read_b64_tr_b16 v[136:137], v176 offset:43008\n\t" \
  "ds_read_b64_tr_b16 v[138:139], v176 offset:44032\n\t" \
  "v_rcp_f32 v68, v68\n\t" \
  "v_mfma_f32_32x32x16_bf16 v[48:63], v[156:159], a[28:31], v[48:63]\n\t" \
  "ds_read_b64_tr_b16 v[140:141], v177 offset:43008\n\t" \
  "ds_read_b64_tr_b16 v[142:143], v177 offset:44032\n\t" \
  "v_rcp_f32 v69, v69\n\t" \
  "v_rcp_f32 v70, v70\n\t" \
  "s_waitcnt lgkmcnt(8)\n\t" \
  "v_mfma_f32_32x32x16_bf16 v[0:15], v[160:163], a[32:35], v[0:15]\n\t" \
  "ds_read_b64_tr_b16 v[144:145], v176 offset:20480\n\t" \
  "ds_read_b64_tr_b16 v[146:147], v176 offset:21504\n\t" \
  "v_rcp_f32 v71, v71\n\t" \
  "v_mul_f32 v100, v100, v68\n\t" \
  "v_mfma_f32_32x32x16_bf16 v[16:31], v[164:167], a[32:35], v[16:31]\n\t" \
  "ds_read_b64_tr_b16 v[148:149], v177 offset:20480\n\t" \
  "ds_read_b64_tr_b16 v[150:151], v177 offset:21504\n\t" \
  "v_mul_f32 v101, v101, v69\n\t" \
  "v_mfma_f32_32x32x16_bf16 v[32:47], v[168:171], a[32:35], v[32:47]\n\t" \
  "ds_read_b64_tr_b16 v[152:153], v176 offset:45056\n\t" \
  "ds_read_b64_tr_b16 v[154:155], v176 offset:46080\n\t" \
  "v_mul_f32 v102, v102, v70\n\t" \
  "v_mul_f32 v103, v103, v71\n\t" \
  "v_mfma_f32_32x32x16_bf16 v[48:63], v[172:175], a[32:35], v[48:63]\n\t" \
  "ds_read_b64_tr_b16 v[156:157], v177 offset:45056\n\t" \
  "ds_read_b64_tr_b16 v[158:159], v177 offset:46080\n\t" \
  "v_cvt_pk_bf16_f32 v68, v100, v101\n\t" \
  "s_waitcnt lgkmcnt(8)\n\t" \
  "v_mfma_f32_32x32x16_bf16 v[0:15], v[128:131], a[36:39], v[0:15]\n\t" \
  "ds_read_b64_tr_b16 v[160:161], v176 offset:22528\n\t" \
  "ds_read_b64_tr_b16 v[162:163], v176 offset:23552\n\t" \
  "ds_read_b64_tr_b16 v[164:165], v177 offset:22528\n\t" \
  "ds_read_b64_tr_b16 v[166:167], v177 offset:23552\n\t" \
  "v_cvt_pk_bf16_f32 v69, v102, v103\n\t" \
  "v_accvgpr_write_b32 a162, v68\n\t" \
  "v_mfma_f32_32x32x16_bf16 v[16:31], v[132:135], a[36:39], v[16:31]\n\t" \
  "ds_read_b64_tr_b16 v[168:169], v176 offset:47104\n\t" \
  "ds_read_b64_tr_b16 v[170:171], v176 offset:48128\n\t" \
  "ds_read_b64_tr_b16 v[172:173], v177 offset:47104\n\t" \
  "ds_read_b64_tr_b16 v[174:175], v177 offset:48128\n\t" \
  "v_accvgpr_write_b32 a163, v69\n\t" \
  "v_mul_f32 v104, v72, v104\n\t" \
  "v_mfma_f32_32x32x16_bf16 v[32:47], v[136:139], a[36:39], v[32:47]\n\t" \
  "v_mul_f32 v105, v73, v105\n\t" \
  "v_mfma_f32_32x32x16_bf16 v[48:63], v[140:143], a[36:39], v[48:63]\n\t" \
  "v_mul_f32 v106, v74, v106\n\t" \
  "v_mul_f32 v107, v75, v107\n\t" \
  "s_waitcnt lgkmcnt(8)\n\t" \
  "v_mfma_f32_32x32x16_bf16 v[0:15], v[144:147], a[40:43], v[0:15]\n\t" \
  "s_waitcnt vmcnt(12) lgkmcnt(0)\n\ts_barrier\n\t" \
  "ds_read_b64_tr_b16 v[128:129], v178 offset:0\n\t" \
  "ds_read_b64_tr_b16 v[130:131], v178 offset:1024\n\t" \
  "v_mul_f32 v72, v196, v72\n\t" \
  "v_mfma_f32_32x32x16_bf16 v[16:31], v[148:151], a[40:43], v[16:31]\n\t" \
  "ds_read_b64_tr_b16 v[132:133], v179 offset:0\n\t" \
  "ds_read_b64_tr_b16 v[134:135], v179 offset:1024\n\t" \
  "s_add_u32 s40, %7, 0\n\t" \
  "s_add_u32 m0, %14, 0\n\t" \
  "s_nop 0\n\t" \
  "buffer_load_dwordx4 v185, %0, s40 offen lds\n\t" \
  "v_mul_f32 v73, v196, v73\n\t" \
  "v_mul_f32 v74, v196, v74\n\t" \
  "v_mfma_f32_32x32x16_bf16 v[32:47], v[152:155], a[40:43], v[32:47]\n\t" \
  "ds_read_b64_tr_b16 v[136:137], v178 offset:24576\n\t" \
  "ds_read_b64_tr_b16 v[138:139], v178 offset:25600\n\t" \
  "buffer_load_dwordx4 v186, %0, s40 offen offset:1024 lds\n\t" \
  "v_mul_f32 v75, v196, v75\n\t" \
  "v_exp_f32 v72, v72\n\t" \
  "v_mfma_f32_32x32x16_bf16 v[48:63], v[156:159], a[40:43], v[48:63]\n\t" \
  "ds_read_b64_tr_b16 v[140:141], v179 offset:24576\n\t" \
  "ds_read_b64_tr_b16 v[142:143], v179 offset:25600\n\t" \
  "v_exp_f32 v73, v73\n\t" \
  "s_waitcnt lgkmcnt(8)\n\t" \
  "v_mfma_f32_32x32x16_bf16 v[0:15], v[160:163], a[44:47], v[0:15]\n\t" \
  "ds_read_b64_tr_b16 v[144:145], v178 offset:2048\n\t" \
  "ds_read_b64_tr_b16 v[146:147], v178 offset:3072\n\t" \
  "v_exp_f32 v74, v74\n\t" \
  "v_exp_f32 v75, v75\n\t" \
  "v_mfma_f32_32x32x16_bf16 v[16:31], v[164:167], a[44:47], v[16:31]\n\t" \
  "ds_read_b64_tr_b16 v[148:149], v179 offset:2048\n\t" \
  "ds_read_b64_tr_b16 v[150:151], v179 offset:3072\n\t" \
  "v_fma_f32 v72, v72, v197, v197\n\t" \
  "v_mfma_f32_32x32x16_bf16 v[32:47], v[168:171], a[44:47], v[32:47]\n\t" \
  "ds_read_b64_tr_b16 v[152:153], v178 offset:26624\n\t" \
  "ds_read_b64_tr_b16 v[154:155], v178 offset:27648\n\t" \
  "buffer_load_dwordx4 v187, %0, s40 offen offset:2048 lds\n\t" \
  "v_fma_f32 v73, v73, v197, v197\n\t" \
  "v_fma_f32 v74, v74, v197, v197\n\t" \
  "v_mfma_f32_32x32x16_bf16 v[48:63], v[172:175], a[44:47], v[48:63]\n\t" \
  "ds_read_b64_tr_b16 v[156:157], v179 offset:26624\n\t" \
  "ds_read_b64_tr_b16 v[158:159], v179 offset:27648\n\t" \
  "v_fma_f32 v75, v75, v197, v197\n\t" \
  "s_waitcnt lgkmcnt(8)\n\t" \
  "v_mfma_f32_32x32x16_bf16 v[0:15], v[128:131], a[48:51], v[0:15]\n\t" \
  "ds_read_b64_tr_b16 v[160:161], v178 offset:4096\n\t" \
  "ds_read_b64_tr_b16 v[162:163], v178 offset:5120\n\t" \
  "v_rcp_f32 v72, v72\n\t" \
  "v_rcp_f32 v73, v73\n\t" \
  "v_mfma_f32_32x32x16_bf16 v[16:31], v[132:135], a[48:51], v[16:31]\n\t" \
  "ds_read_b64_tr_b16 v[164:165], v179 offset:4096\n\t" \
  "ds_read_b64_tr_b16 v[166:167], v179 offset:5120\n\t" \
  "v_rcp_f32 v74, v74\n\t" \
  "v_rcp_f32 v75, v75\n\t" \
  "v_mfma_f32_32x32x16_bf16 v[32:47], v[136:139], a[48:51], v[32:47]\n\t" \
  "ds_read_b64_tr_b16 v[168:169], v178 offset:28672\n\t" \
  "ds_read_b64_tr_b16 v[170:171], v178 offset:29696\n\t" \
  "buffer_load_dwordx4 v188, %0, s40 offen offset:3072 lds\n\t" \
  "v_mul_f32 v104, v104, v72\n\t" \
  "v_mfma_f32_32x32x16_bf16 v[48:63], v[140:143], a[48:51], v[48:63]\n\t" \
  "ds_read_b64_tr_b16 v[172:173], v179 offset:28672\n\t" \
  "ds_read_b64_tr_b16 v[174:175], v179 offset:29696\n\t" \
  "v_mul_f32 v105, v105, v73\n\t" \
  "v_mul_f32 v106, v106, v74\n\t" \
  "s_waitcnt lgkmcnt(8)\n\t" \
  "v_mfma_f32_32x32x16_bf16 v[0:15], v[144:147], a[52:55], v[0:15]\n\t" \
  "ds_read_b64_tr_b16 v[128:129], v178 offset:6144\n\t" \
  "ds_read_b64_tr_b16 v[130:131], v178 offset:7168\n\t" \
  "s_add_u32 s40, %7, 294912\n\t" \
  "s_add_u32 m0, %14, 4096\n\t" \
  "v_mul_f32 v107, v107, v75\n\t" \
  "v_mfma_f32_32x32x16_bf16 v[16:31], v[148:151], a[52:55], v[16:31]\n\t" \
  "ds_read_b64_tr_b16 v[132:133], v179 offset:6144\n\t" \
  "ds_read_b64_tr_b16 v[134:135], v179 offset:7168\n\t" \
  "buffer_load_dwordx4 v185, %0, s40 offen lds\n\t" \
  "v_cvt_pk_bf16_f32 v72, v104, v105\n\t" \
  "v_cvt_pk_bf16_f32 v73, v106, v107\n\t" \
  "v_mfma_f32_32x32x16_bf16 v[32:47], v[152:155], a[52:55], v[32:47]\n\t" \
  "ds_read_b64_tr_b16 v[136:137], v178 offset:30720\n\t" \
  "ds_read_b64_tr_b16 v[138:139], v178 offset:31744\n\t" \
  "v_accvgpr_write_b32 a164, v72\n\t" \
  "v_accvgpr_write_b32 a165, v73\n\t" \
  "v_mfma_f32_32x32x16_bf16 v[48:63], v[156:159], a[52:55], v[48:63]\n\t" \
  "ds_read_b64_tr_b16 v[140:141], v179 offset:30720\n\t" \
  "ds_read_b64_tr_b16 v[142:143], v179 offset:31744\n\t" \
  "v_mul_f32 v108, v76, v108\n\t" \
  "s_waitcnt lgkmcnt(8)\n\t" \
  "v_mfma_f32_32x32x16_bf16 v[0:15], v[160:163], a[56:59], v[0:15]\n\t" \
  "ds_read_b64_tr_b16 v[144:145], v178 offset:8192\n\t" \
  "ds_read_b64_tr_b16 v[146:147], v178 offset:9216\n\t" \
  "v_mul_f32 v109, v77, v109\n\t" \
  "v_mul_f32 v110, v78, v110\n\t" \
  "v_mfma_f32_32x32x16_bf16 v[16:31], v[164:167], a[56:59], v[16:31]\n\t" \
  "ds_read_b64_tr_b16 v[148:149], v179 offset:8192\n\t" \
  "ds_read_b64_tr_b16 v[150:151], v179 offset:9216\n\t" \
  "buffer_load_dwordx4 v186, %0, s40 offen offset:1024 lds\n\t" \
  "v_mul_f32 v111, v79, v111\n\t" \
  "v_mfma_f32_32x32x16_bf16 v[32:47], v[168:171], a[56:59], v[32:47]\n\t" \
  "ds_read_b64_tr_b16 v[152:153], v178 offset:32768\n\t" \
  "ds_read_b64_tr_b16 v[154:155], v178 offset:33792\n\t" \
  "v_mul_f32 v76, v196, v76\n\t" \
  "v_mul_f32 v77, v196, v77\n\t" \
  "v_mfma_f32_32x32x16_bf16 v[48:63], v[172:175], a[56:59], v[48:63]\n\t" \
  "ds_read_b64_tr_b16 v[156:157], v179 offset:32768\n\t" \
  "ds_read_b64_tr_b16 v[158:159], v179 offset:33792\n\t" \
  "v_mul_f32 v78, v196, v78\n\t" \
  "v_mul_f32 v79, v196, v79\n\t" \
  "s_waitcnt lgkmcnt(8)\n\t" \
  "v_mfma_f32_32x32x16_bf16 v[0:15], v[128:131], a[60:63], v[0:15]\n\t" \
  "ds_read_b64_tr_b16 v[160:161], v178 offset:10240\n\t" \
  "ds_read_b64_tr_b16 v[162:163], v178 offset:11264\n\t" \
  "v_exp_f32 v76, v76\n\t" \
  "v_mfma_f32_32x32x16_bf16 v[16:31], v[132:135], a[60:63], v[16:31]\n\t" \
  "ds_read_b64_tr_b16 v[164:165], v179 offset:10240\n\t" \
  "ds_read_b64_tr_b16 v[166:167], v179 offset:11264\n\t" \
  "buffer_load_dwordx4 v187, %0, s40 offen offset:2048 lds\n\t" \
  "v_exp_f32 v77, v77\n\t" \
  "v_exp_f32 v78, v78\n\t" \
  "v_mfma_f32_32x32x16_bf16 v[32:47], v[136:139], a[60:63], v[32:47]\n\t" \
  "ds_read_b64_tr_b16 v[168:169], v178 offset:34816\n\t" \
  "ds_read_b64_tr_b16 v[170:171], v178 offset:35840\n\t" \
  "buffer_load_dwordx4 v188, %0, s40 offen offset:3072 lds\n\t" \
  "v_exp_f32 v79, v79\n\t" \
  "v_mfma_f32_32x32x16_bf16 v[48:63], v[140:143], a[60:63], v[48:63]\n\t" \
  "ds_read_b64_tr_b16 v[172:173], v179 offset:34816\n\t" \
  "ds_read_b64_tr_b16 v[174:175], v179 offset:35840\n\t" \
  "v_fma_f32 v76, v76, v197, v197\n\t" \
  "v_fma_f32 v77, v77, v197, v197\n\t" \
  "s_waitcnt lgkmcnt(8)\n\t" \
  "v_mfma_f32_32x32x16_bf16 v[0:15], v[144:147], a[64:67], v[0:15]\n\t" \
  "ds_read_b64_tr_b16 v[128:129], v178 offset:12288\n\t" \
  "ds_read_b64_tr_b16 v[130:131], v178 offset:13312\n\t" \
  "v_fma_f32 v78, v78, v197, v197\n\t" \
  "v_mfma_f32_32x32x16_bf16 v[16:31], v[148:151], a[64:67], v[16:31]\n\t" \
  "ds_read_b64_tr_b16 v[132:133], v179 offset:12288\n\t" \
  "ds_read_b64_tr_b16 v[134:135], v179 offset:13312\n\t" \
  "s_add_u32 s40, %7, 589824\n\t" \
  "s_add_u32 m0, %14, 8192\n\t" \
  "v_fma_f32 v79, v79, v197, v197\n\t" \
  "v_rcp_f32 v76, v76\n\t" \
  "v_mfma_f32_32x32x16_bf16 v[32:47], v[152:155], a[64:67], v[32:47]\n\t" \
  "ds_read_b64_tr_b16 v[136:137], v178 offset:36864\n\t" \
  "ds_read_b64_tr_b16 v[138:139], v178 offset:37888\n\t" \
  "buffer_load_dwordx4 v185, %0, s40 offen lds\n\t" \
  "v_rcp_f32 v77, v77\n\t" \
  "v_rcp_f32 v78, v78\n\t" \
  "v_mfma_f32_32x32x16_bf16 v[48:63], v[156:159], a[64:67], v[48:63]\n\t" \
  "ds_read_b64_tr_b16 v[140:141], v179 offset:36864\n\t" \
  "ds_read_b64_tr_b16 v[142:143], v179 offset:37888\n\t" \
  "v_rcp_f32 v79, v79\n\t" \
  "s_waitcnt lgkmcnt(8)\n\t" \
  "v_mfma_f32_32x32x16_bf16 v[0:15], v[160:163], a[68:71], v[0:15]\n\t" \
  "ds_read_b64_tr_b16 v[144:145], v178 offset:14336\n\t" \
  "ds_read_b64_tr_b16 v[146:147], v178 offset:15360\n\t" \
  "v_mul_f32 v108, v108, v76\n\t" \
  "v_mul_f32 v109, v109, v77\n\t" \
  "v_mfma_f32_32x32x16_bf16 v[16:31], v[164:167], a[68:71], v[16:31]\n\t" \
  "ds_read_b64_tr_b16 v[148:149], v179 offset:14336\n\t" \
  "ds_read_b64_tr_b16 v[150:151], v179 offset:15360\n\t" \
  "v_mul_f32 v110, v110, v78\n\t" \
  "v_mfma_f32_32x32x16_bf16 v[32:47], v[168:171], a[68:71], v[32:47]\n\t" \
  "ds_read_b64_tr_b16 v[152:153], v178 offset:38912\n\t" \
  "ds_read_b64_tr_b16 v[154:155], v178 offset:39936\n\t" \
  "buffer_load_dwordx4 v186, %0, s40 offen offset:1024 lds\n\t" \
  "v_mul_f32 v111, v111, v79\n\t" \
  "v_cvt_pk_bf16_f32 v76, v108, v109\n\t" \
  "v_mfma_f32_32x32x16_bf16 v[48:63], v[172:175], a[68:71], v[48:63]\n\t" \
  "ds_read_b64_tr_b16 v[156:157], v179 offset:38912\n\t" \
  "ds_read_b64_tr_b16 v[158:159], v179 offset:39936\n\t" \
  "v_cvt_pk_bf16_f32 v77, v110, v111\n\t" \
  "v_accvgpr_write_b32 a166, v76\n\t" \
  "s_waitcnt lgkmcnt(8)\n\t" \
  "v_mfma_f32_32x32x16_bf16 v[0:15], v[128:131], a[72:75], v[0:15]\n\t" \
  "ds_read_b64_tr_b16 v[160:161], v178 offset:16384\n\t" \
  "ds_read_b64_tr_b16 v[162:163], v178 offset:17408\n\t" \
  "v_accvgpr_write_b32 a167, v77\n\t" \
  "v_mfma_f32_32x32x16_bf16 v[16:31], v[132:135], a[72:75], v[16:31]\n\t" \
  "ds_read_b64_tr_b16 v[164:165], v179 offset:16384\n\t" \
  "ds_read_b64_tr_b16 v[166:167], v179 offset:17408\n\t" \
  "buffer_load_dwordx4 v187, %0, s40 offen offset:2048 lds\n\t" \
  "v_mul_f32 v112, v80, v112\n\t" \
  "v_mul_f32 v113, v81, v113\n\t" \
  "v_mfma_f32_32x32x16_bf16 v[32:47], v[136:139], a[72:75], v[32:47]\n\t" \
  "ds_read_b64_tr_b16 v[168:169], v178 offset:40960\n\t" \
  "ds_read_b64_tr_b16 v[170:171], v178 offset:41984\n\t" \
  "v_mul_f32 v114, v82, v114\n\t" \
  "v_mfma_f32_32x32x16_bf16 v[48:63], v[140:143], a[72:75], v[48:63]\n\t" \
  "ds_read_b64_tr_b16 v[172:173], v179 offset:40960\n\t" \
  "ds_read_b64_tr_b16 v[174:175], v179 offset:41984\n\t" \
  "v_mul_f32 v115, v83, v115\n\t" \
  "v_mul_f32 v80, v196, v80\n\t" \
  "s_waitcnt lgkmcnt(8)\n\t" \
  "v_mfma_f32_32x32x16_bf16 v[0:15], v[144:147], a[76:79], v[0:15]\n\t" \
  "ds_read_b64_tr_b16 v[128:129], v178 offset:18432\n\t" \
  "ds_read_b64_tr_b16 v[130:131], v178 offset:19456\n\t" \
  "v_mul_f32 v81, v196, v81\n\t" \
  "v_mul_f32 v82, v196, v82\n\t" \
  "v_mfma_f32_32x32x16_bf16 v[16:31], v[148:151], a[76:79], v[16:31]\n\t" \
  "ds_read_b64_tr_b16 v[132:133], v179 offset:18432\n\t" \
  "ds_read_b64_tr_b16 v[134:135], v179 offset:19456\n\t" \
  "buffer_load_dwordx4 v188, %0, s40 offen offset:3072 lds\n\t" \
  "v_mul_f32 v83, v196, v83\n\t" \
  "v_mfma_f32_32x32x16_bf16 v[32:47], v[152:155], a[76:79], v[32:47]\n\t" \
  "ds_read_b64_tr_b16 v[136:137], v178 offset:43008\n\t" \
  "ds_read_b64_tr_b16 v[138:139], v178 offset:44032\n\t" \
  "v_exp_f32 v80, v80\n\t" \
  "v_exp_f32 v81, v81\n\t" \
  "v_mfma_f32_32x32x16_bf16 v[48:63], v[156:159], a[76:79], v[48:63]\n\t" \
  "ds_read_b64_tr_b16 v[140:141], v179 offset:43008\n\t" \
  "ds_read_b64_tr_b16 v[142:143], v179 offset:44032\n\t" \
  "v_exp_f32 v82, v82\n\t" \
  "s_waitcnt lgkmcnt(8)\n\t" \
  "v_mfma_f32_32x32x16_bf16 v[0:15], v[160:163], a[80:83], v[0:15]\n\t" \
  "ds_read_b64_tr_b16 v[144:145], v178 offset:20480\n\t" \
  "ds_read_b64_tr_b16 v[146:147], v178 offset:21504\n\t" \
  "v_exp_f32 v83, v83\n\t" \
  "v_fma_f32 v80, v80, v197, v197\n\t" \
  "v_mfma_f32_32x32x16_bf16 v[16:31], v[164:167], a[80:83], v[16:31]\n\t" \
  "ds_read_b64_tr_b16 v[148:149], v179 offset:20480\n\t" \
  "ds_read_b64_tr_b16 v[150:151], v179 offset:21504\n\t" \
  "v_fma_f32 v81, v81, v197, v197\n\t" \
  "v_fma_f32 v82, v82, v197, v197\n\t" \
  "v_mfma_f32_32x32x16_bf16 v[32:47], v[168:171], a[80:83], v[32:47]\n\t" \
  "ds_read_b64_tr_b16 v[152:153], v178 offset:45056\n\t" \
  "ds_read_b64_tr_b16 v[154:155], v178 offset:46080\n\t" \
  "v_fma_f32 v83, v83, v197, v197\n\t" \
  "v_mfma_f32_32x32x16_bf16 v[48:63], v[172:175], a[80:83], v[48:63]\n\t" \
  "ds_read_b64_tr_b16 v[156:157], v179 offset:45056\n\t" \
  "ds_read_b64_tr_b16 v[158:159], v179 offset:46080\n\t" \
  "v_rcp_f32 v80, v80\n\t" \
  "v_rcp_f32 v81, v81\n\t" \
  "s_waitcnt lgkmcnt(8)\n\t" \
  "v_mfma_f32_32x32x16_bf16 v[0:15], v[128:131], a[84:87], v[0:15]\n\t" \
  "ds_read_b64_tr_b16 v[160:161], v178 offset:22528\n\t" \
  "ds_read_b64_tr_b16 v[162:163], v178 offset:23552\n\t" \
  "ds_read_b64_tr_b16 v[164:165], v179 offset:22528\n\t" \
  "ds_read_b64_tr_b16 v[166:167], v179 offset:23552\n\t" \
  "v_rcp_f32 v82, v82\n\t" \
  "v_mfma_f32_32x32x16_bf16 v[16:31], v[132:135], a[84:87], v[16:31]\n\t" \
  "ds_read_b64_tr_b16 v[168:169], v178 offset:47104\n\t" \
  "ds_read_b64_tr_b16 v[170:171], v178 offset:48128\n\t" \
  "ds_read_b64_tr_b16 v[172:173], v179 offset:47104\n\t" \
  "ds_read_b64_tr_b16 v[174:175], v179 offset:48128\n\t" \
  "v_rcp_f32 v83, v83\n\t" \
  "v_mul_f32 v112, v112, v80\n\t" \
  "v_mfma_f32_32x32x16_bf16 v[32:47], v[136:139], a[84:87], v[32:47]\n\t" \
  "v_mul_f32 v113, v113, v81\n\t" \
  "v_mfma_f32_32x32x16_bf16 v[48:63], v[140:143], a[84:87], v[48:63]\n\t" \
  "v_mul_f32 v114, v114, v82\n\t" \
  "v_mul_f32 v115, v115, v83\n\t" \
  "s_waitcnt lgkmcnt(8)\n\t" \
  "v_mfma_f32_32x32x16_bf16 v[0:15], v[144:147], a[88:91], v[0:15]\n\t" \
  "s_waitcnt vmcnt(12) lgkmcnt(0)\n\ts_barrier\n\t" \
  "ds_read_b64_tr_b16 v[128:129], v180 offset:0\n\t" \
  "ds_read_b64_tr_b16 v[130:131], v180 offset:1024\n\t" \
  "v_cvt_pk_bf16_f32 v80, v112, v113\n\t" \
  "v_cvt_pk_bf16_f32 v81, v114, v115\n\t" \
  "v_mfma_f32_32x32x16_bf16 v[16:31], v[148:151], a[88:91], v[16:31]\n\t" \
  "ds_read_b64_tr_b16 v[132:133], v181 offset:0\n\t" \
  "ds_read_b64_tr_b16 v[134:135], v181 offset:1024\n\t" \
  "s_add_u32 s40, %7, 1769472\n\t" \
  "s_add_u32 m0, %14, 49152\n\t" \
  "s_nop 0\n\t" \
  "buffer_load_dwordx4 v185, %0, s40 offen lds\n\t" \
  "v_accvgpr_write_b32 a168, v80\n\t" \
  "v_mfma_f32_32x32x16_bf16 v[32:47], v[152:155], a[88:91], v[32:47]\n\t" \
  "ds_read_b64_tr_b16 v[136:137], v180 offset:24576\n\t" \
  "ds_read_b64_tr_b16 v[138:139], v180 offset:25600\n\t" \
  "buffer_load_dwordx4 v186, %0, s40 offen offset:1024 lds\n\t" \
  "v_accvgpr_write_b32 a169, v81\n\t" \
  "v_mul_f32 v116, v84, v116\n\t" \
  "v_mfma_f32_32x32x16_bf16 v[48:63], v[156:159], a[88:91], v[48:63]\n\t" \
  "ds_read_b64_tr_b16 v[140:141], v181 offset:24576\n\t" \
  "ds_read_b64_tr_b16 v[142:143], v181 offset:25600\n\t" \
  "v_mul_f32 v117, v85, v117\n\t" \
  "s_waitcnt lgkmcnt(8)\n\t" \
  "v_mfma_f32_32x32x16_bf16 v[0:15], v[160:163], a[92:95], v[0:15]\n\t" \
  "ds_read_b64_tr_b16 v[144:145], v180 offset:2048\n\t" \
  "ds_read_b64_tr_b16 v[146:147], v180 offset:3072\n\t" \
  "v_mul_f32 v118, v86, v118\n\t" \
  "v_mul_f32 v119, v87, v119\n\t" \
  "v_mfma_f32_32x32x16_bf16 v[16:31], v[164:167], a[92:95], v[16:31]\n\t" \
  "ds_read_b64_tr_b16 v[148:149], v181 offset:2048\n\t" \
  "ds_read_b64_tr_b16 v[150:151], v181 offset:3072\n\t" \
  "v_mul_f32 v84, v196, v84\n\t" \
  "v_mul_f32 v85, v196, v85\n\t" \
  "v_mfma_f32_32x32x16_bf16 v[32:47], v[168:171], a[92:95], v[32:47]\n\t" \
  "ds_read_b64_tr_b16 v[152:153], v180 offset:26624\n\t" \
  "ds_read_b64_tr_b16 v[154:155], v180 offset:27648\n\t" \
  "buffer_load_dwordx4 v187, %0, s40 offen offset:2048 lds\n\t" \
  "v_mul_f32 v86, v196, v86\n\t" \
  "v_mfma_f32_32x32x16_bf16 v[48:63], v[172:175], a[92:95], v[48:63]\n\t" \
  "ds_read_b64_tr_b16 v[156:157], v181 offset:26624\n\t" \
  "ds_read_b64_tr_b16 v[158:159], v181 offset:27648\n\t" \
  "v_mul_f32 v87, v196, v87\n\t" \
  "v_exp_f32 v84, v84\n\t" \
  "s_waitcnt lgkmcnt(8)\n\t" \
  "v_mfma_f32_32x32x16_bf16 v[0:15], v[128:131], a[96:99], v[0:15]\n\t" \
  "ds_read_b64_tr_b16 v[160:161], v180 offset:4096\n\t" \
  "ds_read_b64_tr_b16 v[162:163], v180 offset:5120\n\t" \
  "v_exp_f32 v85, v85\n\t" \
  "v_mfma_f32_32x32x16_bf16 v[16:31], v[132:135], a[96:99], v[16:31]\n\t" \
  "ds_read_b64_tr_b16 v[164:165], v181 offset:4096\n\t" \
  "ds_read_b64_tr_b16 v[166:167], v181 offset:5120\n\t" \
  "v_exp_f32 v86, v86\n\t" \
  "v_exp_f32 v87, v87\n\t" \
  "v_mfma_f32_32x32x16_bf16 v[32:47], v[136:139], a[96:99], v[32:47]\n\t" \
  "ds_read_b64_tr_b16 v[168:169], v180 offset:28672\n\t" \
  "ds_read_b64_tr_b16 v[170:171], v180 offset:29696\n\t" \
  "buffer_load_dwordx4 v188, %0, s40 offen offset:3072 lds\n\t" \
  "v_fma_f32 v84, v84, v197, v197\n\t" \
  "v_fma_f32 v85, v85, v197, v197\n\t" \
  "v_mfma_f32_32x32x16_bf16 v[48:63], v[140:143], a[96:99], v[48:63]\n\t" \
  "ds_read_b64_tr_b16 v[172:173], v181 offset:28672\n\t" \
  "ds_read_b64_tr_b16 v[174:175], v181 offset:29696\n\t" \
  "v_fma_f32 v86, v86, v197, v197\n\t" \
  "s_waitcnt lgkmcnt(8)\n\t" \
  "v_mfma_f32_32x32x16_bf16 v[0:15], v[144:147], a[100:103], v[0:15]\n\t" \
  "ds_read_b64_tr_b16 v[128:129], v180 offset:6144\n\t" \
  "ds_read_b64_tr_b16 v[130:131], v180 offset:7168\n\t" \
  "s_add_u32 s40, %7, 2064384\n\t" \
  "s_add_u32 m0, %14, 53248\n\t" \
  "v_fma_f32 v87, v87, v197, v197\n\t" \
  "v_rcp_f32 v84, v84\n\t" \
  "v_mfma_f32_32x32x16_bf16 v[16:31], v[148:151], a[100:103], v[16:31]\n\t" \
  "ds_read_b64_tr_b16 v[132:133], v181 offset:6144\n\t" \
  "ds_read_b64_tr_b16 v[134:135], v181 offset:7168\n\t" \
  "buffer_load_dwordx4 v185, %0, s40 offen lds\n\t" \
  "v_rcp_f32 v85, v85\n\t" \
  "v_mfma_f32_32x32x16_bf16 v[32:47], v[152:155], a[100:103], v[32:47]\n\t" \
  "ds_read_b64_tr_b16 v[136:137], v180 offset:30720\n\t" \
  "ds_read_b64_tr_b16 v[138:139], v180 offset:31744\n\t" \
  "v_rcp_f32 v86, v86\n\t" \
  "v_rcp_f32 v87, v87\n\t" \
  "v_mfma_f32_32x32x16_bf16 v[48:63], v[156:159], a[100:103], v[48:63]\n\t" \
  "ds_read_b64_tr_b16 v[140:141], v181 offset:30720\n\t" \
  "ds_read_b64_tr_b16 v[142:143], v181 offset:31744\n\t" \
  "v_mul_f32 v116, v116, v84\n\t" \
  "s_waitcnt lgkmcnt(8)\n\t" \
  "v_mfma_f32_32x32x16_bf16 v[0:15], v[160:163], a[104:107], v[0:15]\n\t" \
  "ds_read_b64_tr_b16 v[144:145], v180 offset:8192\n\t" \
  "ds_read_b64_tr_b16 v[146:147], v180 offset:9216\n\t" \
  "v_mul_f32 v117, v117, v85\n\t" \
  "v_mul_f32 v118, v118, v86\n\t" \
  "v_mfma_f32_32x32x16_bf16 v[16:31], v[164:167], a[104:107], v[16:31]\n\t" \
  "ds_read_b64_tr_b16 v[148:149], v181 offset:8192\n\t" \
  "ds_read_b64_tr_b16 v[150:151], v181 offset:9216\n\t" \
  "buffer_load_dwordx4 v186, %0, s40 offen offset:1024 lds\n\t" \
  "v_mul_f32 v119, v119, v87\n\t" \
  "v_cvt_pk_bf16_f32 v84, v116, v117\n\t" \
  "v_mfma_f32_32x32x16_bf16 v[32:47], v[168:171], a[104:107], v[32:47]\n\t" \
  "ds_read_b64_tr_b16 v[152:153], v180 offset:32768\n\t" \
  "ds_read_b64_tr_b16 v[154:155], v180 offset:33792\n\t" \
  "v_cvt_pk_bf16_f32 v85, v118, v119\n\t" \
  "v_mfma_f32_32x32x16_bf16 v[48:63], v[172:175], a[104:107], v[48:63]\n\t" \
  "ds_read_b64_tr_b16 v[156:157], v181 offset:32768\n\t" \
  "ds_read_b64_tr_b16 v[158:159], v181 offset:33792\n\t" \
  "v_accvgpr_write_b32 a170, v84\n\t" \
  "v_accvgpr_write_b32 a171, v85\n\t" \
  "s_waitcnt lgkmcnt(8)\n\t" \
  "v_mfma_f32_32x32x16_bf16 v[0:15], v[128:131], a[108:111], v[0:15]\n\t" \
  "ds_read_b64_tr_b16 v[160:161], v180 offset:10240\n\t" \
  "ds_read_b64_tr_b16 v[162:163], v180 offset:11264\n\t" \
  "v_mul_f32 v120, v88, v120\n\t" \
  "v_mfma_f32_32x32x16_bf16 v[16:31], v[132:135], a[108:111], v[16:31]\n\t" \
  "ds_read_b64_tr_b16 v[164:165], v181 offset:10240\n\t" \
  "ds_read_b64_tr_b16 v[166:167], v181 offset:11264\n\t" \
  "buffer_load_dwordx4 v187, %0, s40 offen offset:2048 lds\n\t" \
  "v_mul_f32 v121, v89, v121\n\t" \
  "v_mul_f32 v122, v90, v122\n\t" \
  "v_mfma_f32_32x32x16_bf16 v[32:47], v[136:139], a[108:111], v[32:47]\n\t" \
  "ds_read_b64_tr_b16 v[168:169], v180 offset:34816\n\t" \
  "ds_read_b64_tr_b16 v[170:171], v180 offset:35840\n\t" \
  "buffer_load_dwordx4 v188, %0, s40 offen offset:3072 lds\n\t" \
  "v_mul_f32 v123, v91, v123\n\t" \
  "v_mul_f32 v88, v196, v88\n\t" \
  "v_mfma_f32_32x32x16_bf16 v[48:63], v[140:143], a[108:111], v[48:63]\n\t" \
  "ds_read_b64_tr_b16 v[172:173], v181 offset:34816\n\t" \
  "ds_read_b64_tr_b16 v[174:175], v181 offset:35840\n\t" \
  "v_mul_f32 v89, v196, v89\n\t" \
  "s_waitcnt lgkmcnt(8)\n\t" \
  "v_mfma_f32_32x32x16_bf16 v[0:15], v[144:147], a[112:115], v[0:15]\n\t" \
  "ds_read_b64_tr_b16 v[128:129], v180 offset:12288\n\t" \
  "ds_read_b64_tr_b16 v[130:131], v180 offset:13312\n\t" \
  "v_mul_f32 v90, v196, v90\n\t" \
  "v_mul_f32 v91, v196, v91\n\t" \
  "v_mfma_f32_32x32x16_bf16 v[16:31], v[148:151], a[112:115], v[16:31]\n\t" \
  "ds_read_b64_tr_b16 v[132:133], v181 offset:12288\n\t" \
  "ds_read_b64_tr_b16 v[134:135], v181 offset:13312\n\t" \
  "s_add_u32 s40, %7, 2359296\n\t" \
  "s_add_u32 m0, %14, 57344\n\t" \
  "v_exp_f32 v88, v88\n\t" \
  "v_mfma_f32_32x32x16_bf16 v[32:47], v[152:155], a[112:115], v[32:47]\n\t" \
  "ds_read_b64_tr_b16 v[136:137], v180 offset:36864\n\t" \
  "ds_read_b64_tr_b16 v[138:139], v180 offset:37888\n\t" \
  "buffer_load_dwordx4 v185, %0, s40 offen lds\n\t" \
  "v_exp_f32 v89, v89\n\t" \
  "v_exp_f32 v90, v90\n\t" \
  "v_mfma_f32_32x32x16_bf16 v[48:63], v[156:159], a[112:115], v[48:63]\n\t" \
  "ds_read_b64_tr_b16 v[140:141], v181 offset:36864\n\t" \
  "ds_read_b64_tr_b16 v[142:143], v181 offset:37888\n\t" \
  "v_exp_f32 v91, v91\n\t" \
  "v_fma_f32 v88, v88, v197, v197\n\t" \
  "s_waitcnt lgkmcnt(8)\n\t" \
  "v_mfma_f32_32x32x16_bf16 v[0:15], v[160:163], a[116:119], v[0:15]\n\t" \
  "ds_read_b64_tr_b16 v[144:145], v180 offset:14336\n\t" \
  "ds_read_b64_tr_b16 v[146:147], v180 offset:15360\n\t" \
  "v_fma_f32 v89, v89, v197, v197\n\t" \
  "v_mfma_f32_32x32x16_bf16 v[16:31], v[164:167], a[116:119], v[16:31]\n\t" \
  "ds_read_b64_tr_b16 v[148:149], v181 offset:14336\n\t" \
  "ds_read_b64_tr_b16 v[150:151], v181 offset:15360\n\t" \
  "v_fma_f32 v90, v90, v197, v197\n\t" \
  "v_fma_f32 v91, v91, v197, v197\n\t" \
  "v_mfma_f32_32x32x16_bf16 v[32:47], v[168:171], a[116:119], v[32:47]\n\t" \
  "ds_read_b64_tr_b16 v[152:153], v180 offset:38912\n\t" \
  "ds_read_b64_tr_b16 v[154:155], v180 offset:39936\n\t" \
  "buffer_load_dwordx4 v186, %0, s40 offen offset:1024 lds\n\t" \
  "v_rcp_f32 v88, v88\n\t" \
  "v_mfma_f32_32x32x16_bf16 v[48:63], v[172:175], a[116:119], v[48:63]\n\t" \
  "ds_read_b64_tr_b16 v[156:157], v181 offset:38912\n\t" \
  "ds_read_b64_tr_b16 v[158:159], v181 offset:39936\n\t" \
  "v_rcp_f32 v89, v89\n\t" \
  "v_rcp_f32 v90, v90\n\t" \
  "s_waitcnt lgkmcnt(8)\n\t" \
  "v_mfma_f32_32x32x16_bf16 v[0:15], v[128:131], a[120:123], v[0:15]\n\t" \
  "ds_read_b64_tr_b16 v[160:161], v180 offset:16384\n\t" \
  "ds_read_b64_tr_b16 v[162:163], v180 offset:17408\n\t" \
  "v_rcp_f32 v91, v91\n\t" \
  "v_mfma_f32_32x32x16_bf16 v[16:31], v[132:135], a[120:123], v[16:31]\n\t" \
  "ds_read_b64_tr_b16 v[164:165], v181 offset:16384\n\t" \
  "ds_read_b64_tr_b16 v[166:167], v181 offset:17408\n\t" \
  "buffer_load_dwordx4 v187, %0, s40 offen offset:2048 lds\n\t" \
  "v_mul_f32 v120, v120, v88\n\t" \
  "v_mul_f32 v121, v121, v89\n\t" \
  "v_mfma_f32_32x32x16_bf16 v[32:47], v[136:139], a[120:123], v[32:47]\n\t" \
  "ds_read_b64_tr_b16 v[168:169], v180 offset:40960\n\t" \
  "ds_read_b64_tr_b16 v[170:171], v180 offset:41984\n\t" \
  "v_mul_f32 v122, v122, v90\n\t" \
  "v_mul_f32 v123, v123, v91\n\t" \
  "v_mfma_f32_32x32x16_bf16 v[48:63], v[140:143], a[120:123], v[48:63]\n\t" \
  "ds_read_b64_tr_b16 v[172:173], v181 offset:40960\n\t" \
  "ds_read_b64_tr_b16 v[174:175], v181 offset:41984\n\t" \
  "v_cvt_pk_bf16_f32 v88, v120, v121\n\t" \
  "s_waitcnt lgkmcnt(8)\n\t" \
  "v_mfma_f32_32x32x16_bf16 v[0:15], v[144:147], a[124:127], v[0:15]\n\t" \
  "ds_read_b64_tr_b16 v[128:129], v180 offset:18432\n\t" \
  "ds_read_b64_tr_b16 v[130:131], v180 offset:19456\n\t" \
  "v_cvt_pk_bf16_f32 v89, v122, v123\n\t" \
  "v_accvgpr_write_b32 a172, v88\n\t" \
  "v_mfma_f32_32x32x16_bf16 v[16:31], v[148:151], a[124:127], v[16:31]\n\t" \
  "ds_read_b64_tr_b16 v[132:133], v181 offset:18432\n\t" \
  "ds_read_b64_tr_b16 v[134:135], v181 offset:19456\n\t" \
  "buffer_load_dwordx4 v188, %0, s40 offen offset:3072 lds\n\t" \
  "v_accvgpr_write_b32 a173, v89\n\t" \
  "v_mfma_f32_32x32x16_bf16 v[32:47], v[152:155], a[124:127], v[32:47]\n\t" \
  "ds_read_b64_tr_b16 v[136:137], v180 offset:43008\n\t" \
  "ds_read_b64_tr_b16 v[138:139], v180 offset:44032\n\t" \
  "v_mul_f32 v124, v92, v124\n\t" \
  "v_mul_f32 v125, v93, v125\n\t" \
  "v_mfma_f32_32x32x16_bf16 v[48:63], v[156:159], a[124:127], v[48:63]\n\t" \
  "ds_read_b64_tr_b16 v[140:141], v181 offset:43008\n\t" \
  "ds_read_b64_tr_b16 v[142:143], v181 offset:44032\n\t" \
  "v_mul_f32 v126, v94, v126\n\t" \
  "v_mul_f32 v127, v95, v127\n\t" \
  "s_waitcnt lgkmcnt(8)\n\t" \
  "v_mfma_f32_32x32x16_bf16 v[0:15], v[160:163], a[128:131], v[0:15]\n\t" \
  "ds_read_b64_tr_b16 v[144:145], v180 offset:20480\n\t" \
  "ds_read_b64_tr_b16 v[146:147], v180 offset:21504\n\t" \
  "v_mul_f32 v92, v196, v92\n\t" \
  "v_mfma_f32_32x32x16_bf16 v[16:31], v[164:167], a[128:131], v[16:31]\n\t" \
  "ds_read_b64_tr_b16 v[148:149], v181 offset:20480\n\t" \
  "ds_read_b64_tr_b16 v[150:151], v181 offset:21504\n\t" \
  "v_mul_f32 v93, v196, v93\n\t" \
  "v_mul_f32 v94, v196, v94\n\t" \
  "v_mfma_f32_32x32x16_bf16 v[32:47], v[168:171], a[128:131], v[32:47]\n\t" \
  "ds_read_b64_tr_b16 v[152:153], v180 offset:45056\n\t" \
  "ds_read_b64_tr_b16 v[154:155], v180 offset:46080\n\t" \
  "v_mul_f32 v95, v196, v95\n\t" \
  "v_mfma_f32_32x32x16_bf16 v[48:63], v[172:175], a[128:131], v[48:63]\n\t" \
  "ds_read_b64_tr_b16 v[156:157], v181 offset:45056\n\t" \
  "ds_read_b64_tr_b16 v[158:159], v181 offset:46080\n\t" \
  "v_exp_f32 v92, v92\n\t" \
  "v_exp_f32 v93, v93\n\t" \
  "s_waitcnt lgkmcnt(8)\n\t" \
  "v_mfma_f32_32x32x16_bf16 v[0:15], v[128:131], a[132:135], v[0:15]\n\t" \
  "ds_read_b64_tr_b16 v[160:161], v180 offset:22528\n\t" \
  "ds_read_b64_tr_b16 v[162:163], v180 offset:23552\n\t" \
  "ds_read_b64_tr_b16 v[164:165], v181 offset:22528\n\t" \
  "ds_read_b64_tr_b16 v[166:167], v181 offset:23552\n\t" \
  "v_exp_f32 v94, v94\n\t" \
  "v_exp_f32 v95, v95\n\t" \
  "v_mfma_f32_32x32x16_bf16 v[16:31], v[132:135], a[132:135], v[16:31]\n\t" \
  "ds_read_b64_tr_b16 v[168:169], v180 offset:47104\n\t" \
  "ds_read_b64_tr_b16 v[170:171], v180 offset:48128\n\t" \
  "ds_read_b64_tr_b16 v[172:173], v181 offset:47104\n\t" \
  "ds_read_b64_tr_b16 v[174:175], v181 offset:48128\n\t" \
  "v_fma_f32 v92, v92, v197, v197\n\t" \
  "v_mfma_f32_32x32x16_bf16 v[32:47], v[136:139], a[132:135], v[32:47]\n\t" \
  "v_fma_f32 v93, v93, v197, v197\n\t" \
  "v_fma_f32 v94, v94, v197, v197\n\t" \
  "v_mfma_f32_32x32x16_bf16 v[48:63], v[140:143], a[132:135], v[48:63]\n\t" \
  "v_fma_f32 v95, v95, v197, v197\n\t" \
  "s_waitcnt lgkmcnt(8)\n\t" \
  "v_mfma_f32_32x32x16_bf16 v[0:15], v[144:147], a[136:139], v[0:15]\n\t" \
  "s_waitcnt vmcnt(12) lgkmcnt(0)\n\ts_barrier\n\t" \
  "ds_read_b64_tr_b16 v[128:129], v176 offset:0\n\t" \
  "ds_read_b64_tr_b16 v[130:131], v176 offset:1024\n\t" \
  "v_rcp_f32 v92, v92\n\t" \
  "v_rcp_f32 v93, v93\n\t" \
  "v_mfma_f32_32x32x16_bf16 v[16:31], v[148:151], a[136:139], v[16:31]\n\t" \
  "ds_read_b64_tr_b16 v[132:133], v177 offset:0\n\t" \
  "ds_read_b64_tr_b16 v[134:135], v177 offset:1024\n\t" \
  "s_add_u32 s40, %7, 3538944\n\t" \
  "s_add_u32 m0, %14, 98304\n\t" \
  "s_nop 0\n\t" \
  "buffer_load_dwordx4 v185, %0, s40 offen lds\n\t" \
  "v_rcp_f32 v94, v94\n\t" \
  "v_mfma_f32_32x32x16_bf16 v[32:47], v[152:155], a[136:139], v[32:47]\n\t" \
  "ds_read_b64_tr_b16 v[136:137], v176 offset:24576\n\t" \
  "ds_read_b64_tr_b16 v[138:139], v176 offset:25600\n\t" \
  "buffer_load_dwordx4 v186, %0, s40 offen offset:1024 lds\n\t" \
  "v_rcp_f32 v95, v95\n\t" \
  "v_mul_f32 v124, v124, v92\n\t" \
  "v_mfma_f32_32x32x16_bf16 v[48:63], v[156:159], a[136:139], v[48:63]\n\t" \
  "ds_read_b64_tr_b16 v[140:141], v177 offset:24576\n\t" \
  "ds_read_b64_tr_b16 v[142:143], v177 offset:25600\n\t" \
  "v_mul_f32 v125, v125, v93\n\t" \
  "v_mul_f32 v126, v126, v94\n\t" \
  "s_waitcnt lgkmcnt(8)\n\t" \
  "v_mfma_f32_32x32x16_bf16 v[0:15], v[160:163], a[140:143], v[0:15]\n\t" \
  "ds_read_b64_tr_b16 v[144:145], v176 offset:2048\n\t" \
  "ds_read_b64_tr_b16 v[146:147], v176 offset:3072\n\t" \
  "v_mul_f32 v127, v127, v95\n\t" \
  "v_mfma_f32_32x32x16_bf16 v[16:31], v[164:167], a[140:143], v[16:31]\n\t" \
  "ds_read_b64_tr_b16 v[148:149], v177 offset:2048\n\t" \
  "ds_read_b64_tr_b16 v[150:151], v177 offset:3072\n\t" \
  "v_cvt_pk_bf16_f32 v92, v124, v125\n\t" \
  "v_cvt_pk_bf16_f32 v93, v126, v127\n\t" \
  "v_mfma_f32_32x32x16_bf16 v[32:47], v[168:171], a[140:143], v[32:47]\n\t" \
  "ds_read_b64_tr_b16 v[152:153], v176 offset:26624\n\t" \
  "ds_read_b64_tr_b16 v[154:155], v176 offset:27648\n\t" \
  "buffer_load_dwordx4 v187, %0, s40 offen offset:2048 lds\n\t" \
  "v_accvgpr_write_b32 a174, v92\n\t" \
  "v_mfma_f32_32x32x16_bf16 v[48:63], v[172:175], a[140:143], v[48:63]\n\t" \
  "ds_read_b64_tr_b16 v[156:157], v177 offset:26624\n\t" \
  "ds_read_b64_tr_b16 v[158:159], v177 offset:27648\n\t" \
  "v_accvgpr_write_b32 a175, v93\n\t" \
  "s_waitcnt lgkmcnt(8)\n\t" \
  "v_mfma_f32_32x32x16_bf16 v[64:79], v[128:131], a[0:3], 0\n\t" \
  "ds_read_b64_tr_b16 v[160:161], v176 offset:4096\n\t" \
  "ds_read_b64_tr_b16 v[162:163], v176 offset:5120\n\t" \
  "v_mfma_f32_32x32x16_bf16 v[80:95], v[132:135], a[0:3], 0\n\t" \
  "ds_read_b64_tr_b16 v[164:165], v177 offset:4096\n\t" \
  "ds_read_b64_tr_b16 v[166:167], v177 offset:5120\n\t" \
  "v_mfma_f32_32x32x16_bf16 v[96:111], v[136:139], a[0:3], 0\n\t" \
  "ds_read_b64_tr_b16 v[168:169], v176 offset:28672\n\t" \
  "ds_read_b64_tr_b16 v[170:171], v176 offset:29696\n\t" \
  "buffer_load_dwordx4 v188, %0, s40 offen offset:3072 lds\n\t" \
  "v_mfma_f32_32x32x16_bf16 v[112:127], v[140:143], a[0:3], 0\n\t" \
  "ds_read_b64_tr_b16 v[172:173], v177 offset:28672\n\t" \
  "ds_read_b64_tr_b16 v[174:175], v177 offset:29696\n\t" \
  "v_mul_f32 v32, v0, v32\n\t" \
  "v_mul_f32 v33, v1, v33\n\t" \
  "v_mul_f32 v34, v2, v34\n\t" \
  "s_waitcnt lgkmcnt(8)\n\t" \
  "v_mfma_f32_32x32x16_bf16 v[64:79], v[144:147], a[4:7], v[64:79]\n\t" \
  "ds_read_b64_tr_b16 v[128:129], v176 offset:6144\n\t" \
  "ds_read_b64_tr_b16 v[130:131], v176 offset:7168\n\t" \
  "s_add_u32 s40, %7, 3833856\n\t" \
  "s_add_u32 m0, %14, 102400\n\t" \
  "v_mul_f32 v35, v3, v35\n\t" \
  "v_mfma_f32_32x32x16_bf16 v[80:95], v[148:151], a[4:7], v[80:95]\n\t" \
  "ds_read_b64_tr_b16 v[132:133], v177 offset:6144\n\t" \
  "ds_read_b64_tr_b16 v[134:135], v177 offset:7168\n\t" \
  "buffer_load_dwordx4 v185, %0, s40 offen lds\n\t" \
  "v_mul_f32 v0, v196, v0\n\t" \
  "v_mul_f32 v1, v196, v1\n\t" \
  "v_mfma_f32_32x32x16_bf16 v[96:111], v[152:155], a[4:7], v[96:111]\n\t" \
  "ds_read_b64_tr_b16 v[136:137], v176 offset:30720\n\t" \
  "ds_read_b64_tr_b16 v[138:139], v176 offset:31744\n\t" \
  "v_mul_f32 v2, v196, v2\n\t" \
  "v_mfma_f32_32x32x16_bf16 v[112:127], v[156:159], a[4:7], v[112:127]\n\t" \
  "ds_read_b64_tr_b16 v[140:141], v177 offset:30720\n\t" \
  "ds_read_b64_tr_b16 v[142:143], v177 offset:31744\n\t" \
  "v_mul_f32 v3, v196, v3\n\t" \
  "v_exp_f32 v0, v0\n\t" \
  "s_waitcnt lgkmcnt(8)\n\t" \
  "v_mfma_f32_32x32x16_bf16 v[64:79], v[160:163], a[8:11], v[64:79]\n\t" \
  "ds_read_b64_tr_b16 v[144:145], v176 offset:8192\n\t" \
  "ds_read_b64_tr_b16 v[146:147], v176 offset:9216\n\t" \
  "v_exp_f32 v1, v1\n\t" \
  "v_exp_f32 v2, v2\n\t" \
  "v_mfma_f32_32x32x16_bf16 v[80:95], v[164:167], a[8:11], v[80:95]\n\t" \
  "ds_read_b64_tr_b16 v[148:149], v177 offset:8192\n\t" \
  "ds_read_b64_tr_b16 v[150:151], v177 offset:9216\n\t" \
  "buffer_load_dwordx4 v186, %0, s40 offen offset:1024 lds\n\t" \
  "v_exp_f32 v3, v3\n\t" \
  "v_mfma_f32_32x32x16_bf16 v[96:111], v[168:171], a[8:11], v[96:111]\n\t" \
  "ds_read_b64_tr_b16 v[152:153], v176 offset:32768\n\t" \
  "ds_read_b64_tr_b16 v[154:155], v176 offset:33792\n\t" \
  "v_fma_f32 v0, v0, v197, v197\n\t" \
  "v_fma_f32 v1, v1, v197, v197\n\t" \
  "v_mfma_f32_32x32x16_bf16 v[112:127], v[172:175], a[8:11], v[112:127]\n\t" \
  "ds_read_b64_tr_b16 v[156:157], v177 offset:32768\n\t" \
  "ds_read_b64_tr_b16 v[158:159], v177 offset:33792\n\t" \
  "v_fma_f32 v2, v2, v197, v197\n\t" \
  "s_waitcnt lgkmcnt(8)\n\t" \
  "v_mfma_f32_32x32x16_bf16 v[64:79], v[128:131], a[12:15], v[64:79]\n\t" \
  "ds_read_b64_tr_b16 v[160:161], v176 offset:10240\n\t" \
  "ds_read_b64_tr_b16 v[162:163], v176 offset:11264\n\t" \
  "v_fma_f32 v3, v3, v197, v197\n\t" \
  "v_rcp_f32 v0, v0\n\t" \
  "v_mfma_f32_32x32x16_bf16 v[80:95], v[132:135], a[12:15], v[80:95]\n\t" \
  "ds_read_b64_tr_b16 v[164:165], v177 offset:10240\n\t" \
  "ds_read_b64_tr_b16 v[166:167], v177 offset:11264\n\t" \
  "buffer_load_dwordx4 v187, %0, s40 offen offset:2048 lds\n\t" \
  "v_rcp_f32 v1, v1\n\t" \
  "v_mfma_f32_32x32x16_bf16 v[96:111], v[136:139], a[12:15], v[96:111]\n\t" \
  "ds_read_b64_tr_b16 v[168:169], v176 offset:34816\n\t" \
  "ds_read_b64_tr_b16 v[170:171], v176 offset:35840\n\t" \
  "buffer_load_dwordx4 v188, %0, s40 offen offset:3072 lds\n\t" \
  "v_rcp_f32 v2, v2\n\t" \
  "v_rcp_f32 v3, v3\n\t" \
  "v_mfma_f32_32x32x16_bf16 v[112:127], v[140:143], a[12:15], v[112:127]\n\t" \
  "ds_read_b64_tr_b16 v[172:173], v177 offset:34816\n\t" \
  "ds_read_b64_tr_b16 v[174:175], v177 offset:35840\n\t" \
  "v_mul_f32 v32, v32, v0\n\t" \
  "v_mul_f32 v33, v33, v1\n\t" \
  "s_waitcnt lgkmcnt(8)\n\t" \
  "v_mfma_f32_32x32x16_bf16 v[64:79], v[144:147], a[16:19], v[64:79]\n\t" \
  "ds_read_b64_tr_b16 v[128:129], v176 offset:12288\n\t" \
  "ds_read_b64_tr_b16 v[130:131], v176 offset:13312\n\t" \
  "v_mul_f32 v34, v34, v2\n\t" \
  "v_mfma_f32_32x32x16_bf16 v[80:95], v[148:151], a[16:19], v[80:95]\n\t" \
  "ds_read_b64_tr_b16 v[132:133], v177 offset:12288\n\t" \
  "ds_read_b64_tr_b16 v[134:135], v177 offset:13312\n\t" \
  "s_add_u32 s40, %7, 4128768\n\t" \
  "s_add_u32 m0, %14, 106496\n\t" \
  "v_mul_f32 v35, v35, v3\n\t" \
  "v_cvt_pk_bf16_f32 v0, v32, v33\n\t" \
  "v_mfma_f32_32x32x16_bf16 v[96:111], v[152:155], a[16:19], v[96:111]\n\t" \
  "ds_read_b64_tr_b16 v[136:137], v176 offset:36864\n\t" \
  "ds_read_b64_tr_b16 v[138:139], v176 offset:37888\n\t" \
  "buffer_load_dwordx4 v185, %0, s40 offen lds\n\t" \
  "v_cvt_pk_bf16_f32 v1, v34, v35\n\t" \
  "v_mfma_f32_32x32x16_bf16 v[112:127], v[156:159], a[16:19], v[112:127]\n\t" \
  "ds_read_b64_tr_b16 v[140:141], v177 offset:36864\n\t" \
  "ds_read_b64_tr_b16 v[142:143], v177 offset:37888\n\t" \
  "v_accvgpr_write_b32 a176, v0\n\t" \
  "v_accvgpr_write_b32 a177, v1\n\t" \
  "s_waitcnt lgkmcnt(8)\n\t" \
  "v_mfma_f32_32x32x16_bf16 v[64:79], v[160:163], a[20:23], v[64:79]\n\t" \
  "ds_read_b64_tr_b16 v[144:145], v176 offset:14336\n\t" \
  "ds_read_b64_tr_b16 v[146:147], v176 offset:15360\n\t" \
  "v_mul_f32 v36, v4, v36\n\t" \
  "v_mul_f32 v37, v5, v37\n\t" \
  "v_mfma_f32_32x32x16_bf16 v[80:95], v[164:167], a[20:23], v[80:95]\n\t" \
  "ds_read_b64_tr_b16 v[148:149], v177 offset:14336\n\t" \
  "ds_read_b64_tr_b16 v[150:151], v177 offset:15360\n\t" \
  "v_mul_f32 v38, v6, v38\n\t" \
  "v_mfma_f32_32x32x16_bf16 v[96:111], v[168:171], a[20:23], v[96:111]\n\t" \
  "ds_read_b64_tr_b16 v[152:153], v176 offset:38912\n\t" \
  "ds_read_b64_tr_b16 v[154:155], v176 offset:39936\n\t" \
  "buffer_load_dwordx4 v186, %0, s40 offen offset:1024 lds\n\t" \
  "v_mul_f32 v39, v7, v39\n\t" \
  "v_mul_f32 v4, v196, v4\n\t" \
  "v_mfma_f32_32x32x16_bf16 v[112:127], v[172:175], a[20:23], v[112:127]\n\t" \
  "ds_read_b64_tr_b16 v[156:157], v177 offset:38912\n\t" \
  "ds_read_b64_tr_b16 v[158:159], v177 offset:39936\n\t" \
  "v_mul_f32 v5, v196, v5\n\t" \
  "s_waitcnt lgkmcnt(8)\n\t" \
  "v_mfma_f32_32x32x16_bf16 v[64:79], v[128:131], a[24:27], v[64:79]\n\t" \
  "ds_read_b64_tr_b16 v[160:161], v176 offset:16384\n\t" \
  "ds_read_b64_tr_b16 v[162:163], v176 offset:17408\n\t" \
  "v_mul_f32 v6, v196, v6\n\t" \
  "v_mul_f32 v7, v196, v7\n\t" \
  "v_mfma_f32_32x32x16_bf16 v[80:95], v[132:135], a[24:27], v[80:95]\n\t" \
  "ds_read_b64_tr_b16 v[164:165], v177 offset:16384\n\t" \
  "ds_read_b64_tr_b16 v[166:167], v177 offset:17408\n\t" \
  "buffer_load_dwordx4 v187, %0, s40 offen offset:2048 lds\n\t" \
  "v_exp_f32 v4, v4\n\t" \
  "v_exp_f32 v5, v5\n\t" \
  "v_mfma_f32_32x32x16_bf16 v[96:111], v[136:139], a[24:27], v[96:111]\n\t" \
  "ds_read_b64_tr_b16 v[168:169], v176 offset:40960\n\t" \
  "ds_read_b64_tr_b16 v[170:171], v176 offset:41984\n\t" \
  "v_exp_f32 v6, v6\n\t" \
  "v_mfma_f32_32x32x16_bf16 v[112:127], v[140:143], a[24:27], v[112:127]\n\t" \
  "ds_read_b64_tr_b16 v[172:173], v177 offset:40960\n\t" \
  "ds_read_b64_tr_b16 v[174:175], v177 offset:41984\n\t" \
  "v_exp_f32 v7, v7\n\t" \
  "v_fma_f32 v4, v4, v197, v197\n\t" \
  "s_waitcnt lgkmcnt(8)\n\t" \
  "v_mfma_f32_32x32x16_bf16 v[64:79], v[144:147], a[28:31], v[64:79]\n\t" \
  "ds_read_b64_tr_b16 v[128:129], v176 offset:18432\n\t" \
  "ds_read_b64_tr_b16 v[130:131], v176 offset:19456\n\t" \
  "v_fma_f32 v5, v5, v197, v197\n\t" \
  "v_mfma_f32_32x32x16_bf16 v[80:95], v[148:151], a[28:31], v[80:95]\n\t" \
  "ds_read_b64_tr_b16 v[132:133], v177 offset:18432\n\t" \
  "ds_read_b64_tr_b16 v[134:135], v177 offset:19456\n\t" \
  "buffer_load_dwordx4 v188, %0, s40 offen offset:3072 lds\n\t" \
  "v_fma_f32 v6, v6, v197, v197\n\t" \
  "v_fma_f32 v7, v7, v197, v197\n\t" \
  "v_mfma_f32_32x32x16_bf16 v[96:111], v[152:155], a[28:31], v[96:111]\n\t" \
  "ds_read_b64_tr_b16 v[136:137], v176 offset:43008\n\t" \
  "ds_read_b64_tr_b16 v[138:139], v176 offset:44032\n\t" \
  "v_rcp_f32 v4, v4\n\t" \
  "v_mfma_f32_32x32x16_bf16 v[112:127], v[156:159], a[28:31], v[112:127]\n\t" \
  "ds_read_b64_tr_b16 v[140:141], v177 offset:43008\n\t" \
  "ds_read_b64_tr_b16 v[142:143], v177 offset:44032\n\t" \
  "v_rcp_f32 v5, v5\n\t" \
  "v_rcp_f32 v6, v6\n\t" \
  "s_waitcnt lgkmcnt(8)\n\t" \
  "v_mfma_f32_32x32x16_bf16 v[64:79], v[160:163], a[32:35], v[64:79]\n\t" \
  "ds_read_b64_tr_b16 v[144:145], v176 offset:20480\n\t" \
  "ds_read_b64_tr_b16 v[146:147], v176 offset:21504\n\t" \
  "v_rcp_f32 v7, v7\n\t" \
  "v_mul_f32 v36, v36, v4\n\t" \
  "v_mfma_f32_32x32x16_bf16 v[80:95], v[164:167], a[32:35], v[80:95]\n\t" \
  "ds_read_b64_tr_b16 v[148:149], v177 offset:20480\n\t" \
  "ds_read_b64_tr_b16 v[150:151], v177 offset:21504\n\t" \
  "v_mul_f32 v37, v37, v5\n\t" \
  "v_mfma_f32_32x32x16_bf16 v[96:111], v[168:171], a[32:35], v[96:111]\n\t" \
  "ds_read_b64_tr_b16 v[152:153], v176 offset:45056\n\t" \
  "ds_read_b64_tr_b16 v[154:155], v176 offset:46080\n\t" \
  "v_mul_f32 v38, v38, v6\n\t" \
  "v_mul_f32 v39, v39, v7\n\t" \
  "v_mfma_f32_32x32x16_bf16 v[112:127], v[172:175], a[32:35], v[112:127]\n\t" \
  "ds_read_b64_tr_b16 v[156:157], v177 offset:45056\n\t" \
  "ds_read_b64_tr_b16 v[158:159], v177 offset:46080\n\t" \
  "v_cvt_pk_bf16_f32 v4, v36, v37\n\t" \
  "s_waitcnt lgkmcnt(8)\n\t" \
  "v_mfma_f32_32x32x16_bf16 v[64:79], v[128:131], a[36:39], v[64:79]\n\t" \
  "ds_read_b64_tr_b16 v[160:161], v176 offset:22528\n\t" \
  "ds_read_b64_tr_b16 v[162:163], v176 offset:23552\n\t" \
  "ds_read_b64_tr_b16 v[164:165], v177 offset:22528\n\t" \
  "ds_read_b64_tr_b16 v[166:167], v177 offset:23552\n\t" \
  "v_cvt_pk_bf16_f32 v5, v38, v39\n\t" \
  "v_accvgpr_write_b32 a178, v4\n\t" \
  "v_mfma_f32_32x32x16_bf16 v[80:95], v[132:135], a[36:39], v[80:95]\n\t" \
  "ds_read_b64_tr_b16 v[168:169], v176 offset:47104\n\t" \
  "ds_read_b64_tr_b16 v[170:171], v176 offset:48128\n\t" \
  "ds_read_b64_tr_b16 v[172:173], v177 offset:47104\n\t" \
  "ds_read_b64_tr_b16 v[174:175], v177 offset:48128\n\t" \
  "v_accvgpr_write_b32 a179, v5\n\t" \
  "v_mul_f32 v40, v8, v40\n\t" \
  "v_mfma_f32_32x32x16_bf16 v[96:111], v[136:139], a[36:39], v[96:111]\n\t" \
  "v_mul_f32 v41, v9, v41\n\t" \
  "v_mfma_f32_32x32x16_bf16 v[112:127], v[140:143], a[36:39], v[112:127]\n\t" \
  "v_mul_f32 v42, v10, v42\n\t" \
  "v_mul_f32 v43, v11, v43\n\t" \
  "s_waitcnt lgkmcnt(8)\n\t" \
  "v_mfma_f32_32x32x16_bf16 v[64:79], v[144:147], a[40:43], v[64:79]\n\t" \
  "s_waitcnt vmcnt(12) lgkmcnt(0)\n\ts_barrier\n\t" \
  "ds_read_b64_tr_b16 v[128:129], v178 offset:0\n\t" \
  "ds_read_b64_tr_b16 v[130:131], v178 offset:1024\n\t" \
  "v_mul_f32 v8, v196, v8\n\t" \
  "v_mfma_f32_32x32x16_bf16 v[80:95], v[148:151], a[40:43], v[80:95]\n\t" \
  "ds_read_b64_tr_b16 v[132:133], v179 offset:0\n\t" \
  "ds_read_b64_tr_b16 v[134:135], v179 offset:1024\n\t" \
  "s_add_u32 s40, %8, 0\n\t" \
  "s_add_u32 m0, %15, 0\n\t" \
  "s_nop 0\n\t" \
  "buffer_load_dwordx4 v189, %0, s40 offen lds\n\t" \
  "v_mul_f32 v9, v196, v9\n\t" \
  "v_mul_f32 v10, v196, v10\n\t" \
  "v_mfma_f32_32x32x16_bf16 v[96:111], v[152:155], a[40:43], v[96:111]\n\t" \
  "ds_read_b64_tr_b16 v[136:137], v178 offset:24576\n\t" \
  "ds_read_b64_tr_b16 v[138:139], v178 offset:25600\n\t" \
  "v_mul_f32 v11, v196, v11\n\t" \
  "v_exp_f32 v8, v8\n\t" \
  "v_mfma_f32_32x32x16_bf16 v[112:127], v[156:159], a[40:43], v[112:127]\n\t" \
  "ds_read_b64_tr_b16 v[140:141], v179 offset:24576\n\t" \
  "ds_read_b64_tr_b16 v[142:143], v179 offset:25600\n\t" \
  "v_exp_f32 v9, v9\n\t" \
  "s_waitcnt lgkmcnt(8)\n\t" \
  "v_mfma_f32_32x32x16_bf16 v[64:79], v[160:163], a[44:47], v[64:79]\n\t" \
  "ds_read_b64_tr_b16 v[144:145], v178 offset:2048\n\t" \
  "ds_read_b64_tr_b16 v[146:147], v178 offset:3072\n\t" \
  "v_exp_f32 v10, v10\n\t" \
  "v_exp_f32 v11, v11\n\t" \
  "v_mfma_f32_32x32x16_bf16 v[80:95], v[164:167], a[44:47], v[80:95]\n\t" \
  "ds_read_b64_tr_b16 v[148:149], v179 offset:2048\n\t" \
  "ds_read_b64_tr_b16 v[150:151], v179 offset:3072\n\t" \
  "v_fma_f32 v8, v8, v197, v197\n\t" \
  "v_mfma_f32_32x32x16_bf16 v[96:111], v[168:171], a[44:47], v[96:111]\n\t" \
  "ds_read_b64_tr_b16 v[152:153], v178 offset:26624\n\t" \
  "ds_read_b64_tr_b16 v[154:155], v178 offset:27648\n\t" \
  "buffer_load_dwordx4 v190, %0, s40 offen offset:1024 lds\n\t" \
  "v_fma_f32 v9, v9, v197, v197\n\t" \
  "v_fma_f32 v10, v10, v197, v197\n\t" \
  "v_mfma_f32_32x32x16_bf16 v[112:127], v[172:175], a[44:47], v[112:127]\n\t" \
  "ds_read_b64_tr_b16 v[156:157], v179 offset:26624\n\t" \
  "ds_read_b64_tr_b16 v[158:159], v179 offset:27648\n\t" \
  "v_fma_f32 v11, v11, v197, v197\n\t" \
  "s_waitcnt lgkmcnt(8)\n\t" \
  "v_mfma_f32_32x32x16_bf16 v[64:79], v[128:131], a[48:51], v[64:79]\n\t" \
  "ds_read_b64_tr_b16 v[160:161], v178 offset:4096\n\t" \
  "ds_read_b64_tr_b16 v[162:163], v178 offset:5120\n\t" \
  "v_rcp_f32 v8, v8\n\t" \
  "v_rcp_f32 v9, v9\n\t" \
  "v_mfma_f32_32x32x16_bf16 v[80:95], v[132:135], a[48:51], v[80:95]\n\t" \
  "ds_read_b64_tr_b16 v[164:165], v179 offset:4096\n\t" \
  "ds_read_b64_tr_b16 v[166:167], v179 offset:5120\n\t" \
  "v_rcp_f32 v10, v10\n\t" \
  "v_rcp_f32 v11, v11\n\t" \
  "v_mfma_f32_32x32x16_bf16 v[96:111], v[136:139], a[48:51], v[96:111]\n\t" \
  "ds_read_b64_tr_b16 v[168:169], v178 offset:28672\n\t" \
  "ds_read_b64_tr_b16 v[170:171], v178 offset:29696\n\t" \
  "v_mul_f32 v40, v40, v8\n\t" \
  "v_mfma_f32_32x32x16_bf16 v[112:127], v[140:143], a[48:51], v[112:127]\n\t" \
  "ds_read_b64_tr_b16 v[172:173], v179 offset:28672\n\t" \
  "ds_read_b64_tr_b16 v[174:175], v179 offset:29696\n\t" \
  "v_mul_f32 v41, v41, v9\n\t" \
  "v_mul_f32 v42, v42, v10\n\t" \
  "s_waitcnt lgkmcnt(8)\n\t" \
  "v_mfma_f32_32x32x16_bf16 v[64:79], v[144:147], a[52:55], v[64:79]\n\t" \
  "ds_read_b64_tr_b16 v[128:129], v178 offset:6144\n\t" \
  "ds_read_b64_tr_b16 v[130:131], v178 offset:7168\n\t" \
  "v_mul_f32 v43, v43, v11\n\t" \
  "v_mfma_f32_32x32x16_bf16 v[80:95], v[148:151], a[52:55], v[80:95]\n\t" \
  "ds_read_b64_tr_b16 v[132:133], v179 offset:6144\n\t" \
  "ds_read_b64_tr_b16 v[134:135], v179 offset:7168\n\t" \
  "buffer_load_dwordx4 v191, %0, s40 offen offset:2048 lds\n\t" \
  "v_cvt_pk_bf16_f32 v8, v40, v41\n\t" \
  "v_cvt_pk_bf16_f32 v9, v42, v43\n\t" \
  "v_mfma_f32_32x32x16_bf16 v[96:111], v[152:155], a[52:55], v[96:111]\n\t" \
  "ds_read_b64_tr_b16 v[136:137], v178 offset:30720\n\t" \
  "ds_read_b64_tr_b16 v[138:139], v178 offset:31744\n\t" \
  "v_accvgpr_write_b32 a180, v8\n\t" \
  "v_accvgpr_write_b32 a181, v9\n\t" \
  "v_mfma_f32_32x32x16_bf16 v[112:127], v[156:159], a[52:55], v[112:127]\n\t" \
  "ds_read_b64_tr_b16 v[140:141], v179 offset:30720\n\t" \
  "ds_read_b64_tr_b16 v[142:143], v179 offset:31744\n\t" \
  "v_mul_f32 v44, v12, v44\n\t" \
  "s_waitcnt lgkmcnt(8)\n\t" \
  "v_mfma_f32_32x32x16_bf16 v[64:79], v[160:163], a[56:59], v[64:79]\n\t" \
  "ds_read_b64_tr_b16 v[144:145], v178 offset:8192\n\t" \
  "ds_read_b64_tr_b16 v[146:147], v178 offset:9216\n\t" \
  "v_mul_f32 v45, v13, v45\n\t" \
  "v_mul_f32 v46, v14, v46\n\t" \
  "v_mfma_f32_32x32x16_bf16 v[80:95], v[164:167], a[56:59], v[80:95]\n\t" \
  "ds_read_b64_tr_b16 v[148:149], v179 offset:8192\n\t" \
  "ds_read_b64_tr_b16 v[150:151], v179 offset:9216\n\t" \
  "v_mul_f32 v47, v15, v47\n\t" \
  "v_mfma_f32_32x32x16_bf16 v[96:111], v[168:171], a[56:59], v[96:111]\n\t" \
  "ds_read_b64_tr_b16 v[152:153], v178 offset:32768\n\t" \
  "ds_read_b64_tr_b16 v[154:155], v178 offset:33792\n\t" \
  "v_mul_f32 v12, v196, v12\n\t" \
  "v_mul_f32 v13, v196, v13\n\t" \
  "v_mfma_f32_32x32x16_bf16 v[112:127], v[172:175], a[56:59], v[112:127]\n\t" \
  "ds_read_b64_tr_b16 v[156:157], v179 offset:32768\n\t" \
  "ds_read_b64_tr_b16 v[158:159], v179 offset:33792\n\t" \
  "v_mul_f32 v14, v196, v14\n\t" \
  "v_mul_f32 v15, v196, v15\n\t" \
  "s_waitcnt lgkmcnt(8)\n\t" \
  "v_mfma_f32_32x32x16_bf16 v[64:79], v[128:131], a[60:63], v[64:79]\n\t" \
  "ds_read_b64_tr_b16 v[160:161], v178 offset:10240\n\t" \
  "ds_read_b64_tr_b16 v[162:163], v178 offset:11264\n\t" \
  "v_exp_f32 v12, v12\n\t" \
  "v_mfma_f32_32x32x16_bf16 v[80:95], v[132:135], a[60:63], v[80:95]\n\t" \
  "ds_read_b64_tr_b16 v[164:165], v179 offset:10240\n\t" \
  "ds_read_b64_tr_b16 v[166:167], v179 offset:11264\n\t" \
  "buffer_load_dwordx4 v192, %0, s40 offen offset:3072 lds\n\t" \
  "v_exp_f32 v13, v13\n\t" \
  "v_exp_f32 v14, v14\n\t" \
  "v_mfma_f32_32x32x16_bf16 v[96:111], v[136:139], a[60:63], v[96:111]\n\t" \
  "ds_read_b64_tr_b16 v[168:169], v178 offset:34816\n\t" \
  "ds_read_b64_tr_b16 v[170:171], v178 offset:35840\n\t" \
  "v_exp_f32 v15, v15\n\t" \
  "v_mfma_f32_32x32x16_bf16 v[112:127], v[140:143], a[60:63], v[112:127]\n\t" \
  "ds_read_b64_tr_b16 v[172:173], v179 offset:34816\n\t" \
  "ds_read_b64_tr_b16 v[174:175], v179 offset:35840\n\t" \
  "v_fma_f32 v12, v12, v197, v197\n\t" \
  "v_fma_f32 v13, v13, v197, v197\n\t" \
  "s_waitcnt lgkmcnt(8)\n\t" \
  "v_mfma_f32_32x32x16_bf16 v[64:79], v[144:147], a[64:67], v[64:79]\n\t" \
  "ds_read_b64_tr_b16 v[128:129], v178 offset:12288\n\t" \
  "ds_read_b64_tr_b16 v[130:131], v178 offset:13312\n\t" \
  "v_fma_f32 v14, v14, v197, v197\n\t" \
  "v_mfma_f32_32x32x16_bf16 v[80:95], v[148:151], a[64:67], v[80:95]\n\t" \
  "ds_read_b64_tr_b16 v[132:133], v179 offset:12288\n\t" \
  "ds_read_b64_tr_b16 v[134:135], v179 offset:13312\n\t" \
  "s_add_u32 s40, %8, 589824\n\t" \
  "s_add_u32 m0, %15, 4096\n\t" \
  "v_fma_f32 v15, v15, v197, v197\n\t" \
  "v_rcp_f32 v12, v12\n\t" \
  "v_mfma_f32_32x32x16_bf16 v[96:111], v[152:155], a[64:67], v[96:111]\n\t" \
  "ds_read_b64_tr_b16 v[136:137], v178 offset:36864\n\t" \
  "ds_read_b64_tr_b16 v[138:139], v178 offset:37888\n\t" \
  "buffer_load_dwordx4 v189, %0, s40 offen lds\n\t" \
  "v_rcp_f32 v13, v13\n\t" \
  "v_rcp_f32 v14, v14\n\t" \
  "v_mfma_f32_32x32x16_bf16 v[112:127], v[156:159], a[64:67], v[112:127]\n\t" \
  "ds_read_b64_tr_b16 v[140:141], v179 offset:36864\n\t" \
  "ds_read_b64_tr_b16 v[142:143], v179 offset:37888\n\t" \
  "v_rcp_f32 v15, v15\n\t" \
  "s_waitcnt lgkmcnt(8)\n\t" \
  "v_mfma_f32_32x32x16_bf16 v[64:79], v[160:163], a[68:71], v[64:79]\n\t" \
  "ds_read_b64_tr_b16 v[144:145], v178 offset:14336\n\t" \
  "ds_read_b64_tr_b16 v[146:147], v178 offset:15360\n\t" \
  "v_mul_f32 v44, v44, v12\n\t" \
  "v_mul_f32 v45, v45, v13\n\t" \
  "v_mfma_f32_32x32x16_bf16 v[80:95], v[164:167], a[68:71], v[80:95]\n\t" \
  "ds_read_b64_tr_b16 v[148:149], v179 offset:14336\n\t" \
  "ds_read_b64_tr_b16 v[150:151], v179 offset:15360\n\t" \
  "v_mul_f32 v46, v46, v14\n\t" \
  "v_mfma_f32_32x32x16_bf16 v[96:111], v[168:171], a[68:71], v[96:111]\n\t" \
  "ds_read_b64_tr_b16 v[152:153], v178 offset:38912\n\t" \
  "ds_read_b64_tr_b16 v[154:155], v178 offset:39936\n\t" \
  "v_mul_f32 v47, v47, v15\n\t" \
  "v_cvt_pk_bf16_f32 v12, v44, v45\n\t" \
  "v_mfma_f32_32x32x16_bf16 v[112:127], v[172:175], a[68:71], v[112:127]\n\t" \
  "ds_read_b64_tr_b16 v[156:157], v179 offset:38912\n\t" \
  "ds_read_b64_tr_b16 v[158:159], v179 offset:39936\n\t" \
  "v_cvt_pk_bf16_f32 v13, v46, v47\n\t" \
  "v_accvgpr_write_b32 a182, v12\n\t" \
  "s_waitcnt lgkmcnt(8)\n\t" \
  "v_mfma_f32_32x32x16_bf16 v[64:79], v[128:131], a[72:75], v[64:79]\n\t" \
  "ds_read_b64_tr_b16 v[160:161], v178 offset:16384\n\t" \
  "ds_read_b64_tr_b16 v[162:163], v178 offset:17408\n\t" \
  "v_accvgpr_write_b32 a183, v13\n\t" \
  "v_mfma_f32_32x32x16_bf16 v[80:95], v[132:135], a[72:75], v[80:95]\n\t" \
  "ds_read_b64_tr_b16 v[164:165], v179 offset:16384\n\t" \
  "ds_read_b64_tr_b16 v[166:167], v179 offset:17408\n\t" \
  "buffer_load_dwordx4 v190, %0, s40 offen offset:1024 lds\n\t" \
  "v_mul_f32 v48, v16, v48\n\t" \
  "v_mul_f32 v49, v17, v49\n\t" \
  "v_mfma_f32_32x32x16_bf16 v[96:111], v[136:139], a[72:75], v[96:111]\n\t" \
  "ds_read_b64_tr_b16 v[168:169], v178 offset:40960\n\t" \
  "ds_read_b64_tr_b16 v[170:171], v178 offset:41984\n\t" \
  "v_mul_f32 v50, v18, v50\n\t" \
  "v_mfma_f32_32x32x16_bf16 v[112:127], v[140:143], a[72:75], v[112:127]\n\t" \
  "ds_read_b64_tr_b16 v[172:173], v179 offset:40960\n\t" \
  "ds_read_b64_tr_b16 v[174:175], v179 offset:41984\n\t" \
  "v_mul_f32 v51, v19, v51\n\t" \
  "v_mul_f32 v16, v196, v16\n\t" \
  "s_waitcnt lgkmcnt(8)\n\t" \
  "v_mfma_f32_32x32x16_bf16 v[64:79], v[144:147], a[76:79], v[64:79]\n\t" \
  "ds_read_b64_tr_b16 v[128:129], v178 offset:18432\n\t" \
  "ds_read_b64_tr_b16 v[130:131], v178 offset:19456\n\t" \
  "v_mul_f32 v17, v196, v17\n\t" \
  "v_mul_f32 v18, v196, v18\n\t" \
  "v_mfma_f32_32x32x16_bf16 v[80:95], v[148:151], a[76:79], v[80:95]\n\t" \
  "ds_read_b64_tr_b16 v[132:133], v179 offset:18432\n\t" \
  "ds_read_b64_tr_b16 v[134:135], v179 offset:19456\n\t" \
  "v_mul_f32 v19, v196, v19\n\t" \
  "v_mfma_f32_32x32x16_bf16 v[96:111], v[152:155], a[76:79], v[96:111]\n\t" \
  "ds_read_b64_tr_b16 v[136:137], v178 offset:43008\n\t" \
  "ds_read_b64_tr_b16 v[138:139], v178 offset:44032\n\t" \
  "v_exp_f32 v16, v16\n\t" \
  "v_exp_f32 v17, v17\n\t" \
  "v_mfma_f32_32x32x16_bf16 v[112:127], v[156:159], a[76:79], v[112:127]\n\t" \
  "ds_read_b64_tr_b16 v[140:141], v179 offset:43008\n\t" \
  "ds_read_b64_tr_b16 v[142:143], v179 offset:44032\n\t" \
  "v_exp_f32 v18, v18\n\t" \
  "s_waitcnt lgkmcnt(8)\n\t" \
  "v_mfma_f32_32x32x16_bf16 v[64:79], v[160:163], a[80:83], v[64:79]\n\t" \
  "ds_read_b64_tr_b16 v[144:145], v178 offset:20480\n\t" \
  "ds_read_b64_tr_b16 v[146:147], v178 offset:21504\n\t" \
  "v_exp_f32 v19, v19\n\t" \
  "v_fma_f32 v16, v16, v197, v197\n\t" \
  "v_mfma_f32_32x32x16_bf16 v[80:95], v[164:167], a[80:83], v[80:95]\n\t" \
  "ds_read_b64_tr_b16 v[148:149], v179 offset:20480\n\t" \
  "ds_read_b64_tr_b16 v[150:151], v179 offset:21504\n\t" \
  "v_fma_f32 v17, v17, v197, v197\n\t" \
  "v_fma_f32 v18, v18, v197, v197\n\t" \
  "v_mfma_f32_32x32x16_bf16 v[96:111], v[168:171], a[80:83], v[96:111]\n\t" \
  "ds_read_b64_tr_b16 v[152:153], v178 offset:45056\n\t" \
  "ds_read_b64_tr_b16 v[154:155], v178 offset:46080\n\t" \
  "v_fma_f32 v19, v19, v197, v197\n\t" \
  "v_mfma_f32_32x32x16_bf16 v[112:127], v[172:175], a[80:83], v[112:127]\n\t" \
  "ds_read_b64_tr_b16 v[156:157], v179 offset:45056\n\t" \
  "ds_read_b64_tr_b16 v[158:159], v179 offset:46080\n\t" \
  "v_rcp_f32 v16, v16\n\t" \
  "v_rcp_f32 v17, v17\n\t" \
  "s_waitcnt lgkmcnt(8)\n\t" \
  "v_mfma_f32_32x32x16_bf16 v[64:79], v[128:131], a[84:87], v[64:79]\n\t" \
  "ds_read_b64_tr_b16 v[160:161], v178 offset:22528\n\t" \
  "ds_read_b64_tr_b16 v[162:163], v178 offset:23552\n\t" \
  "ds_read_b64_tr_b16 v[164:165], v179 offset:22528\n\t" \
  "ds_read_b64_tr_b16 v[166:167], v179 offset:23552\n\t" \
  "v_rcp_f32 v18, v18\n\t" \
  "v_mfma_f32_32x32x16_bf16 v[80:95], v[132:135], a[84:87], v[80:95]\n\t" \
  "ds_read_b64_tr_b16 v[168:169], v178 offset:47104\n\t" \
  "ds_read_b64_tr_b16 v[170:171], v178 offset:48128\n\t" \
  "ds_read_b64_tr_b16 v[172:173], v179 offset:47104\n\t" \
  "ds_read_b64_tr_b16 v[174:175], v179 offset:48128\n\t" \
  "v_rcp_f32 v19, v19\n\t" \
  "v_mul_f32 v48, v48, v16\n\t" \
  "v_mfma_f32_32x32x16_bf16 v[96:111], v[136:139], a[84:87], v[96:111]\n\t" \
  "v_mul_f32 v49, v49, v17\n\t" \
  "v_mfma_f32_32x32x16_bf16 v[112:127], v[140:143], a[84:87], v[112:127]\n\t" \
  "v_mul_f32 v50, v50, v18\n\t" \
  "v_mul_f32 v51, v51, v19\n\t" \
  "s_waitcnt lgkmcnt(8)\n\t" \
  "v_mfma_f32_32x32x16_bf16 v[64:79], v[144:147], a[88:91], v[64:79]\n\t" \
  "s_waitcnt vmcnt(6) lgkmcnt(0)\n\ts_barrier\n\t" \
  "ds_read_b64_tr_b16 v[128:129], v180 offset:0\n\t" \
  "ds_read_b64_tr_b16 v[130:131], v180 offset:1024\n\t" \
  "v_cvt_pk_bf16_f32 v16, v48, v49\n\t" \
  "v_cvt_pk_bf16_f32 v17, v50, v51\n\t" \
  "v_mfma_f32_32x32x16_bf16 v[80:95], v[148:151], a[88:91], v[80:95]\n\t" \
  "ds_read_b64_tr_b16 v[132:133], v181 offset:0\n\t" \
  "ds_read_b64_tr_b16 v[134:135], v181 offset:1024\n\t" \
  "s_add_u32 s40, %8, 1769472\n\t" \
  "s_add_u32 m0, %15, 49152\n\t" \
  "s_nop 0\n\t" \
  "buffer_load_dwordx4 v189, %0, s40 offen lds\n\t" \
  "v_accvgpr_write_b32 a184, v16\n\t" \
  "v_mfma_f32_32x32x16_bf16 v[96:111], v[152:155], a[88:91], v[96:111]\n\t" \
  "ds_read_b64_tr_b16 v[136:137], v180 offset:24576\n\t" \
  "ds_read_b64_tr_b16 v[138:139], v180 offset:25600\n\t" \
  "v_accvgpr_write_b32 a185, v17\n\t" \
  "v_mul_f32 v52, v20, v52\n\t" \
  "v_mfma_f32_32x32x16_bf16 v[112:127], v[156:159], a[88:91], v[112:127]\n\t" \
  "ds_read_b64_tr_b16 v[140:141], v181 offset:24576\n\t" \
  "ds_read_b64_tr_b16 v[142:143], v181 offset:25600\n\t" \
  "v_mul_f32 v53, v21, v53\n\t" \
  "s_waitcnt lgkmcnt(8)\n\t" \
  "v_mfma_f32_32x32x16_bf16 v[64:79], v[160:163], a[92:95], v[64:79]\n\t" \
  "ds_read_b64_tr_b16 v[144:145], v180 offset:2048\n\t" \
  "ds_read_b64_tr_b16 v[146:147], v180 offset:3072\n\t" \
  "v_mul_f32 v54, v22, v54\n\t" \
  "v_mul_f32 v55, v23, v55\n\t" \
  "v_mfma_f32_32x32x16_bf16 v[80:95], v[164:167], a[92:95], v[80:95]\n\t" \
  "ds_read_b64_tr_b16 v[148:149], v181 offset:2048\n\t" \
  "ds_read_b64_tr_b16 v[150:151], v181 offset:3072\n\t" \
  "v_mul_f32 v20, v196, v20\n\t" \
  "v_mul_f32 v21, v196, v21\n\t" \
  "v_mfma_f32_32x32x16_bf16 v[96:111], v[168:171], a[92:95], v[96:111]\n\t" \
  "ds_read_b64_tr_b16 v[152:153], v180 offset:26624\n\t" \
  "ds_read_b64_tr_b16 v[154:155], v180 offset:27648\n\t" \
  "buffer_load_dwordx4 v190, %0, s40 offen offset:1024 lds\n\t" \
  "v_mul_f32 v22, v196, v22\n\t" \
  "v_mfma_f32_32x32x16_bf16 v[112:127], v[172:175], a[92:95], v[112:127]\n\t" \
  "ds_read_b64_tr_b16 v[156:157], v181 offset:26624\n\t" \
  "ds_read_b64_tr_b16 v[158:159], v181 offset:27648\n\t" \
  "v_mul_f32 v23, v196, v23\n\t" \
  "v_exp_f32 v20, v20\n\t" \
  "s_waitcnt lgkmcnt(8)\n\t" \
  "v_mfma_f32_32x32x16_bf16 v[64:79], v[128:131], a[96:99], v[64:79]\n\t" \
  "ds_read_b64_tr_b16 v[160:161], v180 offset:4096\n\t" \
  "ds_read_b64_tr_b16 v[162:163], v180 offset:5120\n\t" \
  "v_exp_f32 v21, v21\n\t" \
  "v_mfma_f32_32x32x16_bf16 v[80:95], v[132:135], a[96:99], v[80:95]\n\t" \
  "ds_read_b64_tr_b16 v[164:165], v181 offset:4096\n\t" \
  "ds_read_b64_tr_b16 v[166:167], v181 offset:5120\n\t" \
  "v_exp_f32 v22, v22\n\t" \
  "v_exp_f32 v23, v23\n\t" \
  "v_mfma_f32_32x32x16_bf16 v[96:111], v[136:139], a[96:99], v[96:111]\n\t" \
  "ds_read_b64_tr_b16 v[168:169], v180 offset:28672\n\t" \
  "ds_read_b64_tr_b16 v[170:171], v180 offset:29696\n\t" \
  "v_fma_f32 v20, v20, v197, v197\n\t" \
  "v_fma_f32 v21, v21, v197, v197\n\t" \
  "v_mfma_f32_32x32x16_bf16 v[112:127], v[140:143], a[96:99], v[112:127]\n\t" \
  "ds_read_b64_tr_b16 v[172:173], v181 offset:28672\n\t" \
  "ds_read_b64_tr_b16 v[174:175], v181 offset:29696\n\t" \
  "v_fma_f32 v22, v22, v197, v197\n\t" \
  "s_waitcnt lgkmcnt(8)\n\t" \
  "v_mfma_f32_32x32x16_bf16 v[64:79], v[144:147], a[100:103], v[64:79]\n\t" \
  "ds_read_b64_tr_b16 v[128:129], v180 offset:6144\n\t" \
  "ds_read_b64_tr_b16 v[130:131], v180 offset:7168\n\t" \
  "v_fma_f32 v23, v23, v197, v197\n\t" \
  "v_rcp_f32 v20, v20\n\t" \
  "v_mfma_f32_32x32x16_bf16 v[80:95], v[148:151], a[100:103], v[80:95]\n\t" \
  "ds_read_b64_tr_b16 v[132:133], v181 offset:6144\n\t" \
  "ds_read_b64_tr_b16 v[134:135], v181 offset:7168\n\t" \
  "buffer_load_dwordx4 v191, %0, s40 offen offset:2048 lds\n\t" \
  "v_rcp_f32 v21, v21\n\t" \
  "v_mfma_f32_32x32x16_bf16 v[96:111], v[152:155], a[100:103], v[96:111]\n\t" \
  "ds_read_b64_tr_b16 v[136:137], v180 offset:30720\n\t" \
  "ds_read_b64_tr_b16 v[138:139], v180 offset:31744\n\t" \
  "v_rcp_f32 v22, v22\n\t" \
  "v_rcp_f32 v23, v23\n\t" \
  "v_mfma_f32_32x32x16_bf16 v[112:127], v[156:159], a[100:103], v[112:127]\n\t" \
  "ds_read_b64_tr_b16 v[140:141], v181 offset:30720\n\t" \
  "ds_read_b64_tr_b16 v[142:143], v181 offset:31744\n\t" \
  "v_mul_f32 v52, v52, v20\n\t" \
  "s_waitcnt lgkmcnt(8)\n\t" \
  "v_mfma_f32_32x32x16_bf16 v[64:79], v[160:163], a[104:107], v[64:79]\n\t" \
  "ds_read_b64_tr_b16 v[144:145], v180 offset:8192\n\t" \
  "ds_read_b64_tr_b16 v[146:147], v180 offset:9216\n\t" \
  "v_mul_f32 v53, v53, v21\n\t" \
  "v_mul_f32 v54, v54, v22\n\t" \
  "v_mfma_f32_32x32x16_bf16 v[80:95], v[164:167], a[104:107], v[80:95]\n\t" \
  "ds_read_b64_tr_b16 v[148:149], v181 offset:8192\n\t" \
  "ds_read_b64_tr_b16 v[150:151], v181 offset:9216\n\t" \
  "v_mul_f32 v55, v55, v23\n\t" \
  "v_cvt_pk_bf16_f32 v20, v52, v53\n\t" \
  "v_mfma_f32_32x32x16_bf16 v[96:111], v[168:171], a[104:107], v[96:111]\n\t" \
  "ds_read_b64_tr_b16 v[152:153], v180 offset:32768\n\t" \
  "ds_read_b64_tr_b16 v[154:155], v180 offset:33792\n\t" \
  "v_cvt_pk_bf16_f32 v21, v54, v55\n\t" \
  "v_mfma_f32_32x32x16_bf16 v[112:127], v[172:175], a[104:107], v[112:127]\n\t" \
  "ds_read_b64_tr_b16 v[156:157], v181 offset:32768\n\t" \
  "ds_read_b64_tr_b16 v[158:159], v181 offset:33792\n\t" \
  "v_accvgpr_write_b32 a186, v20\n\t" \
  "v_accvgpr_write_b32 a187, v21\n\t" \
  "s_waitcnt lgkmcnt(8)\n\t" \
  "v_mfma_f32_32x32x16_bf16 v[64:79], v[128:131], a[108:111], v[64:79]\n\t" \
  "ds_read_b64_tr_b16 v[160:161], v180 offset:10240\n\t" \
  "ds_read_b64_tr_b16 v[162:163], v180 offset:11264\n\t" \
  "v_mul_f32 v56, v24, v56\n\t" \
  "v_mfma_f32_32x32x16_bf16 v[80:95], v[132:135], a[108:111], v[80:95]\n\t" \
  "ds_read_b64_tr_b16 v[164:165], v181 offset:10240\n\t" \
  "ds_read_b64_tr_b16 v[166:167], v181 offset:11264\n\t" \
  "buffer_load_dwordx4 v192, %0, s40 offen offset:3072 lds\n\t" \
  "v_mul_f32 v57, v25, v57\n\t" \
  "v_mul_f32 v58, v26, v58\n\t" \
  "v_mfma_f32_32x32x16_bf16 v[96:111], v[136:139], a[108:111], v[96:111]\n\t" \
  "ds_read_b64_tr_b16 v[168:169], v180 offset:34816\n\t" \
  "ds_read_b64_tr_b16 v[170:171], v180 offset:35840\n\t" \
  "v_mul_f32 v59, v27, v59\n\t" \
  "v_mul_f32 v24, v196, v24\n\t" \
  "v_mfma_f32_32x32x16_bf16 v[112:127], v[140:143], a[108:111], v[112:127]\n\t" \
  "ds_read_b64_tr_b16 v[172:173], v181 offset:34816\n\t" \
  "ds_read_b64_tr_b16 v[174:175], v181 offset:35840\n\t" \
  "v_mul_f32 v25, v196, v25\n\t" \
  "s_waitcnt lgkmcnt(8)\n\t" \
  "v_mfma_f32_32x32x16_bf16 v[64:79], v[144:147], a[112:115], v[64:79]\n\t" \
  "ds_read_b64_tr_b16 v[128:129], v180 offset:12288\n\t" \
  "ds_read_b64_tr_b16 v[130:131], v180 offset:13312\n\t" \
  "v_mul_f32 v26, v196, v26\n\t" \
  "v_mul_f32 v27, v196, v27\n\t" \
  "v_mfma_f32_32x32x16_bf16 v[80:95], v[148:151], a[112:115], v[80:95]\n\t" \
  "ds_read_b64_tr_b16 v[132:133], v181 offset:12288\n\t" \
  "ds_read_b64_tr_b16 v[134:135], v181 offset:13312\n\t" \
  "s_add_u32 s40, %8, 2359296\n\t" \
  "s_add_u32 m0, %15, 53248\n\t" \
  "v_exp_f32 v24, v24\n\t" \
  "v_mfma_f32_32x32x16_bf16 v[96:111], v[152:155], a[112:115], v[96:111]\n\t" \
  "ds_read_b64_tr_b16 v[136:137], v180 offset:36864\n\t" \
  "ds_read_b64_tr_b16 v[138:139], v180 offset:37888\n\t" \
  "buffer_load_dwordx4 v189, %0, s40 offen lds\n\t" \
  "v_exp_f32 v25, v25\n\t" \
  "v_exp_f32 v26, v26\n\t" \
  "v_mfma_f32_32x32x16_bf16 v[112:127], v[156:159], a[112:115], v[112:127]\n\t" \
  "ds_read_b64_tr_b16 v[140:141], v181 offset:36864\n\t" \
  "ds_read_b64_tr_b16 v[142:143], v181 offset:37888\n\t" \
  "v_exp_f32 v27, v27\n\t" \
  "v_fma_f32 v24, v24, v197, v197\n\t" \
  "s_waitcnt lgkmcnt(8)\n\t" \
  "v_mfma_f32_32x32x16_bf16 v[64:79], v[160:163], a[116:119], v[64:79]\n\t" \
  "ds_read_b64_tr_b16 v[144:145], v180 offset:14336\n\t" \
  "ds_read_b64_tr_b16 v[146:147], v180 offset:15360\n\t" \
  "v_fma_f32 v25, v25, v197, v197\n\t" \
  "v_mfma_f32_32x32x16_bf16 v[80:95], v[164:167], a[116:119], v[80:95]\n\t" \
  "ds_read_b64_tr_b16 v[148:149], v181 offset:14336\n\t" \
  "ds_read_b64_tr_b16 v[150:151], v181 offset:15360\n\t" \
  "v_fma_f32 v26, v26, v197, v197\n\t" \
  "v_fma_f32 v27, v27, v197, v197\n\t" \
  "v_mfma_f32_32x32x16_bf16 v[96:111], v[168:171], a[116:119], v[96:111]\n\t" \
  "ds_read_b64_tr_b16 v[152:153], v180 offset:38912\n\t" \
  "ds_read_b64_tr_b16 v[154:155], v180 offset:39936\n\t" \
  "v_rcp_f32 v24, v24\n\t" \
  "v_mfma_f32_32x32x16_bf16 v[112:127], v[172:175], a[116:119], v[112:127]\n\t" \
  "ds_read_b64_tr_b16 v[156:157], v181 offset:38912\n\t" \
  "ds_read_b64_tr_b16 v[158:159], v181 offset:39936\n\t" \
  "v_rcp_f32 v25, v25\n\t" \
  "v_rcp_f32 v26, v26\n\t" \
  "s_waitcnt lgkmcnt(8)\n\t" \
  "v_mfma_f32_32x32x16_bf16 v[64:79], v[128:131], a[120:123], v[64:79]\n\t" \
  "ds_read_b64_tr_b16 v[160:161], v180 offset:16384\n\t" \
  "ds_read_b64_tr_b16 v[162:163], v180 offset:17408\n\t" \
  "v_rcp_f32 v27, v27\n\t" \
  "v_mfma_f32_32x32x16_bf16 v[80:95], v[132:135], a[120:123], v[80:95]\n\t" \
  "ds_read_b64_tr_b16 v[164:165], v181 offset:16384\n\t" \
  "ds_read_b64_tr_b16 v[166:167], v181 offset:17408\n\t" \
  "buffer_load_dwordx4 v190, %0, s40 offen offset:1024 lds\n\t" \
  "v_mul_f32 v56, v56, v24\n\t" \
  "v_mul_f32 v57, v57, v25\n\t" \
  "v_mfma_f32_32x32x16_bf16 v[96:111], v[136:139], a[120:123], v[96:111]\n\t" \
  "ds_read_b64_tr_b16 v[168:169], v180 offset:40960\n\t" \
  "ds_read_b64_tr_b16 v[170:171], v180 offset:41984\n\t" \
  "v_mul_f32 v58, v58, v26\n\t" \
  "v_mul_f32 v59, v59, v27\n\t" \
  "v_mfma_f32_32x32x16_bf16 v[112:127], v[140:143], a[120:123], v[112:127]\n\t" \
  "ds_read_b64_tr_b16 v[172:173], v181 offset:40960\n\t" \
  "ds_read_b64_tr_b16 v[174:175], v181 offset:41984\n\t" \
  "v_cvt_pk_bf16_f32 v24, v56, v57\n\t" \
  "s_waitcnt lgkmcnt(8)\n\t" \
  "v_mfma_f32_32x32x16_bf16 v[64:79], v[144:147], a[124:127], v[64:79]\n\t" \
  "ds_read_b64_tr_b16 v[128:129], v180 offset:18432\n\t" \
  "ds_read_b64_tr_b16 v[130:131], v180 offset:19456\n\t" \
  "v_cvt_pk_bf16_f32 v25, v58, v59\n\t" \
  "v_accvgpr_write_b32 a188, v24\n\t" \
  "v_mfma_f32_32x32x16_bf16 v[80:95], v[148:151], a[124:127], v[80:95]\n\t" \
  "ds_read_b64_tr_b16 v[132:133], v181 offset:18432\n\t" \
  "ds_read_b64_tr_b16 v[134:135], v181 offset:19456\n\t" \
  "v_accvgpr_write_b32 a189, v25\n\t" \
  "v_mfma_f32_32x32x16_bf16 v[96:111], v[152:155], a[124:127], v[96:111]\n\t" \
  "ds_read_b64_tr_b16 v[136:137], v180 offset:43008\n\t" \
  "ds_read_b64_tr_b16 v[138:139], v180 offset:44032\n\t" \
  "v_mul_f32 v60, v28, v60\n\t" \
  "v_mul_f32 v61, v29, v61\n\t" \
  "v_mfma_f32_32x32x16_bf16 v[112:127], v[156:159], a[124:127], v[112:127]\n\t" \
  "ds_read_b64_tr_b16 v[140:141], v181 offset:43008\n\t" \
  "ds_read_b64_tr_b16 v[142:143], v181 offset:44032\n\t" \
  "v_mul_f32 v62, v30, v62\n\t" \
  "v_mul_f32 v63, v31, v63\n\t" \
  "s_waitcnt lgkmcnt(8)\n\t" \
  "v_mfma_f32_32x32x16_bf16 v[64:79], v[160:163], a[128:131], v[64:79]\n\t" \
  "ds_read_b64_tr_b16 v[144:145], v180 offset:20480\n\t" \
  "ds_read_b64_tr_b16 v[146:147], v180 offset:21504\n\t" \
  "v_mul_f32 v28, v196, v28\n\t" \
  "v_mfma_f32_32x32x16_bf16 v[80:95], v[164:167], a[128:131], v[80:95]\n\t" \
  "ds_read_b64_tr_b16 v[148:149], v181 offset:20480\n\t" \
  "ds_read_b64_tr_b16 v[150:151], v181 offset:21504\n\t" \
  "v_mul_f32 v29, v196, v29\n\t" \
  "v_mul_f32 v30, v196, v30\n\t" \
  "v_mfma_f32_32x32x16_bf16 v[96:111], v[168:171], a[128:131], v[96:111]\n\t" \
  "ds_read_b64_tr_b16 v[152:153], v180 offset:45056\n\t" \
  "ds_read_b64_tr_b16 v[154:155], v180 offset:46080\n\t" \
  "v_mul_f32 v31, v196, v31\n\t" \
  "v_mfma_f32_32x32x16_bf16 v[112:127], v[172:175], a[128:131], v[112:127]\n\t" \
  "ds_read_b64_tr_b16 v[156:157], v181 offset:45056\n\t" \
  "ds_read_b64_tr_b16 v[158:159], v181 offset:46080\n\t" \
  "v_exp_f32 v28, v28\n\t" \
  "v_exp_f32 v29, v29\n\t" \
  "s_waitcnt lgkmcnt(8)\n\t" \
  "v_mfma_f32_32x32x16_bf16 v[64:79], v[128:131], a[132:135], v[64:79]\n\t" \
  "ds_read_b64_tr_b16 v[160:161], v180 offset:22528\n\t" \
  "ds_read_b64_tr_b16 v[162:163], v180 offset:23552\n\t" \
  "ds_read_b64_tr_b16 v[164:165], v181 offset:22528\n\t" \
  "ds_read_b64_tr_b16 v[166:167], v181 offset:23552\n\t" \
  "v_exp_f32 v30, v30\n\t" \
  "v_exp_f32 v31, v31\n\t" \
  "v_mfma_f32_32x32x16_bf16 v[80:95], v[132:135], a[132:135], v[80:95]\n\t" \
  "ds_read_b64_tr_b16 v[168:169], v180 offset:47104\n\t" \
  "ds_read_b64_tr_b16 v[170:171], v180 offset:48128\n\t" \
  "ds_read_b64_tr_b16 v[172:173], v181 offset:47104\n\t" \
  "ds_read_b64_tr_b16 v[174:175], v181 offset:48128\n\t" \
  "v_fma_f32 v28, v28, v197, v197\n\t" \
  "v_mfma_f32_32x32x16_bf16 v[96:111], v[136:139], a[132:135], v[96:111]\n\t" \
  "v_fma_f32 v29, v29, v197, v197\n\t" \
  "v_fma_f32 v30, v30, v197, v197\n\t" \
  "v_mfma_f32_32x32x16_bf16 v[112:127], v[140:143], a[132:135], v[112:127]\n\t" \
  "v_fma_f32 v31, v31, v197, v197\n\t" \
  "s_waitcnt lgkmcnt(8)\n\t" \
  "v_mfma_f32_32x32x16_bf16 v[64:79], v[144:147], a[136:139], v[64:79]\n\t" \
  "s_waitcnt vmcnt(6) lgkmcnt(0)\n\ts_barrier\n\t" \
  "ds_read_b64_tr_b16 v[128:129], v182 offset:0\n\t" \
  "ds_read_b64_tr_b16 v[130:131], v182 offset:512\n\t" \
  "v_rcp_f32 v28, v28\n\t" \
  "v_rcp_f32 v29, v29\n\t" \
  "v_mfma_f32_32x32x16_bf16 v[80:95], v[148:151], a[136:139], v[80:95]\n\t" \
  "ds_read_b64_tr_b16 v[132:133], v182 offset:12288\n\t" \
  "ds_read_b64_tr_b16 v[134:135], v182 offset:12800\n\t" \
  "s_add_u32 s40, %8, 3538944\n\t" \
  "s_add_u32 m0, %15, 98304\n\t" \
  "s_nop 0\n\t" \
  "buffer_load_dwordx4 v189, %0, s40 offen lds\n\t" \
  "v_rcp_f32 v30, v30\n\t" \
  "v_mfma_f32_32x32x16_bf16 v[96:111], v[152:155], a[136:139], v[96:111]\n\t" \
  "ds_read_b64_tr_b16 v[136:137], v182 offset:1024\n\t" \
  "ds_read_b64_tr_b16 v[138:139], v182 offset:1536\n\t" \
  "buffer_load_dwordx4 v190, %0, s40 offen offset:1024 lds\n\t" \
  "v_rcp_f32 v31, v31\n\t" \
  "v_mul_f32 v60, v60, v28\n\t" \
  "v_mfma_f32_32x32x16_bf16 v[112:127], v[156:159], a[136:139], v[112:127]\n\t" \
  "ds_read_b64_tr_b16 v[140:141], v182 offset:13312\n\t" \
  "ds_read_b64_tr_b16 v[142:143], v182 offset:13824\n\t" \
  "v_mul_f32 v61, v61, v29\n\t" \
  "v_mul_f32 v62, v62, v30\n\t" \
  "s_waitcnt lgkmcnt(8)\n\t" \
  "v_mfma_f32_32x32x16_bf16 v[64:79], v[160:163], a[140:143], v[64:79]\n\t" \
  "ds_read_b64_tr_b16 v[144:145], v182 offset:2048\n\t" \
  "ds_read_b64_tr_b16 v[146:147], v182 offset:2560\n\t" \
  "v_mul_f32 v63, v63, v31\n\t" \
  "v_mfma_f32_32x32x16_bf16 v[80:95], v[164:167], a[140:143], v[80:95]\n\t" \
  "ds_read_b64_tr_b16 v[148:149], v182 offset:14336\n\t" \
  "ds_read_b64_tr_b16 v[150:151], v182 offset:14848\n\t" \
  "v_cvt_pk_bf16_f32 v28, v60, v61\n\t" \
  "v_cvt_pk_bf16_f32 v29, v62, v63\n\t" \
  "v_mfma_f32_32x32x16_bf16 v[96:111], v[168:171], a[140:143], v[96:111]\n\t" \
  "ds_read_b64_tr_b16 v[152:153], v182 offset:3072\n\t" \
  "ds_read_b64_tr_b16 v[154:155], v182 offset:3584\n\t" \
  "buffer_load_dwordx4 v191, %0, s40 offen offset:2048 lds\n\t" \
  "v_accvgpr_write_b32 a190, v28\n\t" \
  "v_mfma_f32_32x32x16_bf16 v[112:127], v[172:175], a[140:143], v[112:127]\n\t" \
  "ds_read_b64_tr_b16 v[156:157], v182 offset:15360\n\t" \
  "ds_read_b64_tr_b16 v[158:159], v182 offset:15872\n\t" \
  "v_accvgpr_write_b32 a191, v29\n\t" \
  "s_waitcnt lgkmcnt(8)\n\t" \
  "v_mfma_f32_32x32x16_bf16 v[0:15], v[128:131], a[0:3], 0\n\t" \
  "ds_read_b64_tr_b16 v[160:161], v182 offset:4096\n\t" \
  "ds_read_b64_tr_b16 v[162:163], v182 offset:4608\n\t" \
  "v_mfma_f32_32x32x16_bf16 v[32:47], v[132:135], a[0:3], 0\n\t" \
  "ds_read_b64_tr_b16 v[164:165], v182 offset:16384\n\t" \
  "ds_read_b64_tr_b16 v[166:167], v182 offset:16896\n\t" \
  "v_mfma_f32_32x32x16_bf16 v[0:15], v[136:139], a[4:7], v[0:15]\n\t" \
  "ds_read_b64_tr_b16 v[168:169], v182 offset:5120\n\t" \
  "ds_read_b64_tr_b16 v[170:171], v182 offset:5632\n\t" \
  "buffer_load_dwordx4 v192, %0, s40 offen offset:3072 lds\n\t" \
  "v_mfma_f32_32x32x16_bf16 v[32:47], v[140:143], a[4:7], v[32:47]\n\t" \
  "ds_read_b64_tr_b16 v[172:173], v182 offset:17408\n\t" \
  "ds_read_b64_tr_b16 v[174:175], v182 offset:17920\n\t" \
  "v_mul_f32 v96, v64, v96\n\t" \
  "v_mul_f32 v97, v65, v97\n\t" \
  "v_mul_f32 v98, v66, v98\n\t" \
  "v_mul_f32 v99, v67, v99\n\t" \
  "s_waitcnt lgkmcnt(8)\n\t" \
  "v_mfma_f32_32x32x16_bf16 v[0:15], v[144:147], a[8:11], v[0:15]\n\t" \
  "ds_read_b64_tr_b16 v[128:129], v182 offset:6144\n\t" \
  "ds_read_b64_tr_b16 v[130:131], v182 offset:6656\n\t" \
  "s_add_u32 s40, %8, 4128768\n\t" \
  "s_add_u32 m0, %15, 102400\n\t" \
  "v_mul_f32 v64, v196, v64\n\t" \
  "v_mul_f32 v65, v196, v65\n\t" \
  "v_mul_f32 v66, v196, v66\n\t" \
  "v_mfma_f32_32x32x16_bf16 v[32:47], v[148:151], a[8:11], v[32:47]\n\t" \
  "ds_read_b64_tr_b16 v[132:133], v182 offset:18432\n\t" \
  "ds_read_b64_tr_b16 v[134:135], v182 offset:18944\n\t" \
  "buffer_load_dwordx4 v189, %0, s40 offen lds\n\t" \
  "v_mul_f32 v67, v196, v67\n\t" \
  "v_exp_f32 v64, v64\n\t" \
  "v_exp_f32 v65, v65\n\t" \
  "v_exp_f32 v66, v66\n\t" \
  "v_mfma_f32_32x32x16_bf16 v[0:15], v[152:155], a[12:15], v[0:15]\n\t" \
  "ds_read_b64_tr_b16 v[136:137], v182 offset:7168\n\t" \
  "ds_read_b64_tr_b16 v[138:139], v182 offset:7680\n\t" \
  "v_exp_f32 v67, v67\n\t" \
  "v_fma_f32 v64, v64, v197, v197\n\t" \
  "v_fma_f32 v65, v65, v197, v197\n\t" \
  "v_mfma_f32_32x32x16_bf16 v[32:47], v[156:159], a[12:15], v[32:47]\n\t" \
  "ds_read_b64_tr_b16 v[140:141], v182 offset:19456\n\t" \
  "ds_read_b64_tr_b16 v[142:143], v182 offset:19968\n\t" \
  "v_fma_f32 v66, v66, v197, v197\n\t" \
  "v_fma_f32 v67, v67, v197, v197\n\t" \
  "v_rcp_f32 v64, v64\n\t" \
  "s_waitcnt lgkmcnt(8)\n\t" \
  "v_mfma_f32_32x32x16_bf16 v[0:15], v[160:163], a[16:19], v[0:15]\n\t" \
  "ds_read_b64_tr_b16 v[144:145], v182 offset:8192\n\t" \
  "ds_read_b64_tr_b16 v[146:147], v182 offset:8704\n\t" \
  "v_rcp_f32 v65, v65\n\t" \
  "v_rcp_f32 v66, v66\n\t" \
  "v_rcp_f32 v67, v67\n\t" \
  "v_mfma_f32_32x32x16_bf16 v[32:47], v[164:167], a[16:19], v[32:47]\n\t" \
  "ds_read_b64_tr_b16 v[148:149], v182 offset:20480\n\t" \
  "ds_read_b64_tr_b16 v[150:151], v182 offset:20992\n\t" \
  "buffer_load_dwordx4 v190, %0, s40 offen offset:1024 lds\n\t" \
  "v_mul_f32 v96, v96, v64\n\t" \
  "v_mul_f32 v97, v97, v65\n\t" \
  "v_mul_f32 v98, v98, v66\n\t" \
  "v_mul_f32 v99, v99, v67\n\t" \
  "v_mfma_f32_32x32x16_bf16 v[0:15], v[168:171], a[20:23], v[0:15]\n\t" \
  "ds_read_b64_tr_b16 v[152:153], v182 offset:9216\n\t" \
  "ds_read_b64_tr_b16 v[154:155], v182 offset:9728\n\t" \
  "v_cvt_pk_bf16_f32 v64, v96, v97\n\t" \
  "v_cvt_pk_bf16_f32 v65, v98, v99\n\t" \
  "v_accvgpr_write_b32 a192, v64\n\t" \
  "v_mfma_f32_32x32x16_bf16 v[32:47], v[172:175], a[20:23], v[32:47]\n\t" \
  "ds_read_b64_tr_b16 v[156:157], v182 offset:21504\n\t" \
  "ds_read_b64_tr_b16 v[158:159], v182 offset:22016\n\t" \
  "v_accvgpr_write_b32 a193, v65\n\t" \
  "v_mul_f32 v100, v68, v100\n\t" \
  "v_mul_f32 v101, v69, v101\n\t" \
  "s_waitcnt lgkmcnt(8)\n\t" \
  "v_mfma_f32_32x32x16_bf16 v[0:15], v[128:131], a[24:27], v[0:15]\n\t" \
  "ds_read_b64_tr_b16 v[160:161], v182 offset:10240\n\t" \
  "ds_read_b64_tr_b16 v[162:163], v182 offset:10752\n\t" \
  "ds_read_b64_tr_b16 v[164:165], v182 offset:22528\n\t" \
  "ds_read_b64_tr_b16 v[166:167], v182 offset:23040\n\t" \
  "v_mul_f32 v102, v70, v102\n\t" \
  "v_mul_f32 v103, v71, v103\n\t" \
  "v_mul_f32 v68, v196, v68\n\t" \
  "v_mfma_f32_32x32x16_bf16 v[32:47], v[132:135], a[24:27], v[32:47]\n\t" \
  "ds_read_b64_tr_b16 v[168:169], v182 offset:11264\n\t" \
  "ds_read_b64_tr_b16 v[170:171], v182 offset:11776\n\t" \
  "ds_read_b64_tr_b16 v[172:173], v182 offset:23552\n\t" \
  "ds_read_b64_tr_b16 v[174:175], v182 offset:24064\n\t" \
  "v_mul_f32 v69, v196, v69\n\t" \
  "v_mul_f32 v70, v196, v70\n\t" \
  "v_mul_f32 v71, v196, v71\n\t" \
  "v_exp_f32 v68, v68\n\t" \
  "v_mfma_f32_32x32x16_bf16 v[0:15], v[136:139], a[28:31], v[0:15]\n\t" \
  "v_exp_f32 v69, v69\n\t" \
  "v_exp_f32 v70, v70\n\t" \
  "v_exp_f32 v71, v71\n\t" \
  "v_mfma_f32_32x32x16_bf16 v[32:47], v[140:143], a[28:31], v[32:47]\n\t" \
  "v_fma_f32 v68, v68, v197, v197\n\t" \
  "v_fma_f32 v69, v69, v197, v197\n\t" \
  "v_fma_f32 v70, v70, v197, v197\n\t" \
  "s_waitcnt lgkmcnt(8)\n\t" \
  "v_mfma_f32_32x32x16_bf16 v[0:15], v[144:147], a[32:35], v[0:15]\n\t" \
  "s_waitcnt vmcnt(6) lgkmcnt(0)\n\ts_barrier\n\t" \
  "ds_read_b64_tr_b16 v[128:129], v183 offset:0\n\t" \
  "ds_read_b64_tr_b16 v[130:131], v183 offset:512\n\t" \
  "v_fma_f32 v71, v71, v197, v197\n\t" \
  "v_rcp_f32 v68, v68\n\t" \
  "v_rcp_f32 v69, v69\n\t" \
  "v_mfma_f32_32x32x16_bf16 v[32:47], v[148:151], a[32:35], v[32:47]\n\t" \
  "ds_read_b64_tr_b16 v[132:133], v183 offset:12288\n\t" \
  "ds_read_b64_tr_b16 v[134:135], v183 offset:12800\n\t" \
  "s_lshl_b32 s41, %17, 7\n\t" \
  "s_add_u32 s40, %9, s41\n\t" \
  "s_add_u32 m0, %16, 0\n\t" \
  "s_nop 0\n\t" \
  "buffer_load_dwordx4 v193, %1, s40 offen lds\n\t" \
  "v_rcp_f32 v70, v70\n\t" \
  "v_rcp_f32 v71, v71\n\t" \
  "v_mul_f32 v100, v100, v68\n\t" \
  "v_mul_f32 v101, v101, v69\n\t" \
  "v_mfma_f32_32x32x16_bf16 v[0:15], v[152:155], a[36:39], v[0:15]\n\t" \
  "ds_read_b64_tr_b16 v[136:137], v183 offset:1024\n\t" \
  "ds_read_b64_tr_b16 v[138:139], v183 offset:1536\n\t" \
  "buffer_load_dwordx4 v194, %1, s40 offen offset:1024 lds\n\t" \
  "v_mul_f32 v102, v102, v70\n\t" \
  "v_mul_f32 v103, v103, v71\n\t" \
  "v_cvt_pk_bf16_f32 v68, v100, v101\n\t" \
  "v_mfma_f32_32x32x16_bf16 v[32:47], v[156:159], a[36:39], v[32:47]\n\t" \
  "ds_read_b64_tr_b16 v[140:141], v183 offset:13312\n\t" \
  "ds_read_b64_tr_b16 v[142:143], v183 offset:13824\n\t" \
  "v_cvt_pk_bf16_f32 v69, v102, v103\n\t" \
  "v_accvgpr_write_b32 a194, v68\n\t" \
  "v_accvgpr_write_b32 a195, v69\n\t" \
  "s_waitcnt lgkmcnt(8)\n\t" \
  "v_mfma_f32_32x32x16_bf16 v[0:15], v[160:163], a[40:43], v[0:15]\n\t" \
  "ds_read_b64_tr_b16 v[144:145], v183 offset:2048\n\t" \
  "ds_read_b64_tr_b16 v[146:147], v183 offset:2560\n\t" \
  "s_add_u32 s40, %10, s41\n\t" \
  "s_add_u32 m0, %16, 8192\n\t" \
  "v_mul_f32 v104, v72, v104\n\t" \
  "v_mul_f32 v105, v73, v105\n\t" \
  "v_mul_f32 v106, v74, v106\n\t" \
  "v_mfma_f32_32x32x16_bf16 v[32:47], v[164:167], a[40:43], v[32:47]\n\t" \
  "ds_read_b64_tr_b16 v[148:149], v183 offset:14336\n\t" \
  "ds_read_b64_tr_b16 v[150:151], v183 offset:14848\n\t" \
  "buffer_load_dwordx4 v193, %1, s40 offen lds\n\t" \
  "v_mul_f32 v107, v75, v107\n\t" \
  "v_mul_f32 v72, v196, v72\n\t" \
  "v_mul_f32 v73, v196, v73\n\t" \
  "v_mul_f32 v74, v196, v74\n\t" \
  "v_mfma_f32_32x32x16_bf16 v[0:15], v[168:171], a[44:47], v[0:15]\n\t" \
  "ds_read_b64_tr_b16 v[152:153], v183 offset:3072\n\t" \
  "ds_read_b64_tr_b16 v[154:155], v183 offset:3584\n\t" \
  "buffer_load_dwordx4 v194, %1, s40 offen offset:1024 lds\n\t" \
  "v_mul_f32 v75, v196, v75\n\t" \
  "v_exp_f32 v72, v72\n\t" \
  "v_exp_f32 v73, v73\n\t" \
  "v_mfma_f32_32x32x16_bf16 v[32:47], v[172:175], a[44:47], v[32:47]\n\t" \
  "ds_read_b64_tr_b16 v[156:157], v183 offset:15360\n\t" \
  "ds_read_b64_tr_b16 v[158:159], v183 offset:15872\n\t" \
  "v_exp_f32 v74, v74\n\t" \
  "v_exp_f32 v75, v75\n\t" \
  "v_fma_f32 v72, v72, v197, v197\n\t" \
  "s_waitcnt lgkmcnt(8)\n\t" \
  "v_mfma_f32_32x32x16_bf16 v[0:15], v[128:131], a[48:51], v[0:15]\n\t" \
  "ds_read_b64_tr_b16 v[160:161], v183 offset:4096\n\t" \
  "ds_read_b64_tr_b16 v[162:163], v183 offset:4608\n\t" \
  "s_add_u32 s40, %11, s41\n\t" \
  "s_add_u32 m0, %16, 16384\n\t" \
  "v_fma_f32 v73, v73, v197, v197\n\t" \
  "v_fma_f32 v74, v74, v197, v197\n\t" \
  "v_fma_f32 v75, v75, v197, v197\n\t" \
  "v_mfma_f32_32x32x16_bf16 v[32:47], v[132:135], a[48:51], v[32:47]\n\t" \
  "ds_read_b64_tr_b16 v[164:165], v183 offset:16384\n\t" \
  "ds_read_b64_tr_b16 v[166:167], v183 offset:16896\n\t" \
  "buffer_load_dwordx4 v193, %1, s40 offen lds\n\t" \
  "v_rcp_f32 v72, v72\n\t" \
  "v_rcp_f32 v73, v73\n\t" \
  "v_rcp_f32 v74, v74\n\t" \
  "v_rcp_f32 v75, v75\n\t" \
  "v_mfma_f32_32x32x16_bf16 v[0:15], v[136:139], a[52:55], v[0:15]\n\t" \
  "ds_read_b64_tr_b16 v[168:169], v183 offset:5120\n\t" \
  "ds_read_b64_tr_b16 v[170:171], v183 offset:5632\n\t" \
  "buffer_load_dwordx4 v194, %1, s40 offen offset:1024 lds\n\t" \
  "v_mul_f32 v104, v104, v72\n\t" \
  "v_mul_f32 v105, v105, v73\n\t" \
  "v_mul_f32 v106, v106, v74\n\t" \
  "v_mfma_f32_32x32x16_bf16 v[32:47], v[140:143], a[52:55], v[32:47]\n\t" \
  "ds_read_b64_tr_b16 v[172:173], v183 offset:17408\n\t" \
  "ds_read_b64_tr_b16 v[174:175], v183 offset:17920\n\t" \
  "v_mul_f32 v107, v107, v75\n\t" \
  "v_cvt_pk_bf16_f32 v72, v104, v105\n\t" \
  "v_cvt_pk_bf16_f32 v73, v106, v107\n\t" \
  "s_waitcnt lgkmcnt(8)\n\t" \
  "v_mfma_f32_32x32x16_bf16 v[0:15], v[144:147], a[56:59], v[0:15]\n\t" \
  "ds_read_b64_tr_b16 v[128:129], v183 offset:6144\n\t" \
  "ds_read_b64_tr_b16 v[130:131], v183 offset:6656\n\t" \
  "s_add_u32 s40, %12, s41\n\t" \
  "s_add_u32 m0, %16, 24576\n\t" \
  "v_accvgpr_write_b32 a196, v72\n\t" \
  "v_accvgpr_write_b32 a197, v73\n\t" \
  "v_mul_f32 v108, v76, v108\n\t" \
  "v_mfma_f32_32x32x16_bf16 v[32:47], v[148:151], a[56:59], v[32:47]\n\t" \
  "ds_read_b64_tr_b16 v[132:133], v183 offset:18432\n\t" \
  "ds_read_b64_tr_b16 v[134:135], v183 offset:18944\n\t" \
  "buffer_load_dwordx4 v193, %1, s40 offen lds\n\t" \
  "v_mul_f32 v109, v77, v109\n\t" \
  "v_mul_f32 v110, v78, v110\n\t" \
  "v_mul_f32 v111, v79, v111\n\t" \
  "v_mul_f32 v76, v196, v76\n\t" \
  "v_mfma_f32_32x32x16_bf16 v[0:15], v[152:155], a[60:63], v[0:15]\n\t" \
  "ds_read_b64_tr_b16 v[136:137], v183 offset:7168\n\t" \
  "ds_read_b64_tr_b16 v[138:139], v183 offset:7680\n\t" \
  "buffer_load_dwordx4 v194, %1, s40 offen offset:1024 lds\n\t" \
  "v_mul_f32 v77, v196, v77\n\t" \
  "v_mul_f32 v78, v196, v78\n\t" \
  "v_mul_f32 v79, v196, v79\n\t" \
  "v_mfma_f32_32x32x16_bf16 v[32:47], v[156:159], a[60:63], v[32:47]\n\t" \
  "ds_read_b64_tr_b16 v[140:141], v183 offset:19456\n\t" \
  "ds_read_b64_tr_b16 v[142:143], v183 offset:19968\n\t" \
  "v_exp_f32 v76, v76\n\t" \
  "v_exp_f32 v77, v77\n\t" \
  "v_exp_f32 v78, v78\n\t" \
  "s_waitcnt lgkmcnt(8)\n\t" \
  "v_mfma_f32_32x32x16_bf16 v[0:15], v[160:163], a[64:67], v[0:15]\n\t" \
  "ds_read_b64_tr_b16 v[144:145], v183 offset:8192\n\t" \
  "ds_read_b64_tr_b16 v[146:147], v183 offset:8704\n\t" \
  "s_add_u32 s40, %13, s41\n\t" \
  "s_add_u32 m0, %25, 0\n\t" \
  "v_exp_f32 v79, v79\n\t" \
  "v_fma_f32 v76, v76, v197, v197\n\t" \
  "v_fma_f32 v77, v77, v197, v197\n\t" \
  "v_mfma_f32_32x32x16_bf16 v[32:47], v[164:167], a[64:67], v[32:47]\n\t" \
  "ds_read_b64_tr_b16 v[148:149], v183 offset:20480\n\t" \
  "ds_read_b64_tr_b16 v[150:151], v183 offset:20992\n\t" \
  "buffer_load_dwordx4 v193, %1, s40 offen lds\n\t" \
  "v_fma_f32 v78, v78, v197, v197\n\t" \
  "v_fma_f32 v79, v79, v197, v197\n\t" \
  "v_rcp_f32 v76, v76\n\t" \
  "v_rcp_f32 v77, v77\n\t" \
  "v_mfma_f32_32x32x16_bf16 v[0:15], v[168:171], a[68:71], v[0:15]\n\t" \
  "ds_read_b64_tr_b16 v[152:153], v183 offset:9216\n\t" \
  "ds_read_b64_tr_b16 v[154:155], v183 offset:9728\n\t" \
  "v_rcp_f32 v78, v78\n\t" \
  "v_rcp_f32 v79, v79\n\t" \
  "v_mul_f32 v108, v108, v76\n\t" \
  "v_mfma_f32_32x32x16_bf16 v[32:47], v[172:175], a[68:71], v[32:47]\n\t" \
  "ds_read_b64_tr_b16 v[156:157], v183 offset:21504\n\t" \
  "ds_read_b64_tr_b16 v[158:159], v183 offset:22016\n\t" \
  "v_mul_f32 v109, v109, v77\n\t" \
  "v_mul_f32 v110, v110, v78\n\t" \
  "v_mul_f32 v111, v111, v79\n\t" \
  "s_waitcnt lgkmcnt(8)\n\t" \
  "v_mfma_f32_32x32x16_bf16 v[0:15], v[128:131], a[72:75], v[0:15]\n\t" \
  "ds_read_b64_tr_b16 v[160:161], v183 offset:10240\n\t" \
  "ds_read_b64_tr_b16 v[162:163], v183 offset:10752\n\t" \
  "ds_read_b64_tr_b16 v[164:165], v183 offset:22528\n\t" \
  "ds_read_b64_tr_b16 v[166:167], v183 offset:23040\n\t" \
  "v_cvt_pk_bf16_f32 v76, v108, v109\n\t" \
  "v_cvt_pk_bf16_f32 v77, v110, v111\n\t" \
  "v_accvgpr_write_b32 a198, v76\n\t" \
  "v_mfma_f32_32x32x16_bf16 v[32:47], v[132:135], a[72:75], v[32:47]\n\t" \
  "ds_read_b64_tr_b16 v[168:169], v183 offset:11264\n\t" \
  "ds_read_b64_tr_b16 v[170:171], v183 offset:11776\n\t" \
  "ds_read_b64_tr_b16 v[172:173], v183 offset:23552\n\t" \
  "ds_read_b64_tr_b16 v[174:175], v183 offset:24064\n\t" \
  "v_accvgpr_write_b32 a199, v77\n\t" \
  "v_mul_f32 v112, v80, v112\n\t" \
  "v_mul_f32 v113, v81, v113\n\t" \
  "v_mul_f32 v114, v82, v114\n\t" \
  "v_mfma_f32_32x32x16_bf16 v[0:15], v[136:139], a[76:79], v[0:15]\n\t" \
  "v_mul_f32 v115, v83, v115\n\t" \
  "v_mul_f32 v80, v196, v80\n\t" \
  "v_mul_f32 v81, v196, v81\n\t" \
  "v_mfma_f32_32x32x16_bf16 v[32:47], v[140:143], a[76:79], v[32:47]\n\t" \
  "v_mul_f32 v82, v196, v82\n\t" \
  "v_mul_f32 v83, v196, v83\n\t" \
  "v_exp_f32 v80, v80\n\t" \
  "s_waitcnt lgkmcnt(8)\n\t" \
  "v_mfma_f32_32x32x16_bf16 v[0:15], v[144:147], a[80:83], v[0:15]\n\t" \
  "s_waitcnt vmcnt(9) lgkmcnt(0)\n\ts_barrier\n\t" \
  "ds_read_b64_tr_b16 v[128:129], v184 offset:0\n\t" \
  "ds_read_b64_tr_b16 v[130:131], v184 offset:512\n\t" \
  "v_exp_f32 v81, v81\n\t" \
  "v_exp_f32 v82, v82\n\t" \
  "v_exp_f32 v83, v83\n\t" \
  "v_mfma_f32_32x32x16_bf16 v[32:47], v[148:151], a[80:83], v[32:47]\n\t" \
  "ds_read_b64_tr_b16 v[132:133], v184 offset:12288\n\t" \
  "ds_read_b64_tr_b16 v[134:135], v184 offset:12800\n\t" \
  "s_add_u32 s41, s41, 128\n\t" \
  "s_cmp_eq_u32 s41, 1152\n\t" \
  "s_cselect_b32 s41, 0, s41\n\t" \
  "s_add_u32 s40, %9, s41\n\t" \
  "s_add_u32 m0, %16, 49152\n\t" \
  "s_nop 0\n\t" \
  "buffer_load_dwordx4 v193, %1, s40 offen lds\n\t" \
  "v_fma_f32 v80, v80, v197, v197\n\t" \
  "v_fma_f32 v81, v81, v197, v197\n\t" \
  "v_fma_f32 v82, v82, v197, v197\n\t" \
  "v_fma_f32 v83, v83, v197, v197\n\t" \
  "v_mfma_f32_32x32x16_bf16 v[0:15], v[152:155], a[84:87], v[0:15]\n\t" \
  "ds_read_b64_tr_b16 v[136:137], v184 offset:1024\n\t" \
  "ds_read_b64_tr_b16 v[138:139], v184 offset:1536\n\t" \
  "buffer_load_dwordx4 v194, %1, s40 offen offset:1024 lds\n\t" \
  "v_rcp_f32 v80, v80\n\t" \
  "v_rcp_f32 v81, v81\n\t" \
  "v_rcp_f32 v82, v82\n\t" \
  "v_mfma_f32_32x32x16_bf16 v[32:47], v[156:159], a[84:87], v[32:47]\n\t" \
  "ds_read_b64_tr_b16 v[140:141], v184 offset:13312\n\t" \
  "ds_read_b64_tr_b16 v[142:143], v184 offset:13824\n\t" \
  "v_rcp_f32 v83, v83\n\t" \
  "v_mul_f32 v112, v112, v80\n\t" \
  "v_mul_f32 v113, v113, v81\n\t" \
  "s_waitcnt lgkmcnt(8)\n\t" \
  "v_mfma_f32_32x32x16_bf16 v[0:15], v[160:163], a[88:91], v[0:15]\n\t" \
  "ds_read_b64_tr_b16 v[144:145], v184 offset:2048\n\t" \
  "ds_read_b64_tr_b16 v[146:147], v184 offset:2560\n\t" \
  "s_add_u32 s40, %10, s41\n\t" \
  "s_add_u32 m0, %16, 57344\n\t" \
  "v_mul_f32 v114, v114, v82\n\t" \
  "v_mul_f32 v115, v115, v83\n\t" \
  "v_cvt_pk_bf16_f32 v80, v112, v113\n\t" \
  "v_mfma_f32_32x32x16_bf16 v[32:47], v[164:167], a[88:91], v[32:47]\n\t" \
  "ds_read_b64_tr_b16 v[148:149], v184 offset:14336\n\t" \
  "ds_read_b64_tr_b16 v[150:151], v184 offset:14848\n\t" \
  "buffer_load_dwordx4 v193, %1, s40 offen lds\n\t" \
  "v_cvt_pk_bf16_f32 v81, v114, v115\n\t" \
  "v_accvgpr_write_b32 a200, v80\n\t" \
  "v_accvgpr_write_b32 a201, v81\n\t" \
  "v_mul_f32 v116, v84, v116\n\t" \
  "v_mfma_f32_32x32x16_bf16 v[0:15], v[168:171], a[92:95], v[0:15]\n\t" \
  "ds_read_b64_tr_b16 v[152:153], v184 offset:3072\n\t" \
  "ds_read_b64_tr_b16 v[154:155], v184 offset:3584\n\t" \
  "buffer_load_dwordx4 v194, %1, s40 offen offset:1024 lds\n\t" \
  "v_mul_f32 v117, v85, v117\n\t" \
  "v_mul_f32 v118, v86, v118\n\t" \
  "v_mul_f32 v119, v87, v119\n\t" \
  "v_mfma_f32_32x32x16_bf16 v[32:47], v[172:175], a[92:95], v[32:47]\n\t" \
  "ds_read_b64_tr_b16 v[156:157], v184 offset:15360\n\t" \
  "ds_read_b64_tr_b16 v[158:159], v184 offset:15872\n\t" \
  "v_mul_f32 v84, v196, v84\n\t" \
  "v_mul_f32 v85, v196, v85\n\t" \
  "v_mul_f32 v86, v196, v86\n\t" \
  "s_waitcnt lgkmcnt(8)\n\t" \
  "v_mfma_f32_32x32x16_bf16 v[0:15], v[128:131], a[96:99], v[0:15]\n\t" \
  "ds_read_b64_tr_b16 v[160:161], v184 offset:4096\n\t" \
  "ds_read_b64_tr_b16 v[162:163], v184 offset:4608\n\t" \
  "s_add_u32 s40, %11, s41\n\t" \
  "s_add_u32 m0, %16, 65536\n\t" \
  "v_mul_f32 v87, v196, v87\n\t" \
  "v_exp_f32 v84, v84\n\t" \
  "v_exp_f32 v85, v85\n\t" \
  "v_mfma_f32_32x32x16_bf16 v[32:47], v[132:135], a[96:99], v[32:47]\n\t" \
  "ds_read_b64_tr_b16 v[164:165], v184 offset:16384\n\t" \
  "ds_read_b64_tr_b16 v[166:167], v184 offset:16896\n\t" \
  "buffer_load_dwordx4 v193, %1, s40 offen lds\n\t" \
  "v_exp_f32 v86, v86\n\t" \
  "v_exp_f32 v87, v87\n\t" \
  "v_fma_f32 v84, v84, v197, v197\n\t" \
  "v_fma_f32 v85, v85, v197, v197\n\t" \
  "v_mfma_f32_32x32x16_bf16 v[0:15], v[136:139], a[100:103], v[0:15]\n\t" \
  "ds_read_b64_tr_b16 v[168:169], v184 offset:5120\n\t" \
  "ds_read_b64_tr_b16 v[170:171], v184 offset:5632\n\t" \
  "buffer_load_dwordx4 v194, %1, s40 offen offset:1024 lds\n\t" \
  "v_fma_f32 v86, v86, v197, v197\n\t" \
  "v_fma_f32 v87, v87, v197, v197\n\t" \
  "v_rcp_f32 v84, v84\n\t" \
  "v_mfma_f32_32x32x16_bf16 v[32:47], v[140:143], a[100:103], v[32:47]\n\t" \
  "ds_read_b64_tr_b16 v[172:173], v184 offset:17408\n\t" \
  "ds_read_b64_tr_b16 v[174:175], v184 offset:17920\n\t" \
  "v_rcp_f32 v85, v85\n\t" \
  "v_rcp_f32 v86, v86\n\t" \
  "v_rcp_f32 v87, v87\n\t" \
  "s_waitcnt lgkmcnt(8)\n\t" \
  "v_mfma_f32_32x32x16_bf16 v[0:15], v[144:147], a[104:107], v[0:15]\n\t" \
  "ds_read_b64_tr_b16 v[128:129], v184 offset:6144\n\t" \
  "ds_read_b64_tr_b16 v[130:131], v184 offset:6656\n\t" \
  "s_add_u32 s40, %12, s41\n\t" \
  "s_add_u32 m0, %16, 73728\n\t" \
  "v_mul_f32 v116, v116, v84\n\t" \
  "v_mul_f32 v117, v117, v85\n\t" \
  "v_mul_f32 v118, v118, v86\n\t" \
  "v_mfma_f32_32x32x16_bf16 v[32:47], v[148:151], a[104:107], v[32:47]\n\t" \
  "ds_read_b64_tr_b16 v[132:133], v184 offset:18432\n\t" \
  "ds_read_b64_tr_b16 v[134:135], v184 offset:18944\n\t" \
  "buffer_load_dwordx4 v193, %1, s40 offen lds\n\t" \
  "v_mul_f32 v119, v119, v87\n\t" \
  "v_cvt_pk_bf16_f32 v84, v116, v117\n\t" \
  "v_cvt_pk_bf16_f32 v85, v118, v119\n\t" \
  "v_accvgpr_write_b32 a202, v84\n\t" \
  "v_mfma_f32_32x32x16_bf16 v[0:15], v[152:155], a[108:111], v[0:15]\n\t" \
  "ds_read_b64_tr_b16 v[136:137], v184 offset:7168\n\t" \
  "ds_read_b64_tr_b16 v[138:139], v184 offset:7680\n\t" \
  "buffer_load_dwordx4 v194, %1, s40 offen offset:1024 lds\n\t" \
  "v_accvgpr_write_b32 a203, v85\n\t" \
  "v_mul_f32 v120, v88, v120\n\t" \
  "v_mul_f32 v121, v89, v121\n\t" \
  "v_mfma_f32_32x32x16_bf16 v[32:47], v[156:159], a[108:111], v[32:47]\n\t" \
  "ds_read_b64_tr_b16 v[140:141], v184 offset:19456\n\t" \
  "ds_read_b64_tr_b16 v[142:143], v184 offset:19968\n\t" \
  "v_mul_f32 v122, v90, v122\n\t" \
  "v_mul_f32 v123, v91, v123\n\t" \
  "v_mul_f32 v88, v196, v88\n\t" \
  "s_waitcnt lgkmcnt(8)\n\t" \
  "v_mfma_f32_32x32x16_bf16 v[0:15], v[160:163], a[112:115], v[0:15]\n\t" \
  "ds_read_b64_tr_b16 v[144:145], v184 offset:8192\n\t" \
  "ds_read_b64_tr_b16 v[146:147], v184 offset:8704\n\t" \
  "s_add_u32 s40, %13, s41\n\t" \
  "s_add_u32 m0, %25, 49152\n\t" \
  "v_mul_f32 v89, v196, v89\n\t" \
  "v_mul_f32 v90, v196, v90\n\t" \
  "v_mul_f32 v91, v196, v91\n\t" \
  "v_mfma_f32_32x32x16_bf16 v[32:47], v[164:167], a[112:115], v[32:47]\n\t" \
  "ds_read_b64_tr_b16 v[148:149], v184 offset:20480\n\t" \
  "ds_read_b64_tr_b16 v[150:151], v184 offset:20992\n\t" \
  "buffer_load_dwordx4 v193, %1, s40 offen lds\n\t" \
  "v_exp_f32 v88, v88\n\t" \
  "v_exp_f32 v89, v89\n\t" \
  "v_exp_f32 v90, v90\n\t" \
  "v_exp_f32 v91, v91\n\t" \
  "v_mfma_f32_32x32x16_bf16 v[0:15], v[168:171], a[116:119], v[0:15]\n\t" \
  "ds_read_b64_tr_b16 v[152:153], v184 offset:9216\n\t" \
  "ds_read_b64_tr_b16 v[154:155], v184 offset:9728\n\t" \
  "v_fma_f32 v88, v88, v197, v197\n\t" \
  "v_fma_f32 v89, v89, v197, v197\n\t" \
  "v_fma_f32 v90, v90, v197, v197\n\t" \
  "v_mfma_f32_32x32x16_bf16 v[32:47], v[172:175], a[116:119], v[32:47]\n\t" \
  "ds_read_b64_tr_b16 v[156:157], v184 offset:21504\n\t" \
  "ds_read_b64_tr_b16 v[158:159], v184 offset:22016\n\t" \
  "v_fma_f32 v91, v91, v197, v197\n\t" \
  "v_rcp_f32 v88, v88\n\t" \
  "v_rcp_f32 v89, v89\n\t" \
  "s_waitcnt lgkmcnt(8)\n\t" \
  "v_mfma_f32_32x32x16_bf16 v[0:15], v[128:131], a[120:123], v[0:15]\n\t" \
  "ds_read_b64_tr_b16 v[160:161], v184 offset:10240\n\t" \
  "ds_read_b64_tr_b16 v[162:163], v184 offset:10752\n\t" \
  "ds_read_b64_tr_b16 v[164:165], v184 offset:22528\n\t" \
  "ds_read_b64_tr_b16 v[166:167], v184 offset:23040\n\t" \
  "v_rcp_f32 v90, v90\n\t" \
  "v_rcp_f32 v91, v91\n\t" \
  "v_mul_f32 v120, v120, v88\n\t" \
  "v_mfma_f32_32x32x16_bf16 v[32:47], v[132:135], a[120:123], v[32:47]\n\t" \
  "ds_read_b64_tr_b16 v[168:169], v184 offset:11264\n\t" \
  "ds_read_b64_tr_b16 v[170:171], v184 offset:11776\n\t" \
  "ds_read_b64_tr_b16 v[172:173], v184 offset:23552\n\t" \
  "ds_read_b64_tr_b16 v[174:175], v184 offset:24064\n\t" \
  "v_mul_f32 v121, v121, v89\n\t" \
  "v_mul_f32 v122, v122, v90\n\t" \
  "v_mul_f32 v123, v123, v91\n\t" \
  "v_cvt_pk_bf16_f32 v88, v120, v121\n\t" \
  "v_mfma_f32_32x32x16_bf16 v[0:15], v[136:139], a[124:127], v[0:15]\n\t" \
  "v_cvt_pk_bf16_f32 v89, v122, v123\n\t" \
  "v_accvgpr_write_b32 a204, v88\n\t" \
  "v_accvgpr_write_b32 a205, v89\n\t" \
  "v_mfma_f32_32x32x16_bf16 v[32:47], v[140:143], a[124:127], v[32:47]\n\t" \
  "v_mul_f32 v124, v92, v124\n\t" \
  "v_mul_f32 v125, v93, v125\n\t" \
  "v_mul_f32 v126, v94, v126\n\t" \
  "s_waitcnt lgkmcnt(8)\n\t" \
  "v_mfma_f32_32x32x16_bf16 v[0:15], v[144:147], a[128:131], v[0:15]\n\t" \
  "s_waitcnt vmcnt(9) lgkmcnt(0)\n\ts_barrier\n\t" \
  "ds_read_b64_tr_b16 v[128:129], v176 offset:0\n\t" \
  "ds_read_b64_tr_b16 v[130:131], v176 offset:1024\n\t" \
  "v_mul_f32 v127, v95, v127\n\t" \
  "v_mul_f32 v92, v196, v92\n\t" \
  "v_mul_f32 v93, v196, v93\n\t" \
  "v_mfma_f32_32x32x16_bf16 v[32:47], v[148:151], a[128:131], v[32:47]\n\t" \
  "ds_read_b64_tr_b16 v[132:133], v177 offset:0\n\t" \
  "ds_read_b64_tr_b16 v[134:135], v177 offset:1024\n\t" \
  "s_add_u32 s41, s41, 128\n\t" \
  "s_cmp_eq_u32 s41, 1152\n\t" \
  "s_cselect_b32 s41, 0, s41\n\t" \
  "s_add_u32 s40, %9, s41\n\t" \
  "s_add_u32 m0, %16, 98304\n\t" \
  "s_nop 0\n\t" \
  "buffer_load_dwordx4 v193, %1, s40 offen lds\n\t" \
  "v_mul_f32 v94, v196, v94\n\t" \
  "v_mul_f32 v95, v196, v95\n\t" \
  "v_exp_f32 v92, v92\n\t" \
  "v_exp_f32 v93, v93\n\t" \
  "v_mfma_f32_32x32x16_bf16 v[0:15], v[152:155], a[132:135], v[0:15]\n\t" \
  "ds_read_b64_tr_b16 v[136:137], v176 offset:2048\n\t" \
  "ds_read_b64_tr_b16 v[138:139], v176 offset:3072\n\t" \
  "buffer_load_dwordx4 v194, %1, s40 offen offset:1024 lds\n\t" \
  "v_exp_f32 v94, v94\n\t" \
  "v_exp_f32 v95, v95\n\t" \
  "v_fma_f32 v92, v92, v197, v197\n\t" \
  "v_mfma_f32_32x32x16_bf16 v[32:47], v[156:159], a[132:135], v[32:47]\n\t" \
  "ds_read_b64_tr_b16 v[140:141], v177 offset:2048\n\t" \
  "ds_read_b64_tr_b16 v[142:143], v177 offset:3072\n\t" \
  "v_fma_f32 v93, v93, v197, v197\n\t" \
  "v_fma_f32 v94, v94, v197, v197\n\t" \
  "v_fma_f32 v95, v95, v197, v197\n\t" \
  "s_waitcnt lgkmcnt(8)\n\t" \
  "v_mfma_f32_32x32x16_bf16 v[0:15], v[160:163], a[136:139], v[0:15]\n\t" \
  "ds_read_b64_tr_b16 v[144:145], v176 offset:4096\n\t" \
  "ds_read_b64_tr_b16 v[146:147], v176 offset:5120\n\t" \
  "v_rcp_f32 v92, v92\n\t" \
  "v_rcp_f32 v93, v93\n\t" \
  "v_rcp_f32 v94, v94\n\t" \
  "v_mfma_f32_32x32x16_bf16 v[32:47], v[164:167], a[136:139], v[32:47]\n\t" \
  "ds_read_b64_tr_b16 v[148:149], v177 offset:4096\n\t" \
  "ds_read_b64_tr_b16 v[150:151], v177 offset:5120\n\t" \
  "s_add_u32 s40, %10, s41\n\t" \
  "s_add_u32 m0, %16, 106496\n\t" \
  "v_rcp_f32 v95, v95\n\t" \
  "v_mul_f32 v124, v124, v92\n\t" \
  "v_mul_f32 v125, v125, v93\n\t" \
  "v_mul_f32 v126, v126, v94\n\t" \
  "v_mfma_f32_32x32x16_bf16 v[0:15], v[168:171], a[140:143], v[0:15]\n\t" \
  "ds_read_b64_tr_b16 v[152:153], v176 offset:6144\n\t" \
  "ds_read_b64_tr_b16 v[154:155], v176 offset:7168\n\t" \
  "buffer_load_dwordx4 v193, %1, s40 offen lds\n\t" \
  "v_mul_f32 v127, v127, v95\n\t" \
  "v_cvt_pk_bf16_f32 v92, v124, v125\n\t" \
  "v_cvt_pk_bf16_f32 v93, v126, v127\n\t" \
  "v_mfma_f32_32x32x16_bf16 v[32:47], v[172:175], a[140:143], v[32:47]\n\t" \
  "ds_read_b64_tr_b16 v[156:157], v177 offset:6144\n\t" \
  "ds_read_b64_tr_b16 v[158:159], v177 offset:7168\n\t" \
  "v_accvgpr_write_b32 a206, v92\n\t" \
  "v_accvgpr_write_b32 a207, v93\n\t" \
  "s_waitcnt lgkmcnt(8)\n\t" \
  "v_mfma_f32_32x32x16_bf16 v[64:79], v[128:131], a[144:147], 0\n\t" \
  "ds_read_b64_tr_b16 v[160:161], v176 offset:8192\n\t" \
  "ds_read_b64_tr_b16 v[162:163], v176 offset:9216\n\t" \
  "v_mfma_f32_32x32x16_bf16 v[80:95], v[132:135], a[144:147], 0\n\t" \
  "ds_read_b64_tr_b16 v[164:165], v177 offset:8192\n\t" \
  "ds_read_b64_tr_b16 v[166:167], v177 offset:9216\n\t" \
  "v_mfma_f32_32x32x16_bf16 v[64:79], v[136:139], a[148:151], v[64:79]\n\t" \
  "ds_read_b64_tr_b16 v[168:169], v176 offset:10240\n\t" \
  "ds_read_b64_tr_b16 v[170:171], v176 offset:11264\n\t" \
  "buffer_load_dwordx4 v194, %1, s40 offen offset:1024 lds\n\t" \
  "v_mul_f32 v32, v0, v32\n\t" \
  "v_mul_f32 v33, v1, v33\n\t" \
  "v_mul_f32 v34, v2, v34\n\t" \
  "v_mul_f32 v35, v3, v35\n\t" \
  "v_mul_f32 v0, v196, v0\n\t" \
  "v_mfma_f32_32x32x16_bf16 v[80:95], v[140:143], a[148:151], v[80:95]\n\t" \
  "ds_read_b64_tr_b16 v[172:173], v177 offset:10240\n\t" \
  "ds_read_b64_tr_b16 v[174:175], v177 offset:11264\n\t" \
  "v_mul_f32 v1, v196, v1\n\t" \
  "v_mul_f32 v2, v196, v2\n\t" \
  "v_mul_f32 v3, v196, v3\n\t" \
  "v_exp_f32 v0, v0\n\t" \
  "s_waitcnt lgkmcnt(8)\n\t" \
  "v_mfma_f32_32x32x16_bf16 v[64:79], v[144:147], a[152:155], v[64:79]\n\t" \
  "ds_read_b64_tr_b16 v[128:129], v176 offset:12288\n\t" \
  "ds_read_b64_tr_b16 v[130:131], v176 offset:13312\n\t" \
  "s_add_u32 s40, %11, s41\n\t" \
  "s_add_u32 m0, %16, 114688\n\t" \
  "v_exp_f32 v1, v1\n\t" \
  "v_exp_f32 v2, v2\n\t" \
  "v_exp_f32 v3, v3\n\t" \
  "v_fma_f32 v0, v0, v197, v197\n\t" \
  "v_mfma_f32_32x32x16_bf16 v[80:95], v[148:151], a[152:155], v[80:95]\n\t" \
  "ds_read_b64_tr_b16 v[132:133], v177 offset:12288\n\t" \
  "ds_read_b64_tr_b16 v[134:135], v177 offset:13312\n\t" \
  "buffer_load_dwordx4 v193, %1, s40 offen lds\n\t" \
  "v_fma_f32 v1, v1, v197, v197\n\t" \
  "v_fma_f32 v2, v2, v197, v197\n\t" \
  "v_fma_f32 v3, v3, v197, v197\n\t" \
  "v_rcp_f32 v0, v0\n\t" \
  "v_mfma_f32_32x32x16_bf16 v[64:79], v[152:155], a[156:159], v[64:79]\n\t" \
  "ds_read_b64_tr_b16 v[136:137], v176 offset:14336\n\t" \
  "ds_read_b64_tr_b16 v[138:139], v176 offset:15360\n\t" \
  "v_rcp_f32 v1, v1\n\t" \
  "v_rcp_f32 v2, v2\n\t" \
  "v_rcp_f32 v3, v3\n\t" \
  "v_mul_f32 v32, v32, v0\n\t" \
  "v_mfma_f32_32x32x16_bf16 v[80:95], v[156:159], a[156:159], v[80:95]\n\t" \
  "ds_read_b64_tr_b16 v[140:141], v177 offset:14336\n\t" \
  "ds_read_b64_tr_b16 v[142:143], v177 offset:15360\n\t" \
  "v_mul_f32 v33, v33, v1\n\t" \
  "v_mul_f32 v34, v34, v2\n\t" \
  "v_mul_f32 v35, v35, v3\n\t" \
  "v_cvt_pk_bf16_f32 v0, v32, v33\n\t" \
  "s_waitcnt lgkmcnt(8)\n\t" \
  "v_mfma_f32_32x32x16_bf16 v[64:79], v[160:163], a[160:163], v[64:79]\n\t" \
  "ds_read_b64_tr_b16 v[144:145], v176 offset:16384\n\t" \
  "ds_read_b64_tr_b16 v[146:147], v176 offset:17408\n\t" \
  "v_cvt_pk_bf16_f32 v1, v34, v35\n\t" \
  "v_accvgpr_write_b32 a208, v0\n\t" \
  "v_accvgpr_write_b32 a209, v1\n\t" \
  "v_mul_f32 v36, v4, v36\n\t" \
  "v_mfma_f32_32x32x16_bf16 v[80:95], v[164:167], a[160:163], v[80:95]\n\t" \
  "ds_read_b64_tr_b16 v[148:149], v177 offset:16384\n\t" \
  "ds_read_b64_tr_b16 v[150:151], v177 offset:17408\n\t" \
  "buffer_load_dwordx4 v194, %1, s40 offen offset:1024 lds\n\t" \
  "v_mul_f32 v37, v5, v37\n\t" \
  "v_mul_f32 v38, v6, v38\n\t" \
  "v_mul_f32 v39, v7, v39\n\t" \
  "v_mul_f32 v4, v196, v4\n\t" \
  "v_mfma_f32_32x32x16_bf16 v[64:79], v[168:171], a[164:167], v[64:79]\n\t" \
  "ds_read_b64_tr_b16 v[152:153], v176 offset:18432\n\t" \
  "ds_read_b64_tr_b16 v[154:155], v176 offset:19456\n\t" \
  "v_mul_f32 v5, v196, v5\n\t" \
  "v_mul_f32 v6, v196, v6\n\t" \
  "v_mul_f32 v7, v196, v7\n\t" \
  "v_exp_f32 v4, v4\n\t" \
  "v_mfma_f32_32x32x16_bf16 v[80:95], v[172:175], a[164:167], v[80:95]\n\t" \
  "ds_read_b64_tr_b16 v[156:157], v177 offset:18432\n\t" \
  "ds_read_b64_tr_b16 v[158:159], v177 offset:19456\n\t" \
  "v_exp_f32 v5, v5\n\t" \
  "v_exp_f32 v6, v6\n\t" \
  "v_exp_f32 v7, v7\n\t" \
  "v_fma_f32 v4, v4, v197, v197\n\t" \
  "s_waitcnt lgkmcnt(8)\n\t" \
  "v_mfma_f32_32x32x16_bf16 v[64:79], v[128:131], a[168:171], v[64:79]\n\t" \
  "ds_read_b64_tr_b16 v[160:161], v176 offset:20480\n\t" \
  "ds_read_b64_tr_b16 v[162:163], v176 offset:21504\n\t" \
  "s_add_u32 s40, %12, s41\n\t" \
  "s_add_u32 m0, %16, 122880\n\t" \
  "v_fma_f32 v5, v5, v197, v197\n\t" \
  "v_fma_f32 v6, v6, v197, v197\n\t" \
  "v_fma_f32 v7, v7, v197, v197\n\t" \
  "v_rcp_f32 v4, v4\n\t" \
  "v_mfma_f32_32x32x16_bf16 v[80:95], v[132:135], a[168:171], v[80:95]\n\t" \
  "ds_read_b64_tr_b16 v[164:165], v177 offset:20480\n\t" \
  "ds_read_b64_tr_b16 v[166:167], v177 offset:21504\n\t" \
  "buffer_load_dwordx4 v193, %1, s40 offen lds\n\t" \
  "v_rcp_f32 v5, v5\n\t" \
  "v_rcp_f32 v6, v6\n\t" \
  "v_rcp_f32 v7, v7\n\t" \
  "v_mul_f32 v36, v36, v4\n\t" \
  "v_mfma_f32_32x32x16_bf16 v[64:79], v[136:139], a[172:175], v[64:79]\n\t" \
  "ds_read_b64_tr_b16 v[168:169], v176 offset:22528\n\t" \
  "ds_read_b64_tr_b16 v[170:171], v176 offset:23552\n\t" \
  "buffer_load_dwordx4 v194, %1, s40 offen offset:1024 lds\n\t" \
  "v_mul_f32 v37, v37, v5\n\t" \
  "v_mul_f32 v38, v38, v6\n\t" \
  "v_mul_f32 v39, v39, v7\n\t" \
  "v_cvt_pk_bf16_f32 v4, v36, v37\n\t" \
  "v_mfma_f32_32x32x16_bf16 v[80:95], v[140:143], a[172:175], v[80:95]\n\t" \
  "ds_read_b64_tr_b16 v[172:173], v177 offset:22528\n\t" \
  "ds_read_b64_tr_b16 v[174:175], v177 offset:23552\n\t" \
  "v_cvt_pk_bf16_f32 v5, v38, v39\n\t" \
  "v_accvgpr_write_b32 a210, v4\n\t" \
  "v_accvgpr_write_b32 a211, v5\n\t" \
  "v_mul_f32 v40, v8, v40\n\t" \
  "s_waitcnt lgkmcnt(8)\n\t" \
  "v_mfma_f32_32x32x16_bf16 v[64:79], v[144:147], a[176:179], v[64:79]\n\t" \
  "ds_read_b64_tr_b16 v[128:129], v176 offset:24576\n\t" \
  "ds_read_b64_tr_b16 v[130:131], v176 offset:25600\n\t" \
  "v_mul_f32 v41, v9, v41\n\t" \
  "v_mul_f32 v42, v10, v42\n\t" \
  "v_mul_f32 v43, v11, v43\n\t" \
  "v_mul_f32 v8, v196, v8\n\t" \
  "v_mfma_f32_32x32x16_bf16 v[80:95], v[148:151], a[176:179], v[80:95]\n\t" \
  "ds_read_b64_tr_b16 v[132:133], v177 offset:24576\n\t" \
  "ds_read_b64_tr_b16 v[134:135], v177 offset:25600\n\t" \
  "s_add_u32 s40, %13, s41\n\t" \
  "s_add_u32 m0, %25, 98304\n\t" \
  "v_mul_f32 v9, v196, v9\n\t" \
  "v_mul_f32 v10, v196, v10\n\t" \
  "v_mul_f32 v11, v196, v11\n\t" \
  "v_exp_f32 v8, v8\n\t" \
  "v_mfma_f32_32x32x16_bf16 v[64:79], v[152:155], a[180:183], v[64:79]\n\t" \
  "ds_read_b64_tr_b16 v[136:137], v176 offset:26624\n\t" \
  "ds_read_b64_tr_b16 v[138:139], v176 offset:27648\n\t" \
  "buffer_load_dwordx4 v193, %1, s40 offen lds\n\t" \
  "v_exp_f32 v9, v9\n\t" \
  "v_exp_f32 v10, v10\n\t" \
  "v_exp_f32 v11, v11\n\t" \
  "v_fma_f32 v8, v8, v197, v197\n\t" \
  "v_mfma_f32_32x32x16_bf16 v[80:95], v[156:159], a[180:183], v[80:95]\n\t" \
  "ds_read_b64_tr_b16 v[140:141], v177 offset:26624\n\t" \
  "ds_read_b64_tr_b16 v[142:143], v177 offset:27648\n\t" \
  "v_fma_f32 v9, v9, v197, v197\n\t" \
  "v_fma_f32 v10, v10, v197, v197\n\t" \
  "v_fma_f32 v11, v11, v197, v197\n\t" \
  "v_rcp_f32 v8, v8\n\t" \
  "s_waitcnt lgkmcnt(8)\n\t" \
  "v_mfma_f32_32x32x16_bf16 v[64:79], v[160:163], a[184:187], v[64:79]\n\t" \
  "ds_read_b64_tr_b16 v[144:145], v176 offset:28672\n\t" \
  "ds_read_b64_tr_b16 v[146:147], v176 offset:29696\n\t" \
  "v_rcp_f32 v9, v9\n\t" \
  "v_rcp_f32 v10, v10\n\t" \
  "v_rcp_f32 v11, v11\n\t" \
  "v_mul_f32 v40, v40, v8\n\t" \
  "v_mfma_f32_32x32x16_bf16 v[80:95], v[164:167], a[184:187], v[80:95]\n\t" \
  "ds_read_b64_tr_b16 v[148:149], v177 offset:28672\n\t" \
  "ds_read_b64_tr_b16 v[150:151], v177 offset:29696\n\t" \
  "v_mul_f32 v41, v41, v9\n\t" \
  "v_mul_f32 v42, v42, v10\n\t" \
  "v_mul_f32 v43, v43, v11\n\t" \
  "v_cvt_pk_bf16_f32 v8, v40, v41\n\t" \
  "v_mfma_f32_32x32x16_bf16 v[64:79], v[168:171], a[188:191], v[64:79]\n\t" \
  "ds_read_b64_tr_b16 v[152:153], v176 offset:30720\n\t" \
  "ds_read_b64_tr_b16 v[154:155], v176 offset:31744\n\t" \
  "v_cvt_pk_bf16_f32 v9, v42, v43\n\t" \
  "v_accvgpr_write_b32 a212, v8\n\t" \
  "v_accvgpr_write_b32 a213, v9\n\t" \
  "v_mul_f32 v44, v12, v44\n\t" \
  "v_mfma_f32_32x32x16_bf16 v[80:95], v[172:175], a[188:191], v[80:95]\n\t" \
  "ds_read_b64_tr_b16 v[156:157], v177 offset:30720\n\t" \
  "ds_read_b64_tr_b16 v[158:159], v177 offset:31744\n\t" \
  "v_mul_f32 v45, v13, v45\n\t" \
  "v_mul_f32 v46, v14, v46\n\t" \
  "v_mul_f32 v47, v15, v47\n\t" \
  "v_mul_f32 v12, v196, v12\n\t" \
  "s_waitcnt lgkmcnt(8)\n\t" \
  "v_mfma_f32_32x32x16_bf16 v[64:79], v[128:131], a[192:195], v[64:79]\n\t" \
  "ds_read_b64_tr_b16 v[160:161], v176 offset:32768\n\t" \
  "ds_read_b64_tr_b16 v[162:163], v176 offset:33792\n\t" \
  "ds_read_b64_tr_b16 v[164:165], v177 offset:32768\n\t" \
  "ds_read_b64_tr_b16 v[166:167], v177 offset:33792\n\t" \
  "v_mul_f32 v13, v196, v13\n\t" \
  "v_mul_f32 v14, v196, v14\n\t" \
  "v_mul_f32 v15, v196, v15\n\t" \
  "v_exp_f32 v12, v12\n\t" \
  "v_mfma_f32_32x32x16_bf16 v[80:95], v[132:135], a[192:195], v[80:95]\n\t" \
  "ds_read_b64_tr_b16 v[168:169], v176 offset:34816\n\t" \
  "ds_read_b64_tr_b16 v[170:171], v176 offset:35840\n\t" \
  "ds_read_b64_tr_b16 v[172:173], v177 offset:34816\n\t" \
  "ds_read_b64_tr_b16 v[174:175], v177 offset:35840\n\t" \
  "v_exp_f32 v13, v13\n\t" \
  "v_exp_f32 v14, v14\n\t" \
  "v_exp_f32 v15, v15\n\t" \
  "v_fma_f32 v12, v12, v197, v197\n\t" \
  "v_mfma_f32_32x32x16_bf16 v[64:79], v[136:139], a[196:199], v[64:79]\n\t" \
  "v_fma_f32 v13, v13, v197, v197\n\t" \
  "v_fma_f32 v14, v14, v197, v197\n\t" \
  "v_fma_f32 v15, v15, v197, v197\n\t" \
  "v_rcp_f32 v12, v12\n\t" \
  "v_mfma_f32_32x32x16_bf16 v[80:95], v[140:143], a[196:199], v[80:95]\n\t" \
  "v_rcp_f32 v13, v13\n\t" \
  "v_rcp_f32 v14, v14\n\t" \
  "v_rcp_f32 v15, v15\n\t" \
  "v_mul_f32 v44, v44, v12\n\t" \
  "s_waitcnt lgkmcnt(8)\n\t" \
  "v_mfma_f32_32x32x16_bf16 v[64:79], v[144:147], a[200:203], v[64:79]\n\t" \
  "s_waitcnt vmcnt(9) lgkmcnt(0)\n\ts_barrier\n\t" \
  "ds_read_b64_tr_b16 v[128:129], v178 offset:0\n\t" \
  "ds_read_b64_tr_b16 v[130:131], v178 offset:1024\n\t" \
  "v_mul_f32 v45, v45, v13\n\t" \
  "v_mul_f32 v46, v46, v14\n\t" \
  "v_mul_f32 v47, v47, v15\n\t" \
  "v_cvt_pk_bf16_f32 v12, v44, v45\n\t" \
  "v_mfma_f32_32x32x16_bf16 v[80:95], v[148:151], a[200:203], v[80:95]\n\t" \
  "ds_read_b64_tr_b16 v[132:133], v179 offset:0\n\t" \
  "ds_read_b64_tr_b16 v[134:135], v179 offset:1024\n\t" \
  "s_add_u32 s41, s41, 128\n\t" \
  "s_cmp_eq_u32 s41, 1152\n\t" \
  "s_cselect_b32 s41, 0, s41\n\t" \
  "s_add_u32 s40, %9, s41\n\t" \
  "s_add_u32 m0, %16, 0\n\t" \
  "s_nop 0\n\t" \
  "buffer_load_dwordx4 v193, %1, s40 offen lds\n\t" \
  "v_cvt_pk_bf16_f32 v13, v46, v47\n\t" \
  "v_accvgpr_write_b32 a214, v12\n\t" \
  "v_accvgpr_write_b32 a215, v13\n\t" \
  "v_mfma_f32_32x32x16_bf16 v[64:79], v[152:155], a[204:207], v[64:79]\n\t" \
  "ds_read_b64_tr_b16 v[136:137], v178 offset:2048\n\t" \
  "ds_read_b64_tr_b16 v[138:139], v178 offset:3072\n\t" \
  "buffer_load_dwordx4 v194, %1, s40 offen offset:1024 lds\n\t" \
  "v_mfma_f32_32x32x16_bf16 v[80:95], v[156:159], a[204:207], v[80:95]\n\t" \
  "ds_read_b64_tr_b16 v[140:141], v179 offset:2048\n\t" \
  "ds_read_b64_tr_b16 v[142:143], v179 offset:3072\n\t" \
  "s_waitcnt lgkmcnt(8)\n\t" \
  "v_mfma_f32_32x32x16_bf16 v[64:79], v[160:163], a[208:211], v[64:79]\n\t" \
  "ds_read_b64_tr_b16 v[144:145], v178 offset:4096\n\t" \
  "ds_read_b64_tr_b16 v[146:147], v178 offset:5120\n\t" \
  "v_mfma_f32_32x32x16_bf16 v[80:95], v[164:167], a[208:211], v[80:95]\n\t" \
  "ds_read_b64_tr_b16 v[148:149], v179 offset:4096\n\t" \
  "ds_read_b64_tr_b16 v[150:151], v179 offset:5120\n\t" \
  "s_add_u32 s40, %10, s41\n\t" \
  "s_add_u32 m0, %16, 8192\n\t" \
  "v_mfma_f32_32x32x16_bf16 v[64:79], v[168:171], a[212:215], v[64:79]\n\t" \
  "ds_read_b64_tr_b16 v[152:153], v178 offset:6144\n\t" \
  "ds_read_b64_tr_b16 v[154:155], v178 offset:7168\n\t" \
  "buffer_load_dwordx4 v193, %1, s40 offen lds\n\t" \
  "v_mfma_f32_32x32x16_bf16 v[80:95], v[172:175], a[212:215], v[80:95]\n\t" \
  "ds_read_b64_tr_b16 v[156:157], v179 offset:6144\n\t" \
  "ds_read_b64_tr_b16 v[158:159], v179 offset:7168\n\t" \
  "s_waitcnt lgkmcnt(8)\n\t" \
  "v_mfma_f32_32x32x16_bf16 v[0:15], v[128:131], a[144:147], 0\n\t" \
  "ds_read_b64_tr_b16 v[160:161], v178 offset:8192\n\t" \
  "ds_read_b64_tr_b16 v[162:163], v178 offset:9216\n\t" \
  "v_mfma_f32_32x32x16_bf16 v[16:31], v[132:135], a[144:147], 0\n\t" \
  "ds_read_b64_tr_b16 v[164:165], v179 offset:8192\n\t" \
  "ds_read_b64_tr_b16 v[166:167], v179 offset:9216\n\t" \
  "s_lshl_b32 s42, %17, 12\n\t" \
  "v_mfma_f32_32x32x16_bf16 v[0:15], v[136:139], a[148:151], v[0:15]\n\t" \
  "ds_read_b64_tr_b16 v[168:169], v178 offset:10240\n\t" \
  "ds_read_b64_tr_b16 v[170:171], v178 offset:11264\n\t" \
  "buffer_load_dwordx4 v194, %1, s40 offen offset:1024 lds\n\t" \
  "v_mfma_f32_32x32x16_bf16 v[16:31], v[140:143], a[148:151], v[16:31]\n\t" \
  "ds_read_b64_tr_b16 v[172:173], v179 offset:10240\n\t" \
  "ds_read_b64_tr_b16 v[174:175], v179 offset:11264\n\t" \
  "s_waitcnt lgkmcnt(8)\n\t" \
  "v_mfma_f32_32x32x16_bf16 v[0:15], v[144:147], a[152:155], v[0:15]\n\t" \
  "ds_read_b64_tr_b16 v[128:129], v178 offset:12288\n\t" \
  "ds_read_b64_tr_b16 v[130:131], v178 offset:13312\n\t" \
  "s_add_u32 s40, %11, s41\n\t" \
  "s_add_u32 m0, %16, 16384\n\t" \
  "v_cvt_pk_bf16_f32 v208, v64, v65\n\t" \
  "v_cvt_pk_bf16_f32 v209, v66, v67\n\t" \
  "v_mfma_f32_32x32x16_bf16 v[16:31], v[148:151], a[152:155], v[16:31]\n\t" \
  "ds_read_b64_tr_b16 v[132:133], v179 offset:12288\n\t" \
  "ds_read_b64_tr_b16 v[134:135], v179 offset:13312\n\t" \
  "buffer_load_dwordx4 v193, %1, s40 offen lds\n\t" \
  "v_cvt_pk_bf16_f32 v210, v68, v69\n\t" \
  "v_mfma_f32_32x32x16_bf16 v[0:15], v[152:155], a[156:159], v[0:15]\n\t" \
  "ds_read_b64_tr_b16 v[136:137], v178 offset:14336\n\t" \
  "ds_read_b64_tr_b16 v[138:139], v178 offset:15360\n\t" \
  "v_mfma_f32_32x32x16_bf16 v[16:31], v[156:159], a[156:159], v[16:31]\n\t" \
  "ds_read_b64_tr_b16 v[140:141], v179 offset:14336\n\t" \
  "ds_read_b64_tr_b16 v[142:143], v179 offset:15360\n\t" \
  "v_cvt_pk_bf16_f32 v211, v70, v71\n\t" \
  "s_waitcnt lgkmcnt(8)\n\t" \
  "v_mfma_f32_32x32x16_bf16 v[0:15], v[160:163], a[160:163], v[0:15]\n\t" \
  "ds_read_b64_tr_b16 v[144:145], v178 offset:16384\n\t" \
  "ds_read_b64_tr_b16 v[146:147], v178 offset:17408\n\t" \
  "buffer_store_dwordx4 v[208:211], v195, %3, s42 offen\n\t" \
  "v_mfma_f32_32x32x16_bf16 v[16:31], v[164:167], a[160:163], v[16:31]\n\t" \
  "ds_read_b64_tr_b16 v[148:149], v179 offset:16384\n\t" \
  "ds_read_b64_tr_b16 v[150:151], v179 offset:17408\n\t" \
  "buffer_load_dwordx4 v194, %1, s40 offen offset:1024 lds\n\t" \
  "v_cvt_pk_bf16_f32 v212, v72, v73\n\t" \
  "v_mfma_f32_32x32x16_bf16 v[0:15], v[168:171], a[164:167], v[0:15]\n\t" \
  "ds_read_b64_tr_b16 v[152:153], v178 offset:18432\n\t" \
  "ds_read_b64_tr_b16 v[154:155], v178 offset:19456\n\t" \
  "v_cvt_pk_bf16_f32 v213, v74, v75\n\t" \
  "v_mfma_f32_32x32x16_bf16 v[16:31], v[172:175], a[164:167], v[16:31]\n\t" \
  "ds_read_b64_tr_b16 v[156:157], v179 offset:18432\n\t" \
  "ds_read_b64_tr_b16 v[158:159], v179 offset:19456\n\t" \
  "v_cvt_pk_bf16_f32 v214, v76, v77\n\t" \
  "s_waitcnt lgkmcnt(8)\n\t" \
  "v_mfma_f32_32x32x16_bf16 v[0:15], v[128:131], a[168:171], v[0:15]\n\t" \
  "ds_read_b64_tr_b16 v[160:161], v178 offset:20480\n\t" \
  "ds_read_b64_tr_b16 v[162:163], v178 offset:21504\n\t" \
  "s_add_u32 s40, %12, s41\n\t" \
  "s_add_u32 m0, %16, 24576\n\t" \
  "v_mfma_f32_32x32x16_bf16 v[16:31], v[132:135], a[168:171], v[16:31]\n\t" \
  "ds_read_b64_tr_b16 v[164:165], v179 offset:20480\n\t" \
  "ds_read_b64_tr_b16 v[166:167], v179 offset:21504\n\t" \
  "buffer_load_dwordx4 v193, %1, s40 offen lds\n\t" \
  "v_cvt_pk_bf16_f32 v215, v78, v79\n\t" \
  "v_mfma_f32_32x32x16_bf16 v[0:15], v[136:139], a[172:175], v[0:15]\n\t" \
  "ds_read_b64_tr_b16 v[168:169], v178 offset:22528\n\t" \
  "ds_read_b64_tr_b16 v[170:171], v178 offset:23552\n\t" \
  "buffer_load_dwordx4 v194, %1, s40 offen offset:1024 lds\n\t" \
  "buffer_store_dwordx4 v[212:215], v195, %3, s42 offen offset:1024\n\t" \
  "v_mfma_f32_32x32x16_bf16 v[16:31], v[140:143], a[172:175], v[16:31]\n\t" \
  "ds_read_b64_tr_b16 v[172:173], v179 offset:22528\n\t" \
  "ds_read_b64_tr_b16 v[174:175], v179 offset:23552\n\t" \
  "v_cvt_pk_bf16_f32 v216, v80, v81\n\t" \
  "s_waitcnt lgkmcnt(8)\n\t" \
  "v_mfma_f32_32x32x16_bf16 v[0:15], v[144:147], a[176:179], v[0:15]\n\t" \
  "ds_read_b64_tr_b16 v[128:129], v178 offset:24576\n\t" \
  "ds_read_b64_tr_b16 v[130:131], v178 offset:25600\n\t" \
  "v_cvt_pk_bf16_f32 v217, v82, v83\n\t" \
  "v_mfma_f32_32x32x16_bf16 v[16:31], v[148:151], a[176:179], v[16:31]\n\t" \
  "ds_read_b64_tr_b16 v[132:133], v179 offset:24576\n\t" \
  "ds_read_b64_tr_b16 v[134:135], v179 offset:25600\n\t" \
  "s_add_u32 s40, %13, s41\n\t" \
  "s_add_u32 m0, %25, 0\n\t" \
  "v_cvt_pk_bf16_f32 v218, v84, v85\n\t" \
  "v_mfma_f32_32x32x16_bf16 v[0:15], v[152:155], a[180:183], v[0:15]\n\t" \
  "ds_read_b64_tr_b16 v[136:137], v178 offset:26624\n\t" \
  "ds_read_b64_tr_b16 v[138:139], v178 offset:27648\n\t" \
  "buffer_load_dwordx4 v193, %1, s40 offen lds\n\t" \
  "v_mfma_f32_32x32x16_bf16 v[16:31], v[156:159], a[180:183], v[16:31]\n\t" \
  "ds_read_b64_tr_b16 v[140:141], v179 offset:26624\n\t" \
  "ds_read_b64_tr_b16 v[142:143], v179 offset:27648\n\t" \
  "v_cvt_pk_bf16_f32 v219, v86, v87\n\t" \
  "s_waitcnt lgkmcnt(8)\n\t" \
  "v_mfma_f32_32x32x16_bf16 v[0:15], v[160:163], a[184:187], v[0:15]\n\t" \
  "ds_read_b64_tr_b16 v[144:145], v178 offset:28672\n\t" \
  "ds_read_b64_tr_b16 v[146:147], v178 offset:29696\n\t" \
  "buffer_store_dwordx4 v[216:219], v195, %3, s42 offen offset:2048\n\t" \
  "v_mfma_f32_32x32x16_bf16 v[16:31], v[164:167], a[184:187], v[16:31]\n\t" \
  "ds_read_b64_tr_b16 v[148:149], v179 offset:28672\n\t" \
  "ds_read_b64_tr_b16 v[150:151], v179 offset:29696\n\t" \
  "v_cvt_pk_bf16_f32 v220, v88, v89\n\t" \
  "v_mfma_f32_32x32x16_bf16 v[0:15], v[168:171], a[188:191], v[0:15]\n\t" \
  "ds_read_b64_tr_b16 v[152:153], v178 offset:30720\n\t" \
  "ds_read_b64_tr_b16 v[154:155], v178 offset:31744\n\t" \
  "v_cvt_pk_bf16_f32 v221, v90, v91\n\t" \
  "v_mfma_f32_32x32x16_bf16 v[16:31], v[172:175], a[188:191], v[16:31]\n\t" \
  "ds_read_b64_tr_b16 v[156:157], v179 offset:30720\n\t" \
  "ds_read_b64_tr_b16 v[158:159], v179 offset:31744\n\t" \
  "v_cvt_pk_bf16_f32 v222, v92, v93\n\t" \
  "s_waitcnt lgkmcnt(8)\n\t" \
  "v_mfma_f32_32x32x16_bf16 v[0:15], v[128:131], a[192:195], v[0:15]\n\t" \
  "ds_read_b64_tr_b16 v[160:161], v178 offset:32768\n\t" \
  "ds_read_b64_tr_b16 v[162:163], v178 offset:33792\n\t" \
  "ds_read_b64_tr_b16 v[164:165], v179 offset:32768\n\t" \
  "ds_read_b64_tr_b16 v[166:167], v179 offset:33792\n\t" \
  "v_mfma_f32_32x32x16_bf16 v[16:31], v[132:135], a[192:195], v[16:31]\n\t" \
  "ds_read_b64_tr_b16 v[168:169], v178 offset:34816\n\t" \
  "ds_read_b64_tr_b16 v[170:171], v178 offset:35840\n\t" \
  "ds_read_b64_tr_b16 v[172:173], v179 offset:34816\n\t" \
  "ds_read_b64_tr_b16 v[174:175], v179 offset:35840\n\t" \
  "v_cvt_pk_bf16_f32 v223, v94, v95\n\t" \
  "v_mfma_f32_32x32x16_bf16 v[0:15], v[136:139], a[196:199], v[0:15]\n\t" \
  "buffer_store_dwordx4 v[220:223], v195, %3, s42 offen offset:3072\n\t" \
  "v_mfma_f32_32x32x16_bf16 v[16:31], v[140:143], a[196:199], v[16:31]\n\t" \
  "s_waitcnt lgkmcnt(8)\n\t" \
  "v_mfma_f32_32x32x16_bf16 v[0:15], v[144:147], a[200:203], v[0:15]\n\t" \
  "s_waitcnt vmcnt(13) lgkmcnt(0)\n\ts_barrier\n\t" \
  "ds_read_b64_tr_b16 v[128:129], v180 offset:0\n\t" \
  "ds_read_b64_tr_b16 v[130:131], v180 offset:1024\n\t" \
  "v_mfma_f32_32x32x16_bf16 v[16:31], v[148:151], a[200:203], v[16:31]\n\t" \
  "ds_read_b64_tr_b16 v[132:133], v181 offset:0\n\t" \
  "ds_read_b64_tr_b16 v[134:135], v181 offset:1024\n\t" \
  "s_add_u32 s41, s41, 128\n\t" \
  "s_cmp_eq_u32 s41, 1152\n\t" \
  "s_cselect_b32 s41, 0, s41\n\t" \
  "s_add_u32 s40, %9, s41\n\t" \
  "s_add_u32 m0, %16, 49152\n\t" \
  "s_nop 0\n\t" \
  "buffer_load_dwordx4 v193, %1, s40 offen lds\n\t" \
  "v_mfma_f32_32x32x16_bf16 v[0:15], v[152:155], a[204:207], v[0:15]\n\t" \
  "ds_read_b64_tr_b16 v[136:137], v180 offset:2048\n\t" \
  "ds_read_b64_tr_b16 v[138:139], v180 offset:3072\n\t" \
  "buffer_load_dwordx4 v194, %1, s40 offen offset:1024 lds\n\t" \
  "v_mfma_f32_32x32x16_bf16 v[16:31], v[156:159], a[204:207], v[16:31]\n\t" \
  "ds_read_b64_tr_b16 v[140:141], v181 offset:2048\n\t" \
  "ds_read_b64_tr_b16 v[142:143], v181 offset:3072\n\t" \
  "s_waitcnt lgkmcnt(8)\n\t" \
  "v_mfma_f32_32x32x16_bf16 v[0:15], v[160:163], a[208:211], v[0:15]\n\t" \
  "ds_read_b64_tr_b16 v[144:145], v180 offset:4096\n\t" \
  "ds_read_b64_tr_b16 v[146:147], v180 offset:5120\n\t" \
  "v_mfma_f32_32x32x16_bf16 v[16:31], v[164:167], a[208:211], v[16:31]\n\t" \
  "ds_read_b64_tr_b16 v[148:149], v181 offset:4096\n\t" \
  "ds_read_b64_tr_b16 v[150:151], v181 offset:5120\n\t" \
  "s_add_u32 s40, %10, s41\n\t" \
  "s_add_u32 m0, %16, 57344\n\t" \
  "v_mfma_f32_32x32x16_bf16 v[0:15], v[168:171], a[212:215], v[0:15]\n\t" \
  "ds_read_b64_tr_b16 v[152:153], v180 offset:6144\n\t" \
  "ds_read_b64_tr_b16 v[154:155], v180 offset:7168\n\t" \
  "buffer_load_dwordx4 v193, %1, s40 offen lds\n\t" \
  "v_mfma_f32_32x32x16_bf16 v[16:31], v[172:175], a[212:215], v[16:31]\n\t" \
  "ds_read_b64_tr_b16 v[156:157], v181 offset:6144\n\t" \
  "ds_read_b64_tr_b16 v[158:159], v181 offset:7168\n\t" \
  "s_waitcnt lgkmcnt(8)\n\t" \
  "v_mfma_f32_32x32x16_bf16 v[64:79], v[128:131], a[144:147], 0\n\t" \
  "ds_read_b64_tr_b16 v[160:161], v180 offset:8192\n\t" \
  "ds_read_b64_tr_b16 v[162:163], v180 offset:9216\n\t" \
  "v_mfma_f32_32x32x16_bf16 v[80:95], v[132:135], a[144:147], 0\n\t" \
  "ds_read_b64_tr_b16 v[164:165], v181 offset:8192\n\t" \
  "ds_read_b64_tr_b16 v[166:167], v181 offset:9216\n\t" \
  "s_add_u32 s42, s42, 4096\n\t" \
  "s_cmp_eq_u32 s42, 36864\n\t" \
  "s_cselect_b32 s42, 0, s42\n\t" \
  "v_mfma_f32_32x32x16_bf16 v[64:79], v[136:139], a[148:151], v[64:79]\n\t" \
  "ds_read_b64_tr_b16 v[168:169], v180 offset:10240\n\t" \
  "ds_read_b64_tr_b16 v[170:171], v180 offset:11264\n\t" \
  "buffer_load_dwordx4 v194, %1, s40 offen offset:1024 lds\n\t" \
  "v_mfma_f32_32x32x16_bf16 v[80:95], v[140:143], a[148:151], v[80:95]\n\t" \
  "ds_read_b64_tr_b16 v[172:173], v181 offset:10240\n\t" \
  "ds_read_b64_tr_b16 v[174:175], v181 offset:11264\n\t" \
  "s_waitcnt lgkmcnt(8)\n\t" \
  "v_mfma_f32_32x32x16_bf16 v[64:79], v[144:147], a[152:155], v[64:79]\n\t" \
  "ds_read_b64_tr_b16 v[128:129], v180 offset:12288\n\t" \
  "ds_read_b64_tr_b16 v[130:131], v180 offset:13312\n\t" \
  "s_add_u32 s40, %11, s41\n\t" \
  "s_add_u32 m0, %16, 65536\n\t" \
  "v_cvt_pk_bf16_f32 v208, v0, v1\n\t" \
  "v_cvt_pk_bf16_f32 v209, v2, v3\n\t" \
  "v_mfma_f32_32x32x16_bf16 v[80:95], v[148:151], a[152:155], v[80:95]\n\t" \
  "ds_read_b64_tr_b16 v[132:133], v181 offset:12288\n\t" \
  "ds_read_b64_tr_b16 v[134:135], v181 offset:13312\n\t" \
  "buffer_load_dwordx4 v193, %1, s40 offen lds\n\t" \
  "v_cvt_pk_bf16_f32 v210, v4, v5\n\t" \
  "v_mfma_f32_32x32x16_bf16 v[64:79], v[152:155], a[156:159], v[64:79]\n\t" \
  "ds_read_b64_tr_b16 v[136:137], v180 offset:14336\n\t" \
  "ds_read_b64_tr_b16 v[138:139], v180 offset:15360\n\t" \
  "v_mfma_f32_32x32x16_bf16 v[80:95], v[156:159], a[156:159], v[80:95]\n\t" \
  "ds_read_b64_tr_b16 v[140:141], v181 offset:14336\n\t" \
  "ds_read_b64_tr_b16 v[142:143], v181 offset:15360\n\t" \
  "v_cvt_pk_bf16_f32 v211, v6, v7\n\t" \
  "s_waitcnt lgkmcnt(8)\n\t" \
  "v_mfma_f32_32x32x16_bf16 v[64:79], v[160:163], a[160:163], v[64:79]\n\t" \
  "ds_read_b64_tr_b16 v[144:145], v180 offset:16384\n\t" \
  "ds_read_b64_tr_b16 v[146:147], v180 offset:17408\n\t" \
  "buffer_store_dwordx4 v[208:211], v195, %3, s42 offen\n\t" \
  "v_mfma_f32_32x32x16_bf16 v[80:95], v[164:167], a[160:163], v[80:95]\n\t" \
  "ds_read_b64_tr_b16 v[148:149], v181 offset:16384\n\t" \
  "ds_read_b64_tr_b16 v[150:151], v181 offset:17408\n\t" \
  "buffer_load_dwordx4 v194, %1, s40 offen offset:1024 lds\n\t" \
  "v_cvt_pk_bf16_f32 v212, v8, v9\n\t" \
  "v_mfma_f32_32x32x16_bf16 v[64:79], v[168:171], a[164:167], v[64:79]\n\t" \
  "ds_read_b64_tr_b16 v[152:153], v180 offset:18432\n\t" \
  "ds_read_b64_tr_b16 v[154:155], v180 offset:19456\n\t" \
  "v_cvt_pk_bf16_f32 v213, v10, v11\n\t" \
  "v_mfma_f32_32x32x16_bf16 v[80:95], v[172:175], a[164:167], v[80:95]\n\t" \
  "ds_read_b64_tr_b16 v[156:157], v181 offset:18432\n\t" \
  "ds_read_b64_tr_b16 v[158:159], v181 offset:19456\n\t" \
  "v_cvt_pk_bf16_f32 v214, v12, v13\n\t" \
  "s_waitcnt lgkmcnt(8)\n\t" \
  "v_mfma_f32_32x32x16_bf16 v[64:79], v[128:131], a[168:171], v[64:79]\n\t" \
  "ds_read_b64_tr_b16 v[160:161], v180 offset:20480\n\t" \
  "ds_read_b64_tr_b16 v[162:163], v180 offset:21504\n\t" \
  "s_add_u32 s40, %12, s41\n\t" \
  "s_add_u32 m0, %16, 73728\n\t" \
  "v_mfma_f32_32x32x16_bf16 v[80:95], v[132:135], a[168:171], v[80:95]\n\t" \
  "ds_read_b64_tr_b16 v[164:165], v181 offset:20480\n\t" \
  "ds_read_b64_tr_b16 v[166:167], v181 offset:21504\n\t" \
  "buffer_load_dwordx4 v193, %1, s40 offen lds\n\t" \
  "v_cvt_pk_bf16_f32 v215, v14, v15\n\t" \
  "v_mfma_f32_32x32x16_bf16 v[64:79], v[136:139], a[172:175], v[64:79]\n\t" \
  "ds_read_b64_tr_b16 v[168:169], v180 offset:22528\n\t" \
  "ds_read_b64_tr_b16 v[170:171], v180 offset:23552\n\t" \
  "buffer_load_dwordx4 v194, %1, s40 offen offset:1024 lds\n\t" \
  "buffer_store_dwordx4 v[212:215], v195, %3, s42 offen offset:1024\n\t" \
  "v_mfma_f32_32x32x16_bf16 v[80:95], v[140:143], a[172:175], v[80:95]\n\t" \
  "ds_read_b64_tr_b16 v[172:173], v181 offset:22528\n\t" \
  "ds_read_b64_tr_b16 v[174:175], v181 offset:23552\n\t" \
  "v_cvt_pk_bf16_f32 v216, v16, v17\n\t" \
  "s_waitcnt lgkmcnt(8)\n\t" \
  "v_mfma_f32_32x32x16_bf16 v[64:79], v[144:147], a[176:179], v[64:79]\n\t" \
  "ds_read_b64_tr_b16 v[128:129], v180 offset:24576\n\t" \
  "ds_read_b64_tr_b16 v[130:131], v180 offset:25600\n\t" \
  "v_cvt_pk_bf16_f32 v217, v18, v19\n\t" \
  "v_mfma_f32_32x32x16_bf16 v[80:95], v[148:151], a[176:179], v[80:95]\n\t" \
  "ds_read_b64_tr_b16 v[132:133], v181 offset:24576\n\t" \
  "ds_read_b64_tr_b16 v[134:135], v181 offset:25600\n\t" \
  "s_add_u32 s40, %13, s41\n\t" \
  "s_add_u32 m0, %25, 49152\n\t" \
  "v_cvt_pk_bf16_f32 v218, v20, v21\n\t" \
  "v_mfma_f32_32x32x16_bf16 v[64:79], v[152:155], a[180:183], v[64:79]\n\t" \
  "ds_read_b64_tr_b16 v[136:137], v180 offset:26624\n\t" \
  "ds_read_b64_tr_b16 v[138:139], v180 offset:27648\n\t" \
  "buffer_load_dwordx4 v193, %1, s40 offen lds\n\t" \
  "v_mfma_f32_32x32x16_bf16 v[80:95], v[156:159], a[180:183], v[80:95]\n\t" \
  "ds_read_b64_tr_b16 v[140:141], v181 offset:26624\n\t" \
  "ds_read_b64_tr_b16 v[142:143], v181 offset:27648\n\t" \
  "v_cvt_pk_bf16_f32 v219, v22, v23\n\t" \
  "s_waitcnt lgkmcnt(8)\n\t" \
  "v_mfma_f32_32x32x16_bf16 v[64:79], v[160:163], a[184:187], v[64:79]\n\t" \
  "ds_read_b64_tr_b16 v[144:145], v180 offset:28672\n\t" \
  "ds_read_b64_tr_b16 v[146:147], v180 offset:29696\n\t" \
  "buffer_store_dwordx4 v[216:219], v195, %3, s42 offen offset:2048\n\t" \
  "v_mfma_f32_32x32x16_bf16 v[80:95], v[164:167], a[184:187], v[80:95]\n\t" \
  "ds_read_b64_tr_b16 v[148:149], v181 offset:28672\n\t" \
  "ds_read_b64_tr_b16 v[150:151], v181 offset:29696\n\t" \
  "v_cvt_pk_bf16_f32 v220, v24, v25\n\t" \
  "v_mfma_f32_32x32x16_bf16 v[64:79], v[168:171], a[188:191], v[64:79]\n\t" \
  "ds_read_b64_tr_b16 v[152:153], v180 offset:30720\n\t" \
  "ds_read_b64_tr_b16 v[154:155], v180 offset:31744\n\t" \
  "v_cvt_pk_bf16_f32 v221, v26, v27\n\t" \
  "v_mfma_f32_32x32x16_bf16 v[80:95], v[172:175], a[188:191], v[80:95]\n\t" \
  "ds_read_b64_tr_b16 v[156:157], v181 offset:30720\n\t" \
  "ds_read_b64_tr_b16 v[158:159], v181 offset:31744\n\t" \
  "v_cvt_pk_bf16_f32 v222, v28, v29\n\t" \
  "s_waitcnt lgkmcnt(8)\n\t" \
  "v_mfma_f32_32x32x16_bf16 v[64:79], v[128:131], a[192:195], v[64:79]\n\t" \
  "ds_read_b64_tr_b16 v[160:161], v180 offset:32768\n\t" \
  "ds_read_b64_tr_b16 v[162:163], v180 offset:33792\n\t" \
  "ds_read_b64_tr_b16 v[164:165], v181 offset:32768\n\t" \
  "ds_read_b64_tr_b16 v[166:167], v181 offset:33792\n\t" \
  "v_mfma_f32_32x32x16_bf16 v[80:95], v[132:135], a[192:195], v[80:95]\n\t" \
  "ds_read_b64_tr_b16 v[168:169], v180 offset:34816\n\t" \
  "ds_read_b64_tr_b16 v[170:171], v180 offset:35840\n\t" \
  "ds_read_b64_tr_b16 v[172:173], v181 offset:34816\n\t" \
  "ds_read_b64_tr_b16 v[174:175], v181 offset:35840\n\t" \
  "v_cvt_pk_bf16_f32 v223, v30, v31\n\t" \
  "v_mfma_f32_32x32x16_bf16 v[64:79], v[136:139], a[196:199], v[64:79]\n\t" \
  "buffer_store_dwordx4 v[220:223], v195, %3, s42 offen offset:3072\n\t" \
  "v_mfma_f32_32x32x16_bf16 v[80:95], v[140:143], a[196:199], v[80:95]\n\t" \
  "s_waitcnt lgkmcnt(8)\n\t" \
  "v_mfma_f32_32x32x16_bf16 v[64:79], v[144:147], a[200:203], v[64:79]\n\t" \
  "s_waitcnt vmcnt(15) lgkmcnt(0)\n\ts_barrier\n\t" \
  "ds_read_b64_tr_b16 v[128:129], v176 offset:0\n\t" \
  "ds_read_b64_tr_b16 v[130:131], v176 offset:1024\n\t" \
  "v_mfma_f32_32x32x16_bf16 v[80:95], v[148:151], a[200:203], v[80:95]\n\t" \
  "ds_read_b64_tr_b16 v[132:133], v177 offset:0\n\t" \
  "ds_read_b64_tr_b16 v[134:135], v177 offset:1024\n\t" \
  "s_add_u32 s41, s41, 128\n\t" \
  "s_cmp_eq_u32 s41, 1152\n\t" \
  "s_cselect_b32 s41, 0, s41\n\t" \
  "s_add_u32 s40, %9, s41\n\t" \
  "s_add_u32 m0, %16, 98304\n\t" \
  "s_nop 0\n\t" \
  "buffer_load_dwordx4 v193, %1, s40 offen lds\n\t" \
  "v_mfma_f32_32x32x16_bf16 v[64:79], v[152:155], a[204:207], v[64:79]\n\t" \
  "ds_read_b64_tr_b16 v[136:137], v176 offset:2048\n\t" \
  "ds_read_b64_tr_b16 v[138:139], v176 offset:3072\n\t" \
  "buffer_load_dwordx4 v194, %1, s40 offen offset:1024 lds\n\t" \
  "v_mfma_f32_32x32x16_bf16 v[80:95], v[156:159], a[204:207], v[80:95]\n\t" \
  "ds_read_b64_tr_b16 v[140:141], v177 offset:2048\n\t" \
  "ds_read_b64_tr_b16 v[142:143], v177 offset:3072\n\t" \
  "s_waitcnt lgkmcnt(8)\n\t" \
  "v_mfma_f32_32x32x16_bf16 v[64:79], v[160:163], a[208:211], v[64:79]\n\t" \
  "ds_read_b64_tr_b16 v[144:145], v176 offset:4096\n\t" \
  "ds_read_b64_tr_b16 v[146:147], v176 offset:5120\n\t" \
  "v_mfma_f32_32x32x16_bf16 v[80:95], v[164:167], a[208:211], v[80:95]\n\t" \
  "ds_read_b64_tr_b16 v[148:149], v177 offset:4096\n\t" \
  "ds_read_b64_tr_b16 v[150:151], v177 offset:5120\n\t" \
  "s_add_u32 s40, %10, s41\n\t" \
  "s_add_u32 m0, %16, 106496\n\t" \
  "v_mfma_f32_32x32x16_bf16 v[64:79], v[168:171], a[212:215], v[64:79]\n\t" \
  "ds_read_b64_tr_b16 v[152:153], v176 offset:6144\n\t" \
  "ds_read_b64_tr_b16 v[154:155], v176 offset:7168\n\t" \
  "buffer_load_dwordx4 v193, %1, s40 offen lds\n\t" \
  "v_mfma_f32_32x32x16_bf16 v[80:95], v[172:175], a[212:215], v[80:95]\n\t" \
  "ds_read_b64_tr_b16 v[156:157], v177 offset:6144\n\t" \
  "ds_read_b64_tr_b16 v[158:159], v177 offset:7168\n\t" \
  "s_waitcnt lgkmcnt(8)\n\t" \
  "v_mfma_f32_32x32x16_bf16 v[0:15], v[128:131], a[144:147], 0\n\t" \
  "ds_read_b64_tr_b16 v[160:161], v176 offset:8192\n\t" \
  "ds_read_b64_tr_b16 v[162:163], v176 offset:9216\n\t" \
  "v_mfma_f32_32x32x16_bf16 v[16:31], v[132:135], a[144:147], 0\n\t" \
  "ds_read_b64_tr_b16 v[164:165], v177 offset:8192\n\t" \
  "ds_read_b64_tr_b16 v[166:167], v177 offset:9216\n\t" \
  "s_add_u32 s42, s42, 4096\n\t" \
  "s_cmp_eq_u32 s42, 36864\n\t" \
  "s_cselect_b32 s42, 0, s42\n\t" \
  "v_mfma_f32_32x32x16_bf16 v[0:15], v[136:139], a[148:151], v[0:15]\n\t" \
  "ds_read_b64_tr_b16 v[168:169], v176 offset:10240\n\t" \
  "ds_read_b64_tr_b16 v[170:171], v176 offset:11264\n\t" \
  "buffer_load_dwordx4 v194, %1, s40 offen offset:1024 lds\n\t" \
  "v_mfma_f32_32x32x16_bf16 v[16:31], v[140:143], a[148:151], v[16:31]\n\t" \
  "ds_read_b64_tr_b16 v[172:173], v177 offset:10240\n\t" \
  "ds_read_b64_tr_b16 v[174:175], v177 offset:11264\n\t" \
  "s_waitcnt lgkmcnt(8)\n\t" \
  "v_mfma_f32_32x32x16_bf16 v[0:15], v[144:147], a[152:155], v[0:15]\n\t" \
  "ds_read_b64_tr_b16 v[128:129], v176 offset:12288\n\t" \
  "ds_read_b64_tr_b16 v[130:131], v176 offset:13312\n\t" \
  "s_add_u32 s40, %11, s41\n\t" \
  "s_add_u32 m0, %16, 114688\n\t" \
  "v_cvt_pk_bf16_f32 v208, v64, v65\n\t" \
  "v_cvt_pk_bf16_f32 v209, v66, v67\n\t" \
  "v_mfma_f32_32x32x16_bf16 v[16:31], v[148:151], a[152:155], v[16:31]\n\t" \
  "ds_read_b64_tr_b16 v[132:133], v177 offset:12288\n\t" \
  "ds_read_b64_tr_b16 v[134:135], v177 offset:13312\n\t" \
  "buffer_load_dwordx4 v193, %1, s40 offen lds\n\t" \
  "v_cvt_pk_bf16_f32 v210, v68, v69\n\t" \
  "v_mfma_f32_32x32x16_bf16 v[0:15], v[152:155], a[156:159], v[0:15]\n\t" \
  "ds_read_b64_tr_b16 v[136:137], v176 offset:14336\n\t" \
  "ds_read_b64_tr_b16 v[138:139], v176 offset:15360\n\t" \
  "v_mfma_f32_32x32x16_bf16 v[16:31], v[156:159], a[156:159], v[16:31]\n\t" \
  "ds_read_b64_tr_b16 v[140:141], v177 offset:14336\n\t" \
  "ds_read_b64_tr_b16 v[142:143], v177 offset:15360\n\t" \
  "v_cvt_pk_bf16_f32 v211, v70, v71\n\t" \
  "s_waitcnt lgkmcnt(8)\n\t" \
  "v_mfma_f32_32x32x16_bf16 v[0:15], v[160:163], a[160:163], v[0:15]\n\t" \
  "ds_read_b64_tr_b16 v[144:145], v176 offset:16384\n\t" \
  "ds_read_b64_tr_b16 v[146:147], v176 offset:17408\n\t" \
  "buffer_store_dwordx4 v[208:211], v195, %3, s42 offen\n\t" \
  "v_mfma_f32_32x32x16_bf16 v[16:31], v[164:167], a[160:163], v[16:31]\n\t" \
  "ds_read_b64_tr_b16 v[148:149], v177 offset:16384\n\t" \
  "ds_read_b64_tr_b16 v[150:151], v177 offset:17408\n\t" \
  "buffer_load_dwordx4 v194, %1, s40 offen offset:1024 lds\n\t" \
  "v_cvt_pk_bf16_f32 v212, v72, v73\n\t" \
  "v_mfma_f32_32x32x16_bf16 v[0:15], v[168:171], a[164:167], v[0:15]\n\t" \
  "ds_read_b64_tr_b16 v[152:153], v176 offset:18432\n\t" \
  "ds_read_b64_tr_b16 v[154:155], v176 offset:19456\n\t" \
  "v_cvt_pk_bf16_f32 v213, v74, v75\n\t" \
  "v_mfma_f32_32x32x16_bf16 v[16:31], v[172:175], a[164:167], v[16:31]\n\t" \
  "ds_read_b64_tr_b16 v[156:157], v177 offset:18432\n\t" \
  "ds_read_b64_tr_b16 v[158:159], v177 offset:19456\n\t" \
  "v_cvt_pk_bf16_f32 v214, v76, v77\n\t" \
  "s_waitcnt lgkmcnt(8)\n\t" \
  "v_mfma_f32_32x32x16_bf16 v[0:15], v[128:131], a[168:171], v[0:15]\n\t" \
  "ds_read_b64_tr_b16 v[160:161], v176 offset:20480\n\t" \
  "ds_read_b64_tr_b16 v[162:163], v176 offset:21504\n\t" \
  "s_add_u32 s40, %12, s41\n\t" \
  "s_add_u32 m0, %16, 122880\n\t" \
  "v_mfma_f32_32x32x16_bf16 v[16:31], v[132:135], a[168:171], v[16:31]\n\t" \
  "ds_read_b64_tr_b16 v[164:165], v177 offset:20480\n\t" \
  "ds_read_b64_tr_b16 v[166:167], v177 offset:21504\n\t" \
  "buffer_load_dwordx4 v193, %1, s40 offen lds\n\t" \
  "v_cvt_pk_bf16_f32 v215, v78, v79\n\t" \
  "v_mfma_f32_32x32x16_bf16 v[0:15], v[136:139], a[172:175], v[0:15]\n\t" \
  "ds_read_b64_tr_b16 v[168:169], v176 offset:22528\n\t" \
  "ds_read_b64_tr_b16 v[170:171], v176 offset:23552\n\t" \
  "buffer_load_dwordx4 v194, %1, s40 offen offset:1024 lds\n\t" \
  "buffer_store_dwordx4 v[212:215], v195, %3, s42 offen offset:1024\n\t" \
  "v_mfma_f32_32x32x16_bf16 v[16:31], v[140:143], a[172:175], v[16:31]\n\t" \
  "ds_read_b64_tr_b16 v[172:173], v177 offset:22528\n\t" \
  "ds_read_b64_tr_b16 v[174:175], v177 offset:23552\n\t" \
  "v_cvt_pk_bf16_f32 v216, v80, v81\n\t" \
  "s_waitcnt lgkmcnt(8)\n\t" \
  "v_mfma_f32_32x32x16_bf16 v[0:15], v[144:147], a[176:179], v[0:15]\n\t" \
  "ds_read_b64_tr_b16 v[128:129], v176 offset:24576\n\t" \
  "ds_read_b64_tr_b16 v[130:131], v176 offset:25600\n\t" \
  "v_cvt_pk_bf16_f32 v217, v82, v83\n\t" \
  "v_mfma_f32_32x32x16_bf16 v[16:31], v[148:151], a[176:179], v[16:31]\n\t" \
  "ds_read_b64_tr_b16 v[132:133], v177 offset:24576\n\t" \
  "ds_read_b64_tr_b16 v[134:135], v177 offset:25600\n\t" \
  "s_add_u32 s40, %13, s41\n\t" \
  "s_add_u32 m0, %25, 98304\n\t" \
  "v_cvt_pk_bf16_f32 v218, v84, v85\n\t" \
  "v_mfma_f32_32x32x16_bf16 v[0:15], v[152:155], a[180:183], v[0:15]\n\t" \
  "ds_read_b64_tr_b16 v[136:137], v176 offset:26624\n\t" \
  "ds_read_b64_tr_b16 v[138:139], v176 offset:27648\n\t" \
  "buffer_load_dwordx4 v193, %1, s40 offen lds\n\t" \
  "v_mfma_f32_32x32x16_bf16 v[16:31], v[156:159], a[180:183], v[16:31]\n\t" \
  "ds_read_b64_tr_b16 v[140:141], v177 offset:26624\n\t" \
  "ds_read_b64_tr_b16 v[142:143], v177 offset:27648\n\t" \
  "v_cvt_pk_bf16_f32 v219, v86, v87\n\t" \
  "s_waitcnt lgkmcnt(8)\n\t" \
  "v_mfma_f32_32x32x16_bf16 v[0:15], v[160:163], a[184:187], v[0:15]\n\t" \
  "ds_read_b64_tr_b16 v[144:145], v176 offset:28672\n\t" \
  "ds_read_b64_tr_b16 v[146:147], v176 offset:29696\n\t" \
  "buffer_store_dwordx4 v[216:219], v195, %3, s42 offen offset:2048\n\t" \
  "v_mfma_f32_32x32x16_bf16 v[16:31], v[164:167], a[184:187], v[16:31]\n\t" \
  "ds_read_b64_tr_b16 v[148:149], v177 offset:28672\n\t" \
  "ds_read_b64_tr_b16 v[150:151], v177 offset:29696\n\t" \
  "v_cvt_pk_bf16_f32 v220, v88, v89\n\t" \
  "v_mfma_f32_32x32x16_bf16 v[0:15], v[168:171], a[188:191], v[0:15]\n\t" \
  "ds_read_b64_tr_b16 v[152:153], v176 offset:30720\n\t" \
  "ds_read_b64_tr_b16 v[154:155], v176 offset:31744\n\t" \
  "v_cvt_pk_bf16_f32 v221, v90, v91\n\t" \
  "v_mfma_f32_32x32x16_bf16 v[16:31], v[172:175], a[188:191], v[16:31]\n\t" \
  "ds_read_b64_tr_b16 v[156:157], v177 offset:30720\n\t" \
  "ds_read_b64_tr_b16 v[158:159], v177 offset:31744\n\t" \
  "v_cvt_pk_bf16_f32 v222, v92, v93\n\t" \
  "s_waitcnt lgkmcnt(8)\n\t" \
  "v_mfma_f32_32x32x16_bf16 v[0:15], v[128:131], a[192:195], v[0:15]\n\t" \
  "ds_read_b64_tr_b16 v[160:161], v176 offset:32768\n\t" \
  "ds_read_b64_tr_b16 v[162:163], v176 offset:33792\n\t" \
  "ds_read_b64_tr_b16 v[164:165], v177 offset:32768\n\t" \
  "ds_read_b64_tr_b16 v[166:167], v177 offset:33792\n\t" \
  "v_mfma_f32_32x32x16_bf16 v[16:31], v[132:135], a[192:195], v[16:31]\n\t" \
  "ds_read_b64_tr_b16 v[168:169], v176 offset:34816\n\t" \
  "ds_read_b64_tr_b16 v[170:171], v176 offset:35840\n\t" \
  "ds_read_b64_tr_b16 v[172:173], v177 offset:34816\n\t" \
  "ds_read_b64_tr_b16 v[174:175], v177 offset:35840\n\t" \
  "v_cvt_pk_bf16_f32 v223, v94, v95\n\t" \
  "v_mfma_f32_32x32x16_bf16 v[0:15], v[136:139], a[196:199], v[0:15]\n\t" \
  "buffer_store_dwordx4 v[220:223], v195, %3, s42 offen offset:3072\n\t" \
  "v_mfma_f32_32x32x16_bf16 v[16:31], v[140:143], a[196:199], v[16:31]\n\t" \
  "s_waitcnt lgkmcnt(8)\n\t" \
  "v_mfma_f32_32x32x16_bf16 v[0:15], v[144:147], a[200:203], v[0:15]\n\t" \
  "s_waitcnt vmcnt(15) lgkmcnt(0)\n\ts_barrier\n\t" \
  "ds_read_b64_tr_b16 v[128:129], v178 offset:0\n\t" \
  "ds_read_b64_tr_b16 v[130:131], v178 offset:1024\n\t" \
  "v_mfma_f32_32x32x16_bf16 v[16:31], v[148:151], a[200:203], v[16:31]\n\t" \
  "ds_read_b64_tr_b16 v[132:133], v179 offset:0\n\t" \
  "ds_read_b64_tr_b16 v[134:135], v179 offset:1024\n\t" \
  "s_add_u32 s41, s41, 128\n\t" \
  "s_cmp_eq_u32 s41, 1152\n\t" \
  "s_cselect_b32 s41, 0, s41\n\t" \
  "s_add_u32 s40, %9, s41\n\t" \
  "s_add_u32 m0, %16, 0\n\t" \
  "s_nop 0\n\t" \
  "buffer_load_dwordx4 v193, %1, s40 offen lds\n\t" \
  "v_mfma_f32_32x32x16_bf16 v[0:15], v[152:155], a[204:207], v[0:15]\n\t" \
  "ds_read_b64_tr_b16 v[136:137], v178 offset:2048\n\t" \
  "ds_read_b64_tr_b16 v[138:139], v178 offset:3072\n\t" \
  "buffer_load_dwordx4 v194, %1, s40 offen offset:1024 lds\n\t" \
  "v_mfma_f32_32x32x16_bf16 v[16:31], v[156:159], a[204:207], v[16:31]\n\t" \
  "ds_read_b64_tr_b16 v[140:141], v179 offset:2048\n\t" \
  "ds_read_b64_tr_b16 v[142:143], v179 offset:3072\n\t" \
  "s_waitcnt lgkmcnt(8)\n\t" \
  "v_mfma_f32_32x32x16_bf16 v[0:15], v[160:163], a[208:211], v[0:15]\n\t" \
  "ds_read_b64_tr_b16 v[144:145], v178 offset:4096\n\t" \
  "ds_read_b64_tr_b16 v[146:147], v178 offset:5120\n\t" \
  "v_mfma_f32_32x32x16_bf16 v[16:31], v[164:167], a[208:211], v[16:31]\n\t" \
  "ds_read_b64_tr_b16 v[148:149], v179 offset:4096\n\t" \
  "ds_read_b64_tr_b16 v[150:151], v179 offset:5120\n\t" \
  "s_add_u32 s40, %10, s41\n\t" \
  "s_add_u32 m0, %16, 8192\n\t" \
  "v_mfma_f32_32x32x16_bf16 v[0:15], v[168:171], a[212:215], v[0:15]\n\t" \
  "ds_read_b64_tr_b16 v[152:153], v178 offset:6144\n\t" \
  "ds_read_b64_tr_b16 v[154:155], v178 offset:7168\n\t" \
  "buffer_load_dwordx4 v193, %1, s40 offen lds\n\t" \
  "v_mfma_f32_32x32x16_bf16 v[16:31], v[172:175], a[212:215], v[16:31]\n\t" \
  "ds_read_b64_tr_b16 v[156:157], v179 offset:6144\n\t" \
  "ds_read_b64_tr_b16 v[158:159], v179 offset:7168\n\t" \
  "s_waitcnt lgkmcnt(8)\n\t" \
  "v_mfma_f32_32x32x16_bf16 v[64:79], v[128:131], a[144:147], 0\n\t" \
  "ds_read_b64_tr_b16 v[160:161], v178 offset:8192\n\t" \
  "ds_read_b64_tr_b16 v[162:163], v178 offset:9216\n\t" \
  "v_mfma_f32_32x32x16_bf16 v[80:95], v[132:135], a[144:147], 0\n\t" \
  "ds_read_b64_tr_b16 v[164:165], v179 offset:8192\n\t" \
  "ds_read_b64_tr_b16 v[166:167], v179 offset:9216\n\t" \
  "s_add_u32 s42, s42, 4096\n\t" \
  "s_cmp_eq_u32 s42, 36864\n\t" \
  "s_cselect_b32 s42, 0, s42\n\t" \
  "v_mfma_f32_32x32x16_bf16 v[64:79], v[136:139], a[148:151], v[64:79]\n\t" \
  "ds_read_b64_tr_b16 v[168:169], v178 offset:10240\n\t" \
  "ds_read_b64_tr_b16 v[170:171], v178 offset:11264\n\t" \
  "buffer_load_dwordx4 v194, %1, s40 offen offset:1024 lds\n\t" \
  "v_mfma_f32_32x32x16_bf16 v[80:95], v[140:143], a[148:151], v[80:95]\n\t" \
  "ds_read_b64_tr_b16 v[172:173], v179 offset:10240\n\t" \
  "ds_read_b64_tr_b16 v[174:175], v179 offset:11264\n\t" \
  "s_waitcnt lgkmcnt(8)\n\t" \
  "v_mfma_f32_32x32x16_bf16 v[64:79], v[144:147], a[152:155], v[64:79]\n\t" \
  "ds_read_b64_tr_b16 v[128:129], v178 offset:12288\n\t" \
  "ds_read_b64_tr_b16 v[130:131], v178 offset:13312\n\t" \
  "s_add_u32 s40, %11, s41\n\t" \
  "s_add_u32 m0, %16, 16384\n\t" \
  "v_cvt_pk_bf16_f32 v208, v0, v1\n\t" \
  "v_cvt_pk_bf16_f32 v209, v2, v3\n\t" \
  "v_mfma_f32_32x32x16_bf16 v[80:95], v[148:151], a[152:155], v[80:95]\n\t" \
  "ds_read_b64_tr_b16 v[132:133], v179 offset:12288\n\t" \
  "ds_read_b64_tr_b16 v[134:135], v179 offset:13312\n\t" \
  "buffer_load_dwordx4 v193, %1, s40 offen lds\n\t" \
  "v_cvt_pk_bf16_f32 v210, v4, v5\n\t" \
  "v_mfma_f32_32x32x16_bf16 v[64:79], v[152:155], a[156:159], v[64:79]\n\t" \
  "ds_read_b64_tr_b16 v[136:137], v178 offset:14336\n\t" \
  "ds_read_b64_tr_b16 v[138:139], v178 offset:15360\n\t" \
  "v_mfma_f32_32x32x16_bf16 v[80:95], v[156:159], a[156:159], v[80:95]\n\t" \
  "ds_read_b64_tr_b16 v[140:141], v179 offset:14336\n\t" \
  "ds_read_b64_tr_b16 v[142:143], v179 offset:15360\n\t" \
  "v_cvt_pk_bf16_f32 v211, v6, v7\n\t" \
  "s_waitcnt lgkmcnt(8)\n\t" \
  "v_mfma_f32_32x32x16_bf16 v[64:79], v[160:163], a[160:163], v[64:79]\n\t" \
  "ds_read_b64_tr_b16 v[144:145], v178 offset:16384\n\t" \
  "ds_read_b64_tr_b16 v[146:147], v178 offset:17408\n\t" \
  "buffer_store_dwordx4 v[208:211], v195, %3, s42 offen\n\t" \
  "v_mfma_f32_32x32x16_bf16 v[80:95], v[164:167], a[160:163], v[80:95]\n\t" \
  "ds_read_b64_tr_b16 v[148:149], v179 offset:16384\n\t" \
  "ds_read_b64_tr_b16 v[150:151], v179 offset:17408\n\t" \
  "buffer_load_dwordx4 v194, %1, s40 offen offset:1024 lds\n\t" \
  "v_cvt_pk_bf16_f32 v212, v8, v9\n\t" \
  "v_mfma_f32_32x32x16_bf16 v[64:79], v[168:171], a[164:167], v[64:79]\n\t" \
  "ds_read_b64_tr_b16 v[152:153], v178 offset:18432\n\t" \
  "ds_read_b64_tr_b16 v[154:155], v178 offset:19456\n\t" \
  "v_cvt_pk_bf16_f32 v213, v10, v11\n\t" \
  "v_mfma_f32_32x32x16_bf16 v[80:95], v[172:175], a[164:167], v[80:95]\n\t" \
  "ds_read_b64_tr_b16 v[156:157], v179 offset:18432\n\t" \
  "ds_read_b64_tr_b16 v[158:159], v179 offset:19456\n\t" \
  "v_cvt_pk_bf16_f32 v214, v12, v13\n\t" \
  "s_waitcnt lgkmcnt(8)\n\t" \
  "v_mfma_f32_32x32x16_bf16 v[64:79], v[128:131], a[168:171], v[64:79]\n\t" \
  "ds_read_b64_tr_b16 v[160:161], v178 offset:20480\n\t" \
  "ds_read_b64_tr_b16 v[162:163], v178 offset:21504\n\t" \
  "s_add_u32 s40, %12, s41\n\t" \
  "s_add_u32 m0, %16, 24576\n\t" \
  "v_mfma_f32_32x32x16_bf16 v[80:95], v[132:135], a[168:171], v[80:95]\n\t" \
  "ds_read_b64_tr_b16 v[164:165], v179 offset:20480\n\t" \
  "ds_read_b64_tr_b16 v[166:167], v179 offset:21504\n\t" \
  "buffer_load_dwordx4 v193, %1, s40 offen lds\n\t" \
  "v_cvt_pk_bf16_f32 v215, v14, v15\n\t" \
  "v_mfma_f32_32x32x16_bf16 v[64:79], v[136:139], a[172:175], v[64:79]\n\t" \
  "ds_read_b64_tr_b16 v[168:169], v178 offset:22528\n\t" \
  "ds_read_b64_tr_b16 v[170:171], v178 offset:23552\n\t" \
  "buffer_load_dwordx4 v194, %1, s40 offen offset:1024 lds\n\t" \
  "buffer_store_dwordx4 v[212:215], v195, %3, s42 offen offset:1024\n\t" \
  "v_mfma_f32_32x32x16_bf16 v[80:95], v[140:143], a[172:175], v[80:95]\n\t" \
  "ds_read_b64_tr_b16 v[172:173], v179 offset:22528\n\t" \
  "ds_read_b64_tr_b16 v[174:175], v179 offset:23552\n\t" \
  "v_cvt_pk_bf16_f32 v216, v16, v17\n\t" \
  "s_waitcnt lgkmcnt(8)\n\t" \
  "v_mfma_f32_32x32x16_bf16 v[64:79], v[144:147], a[176:179], v[64:79]\n\t" \
  "ds_read_b64_tr_b16 v[128:129], v178 offset:24576\n\t" \
  "ds_read_b64_tr_b16 v[130:131], v178 offset:25600\n\t" \
  "v_cvt_pk_bf16_f32 v217, v18, v19\n\t" \
  "v_mfma_f32_32x32x16_bf16 v[80:95], v[148:151], a[176:179], v[80:95]\n\t" \
  "ds_read_b64_tr_b16 v[132:133], v179 offset:24576\n\t" \
  "ds_read_b64_tr_b16 v[134:135], v179 offset:25600\n\t" \
  "s_add_u32 s40, %13, s41\n\t" \
  "s_add_u32 m0, %25, 0\n\t" \
  "v_cvt_pk_bf16_f32 v218, v20, v21\n\t" \
  "v_mfma_f32_32x32x16_bf16 v[64:79], v[152:155], a[180:183], v[64:79]\n\t" \
  "ds_read_b64_tr_b16 v[136:137], v178 offset:26624\n\t" \
  "ds_read_b64_tr_b16 v[138:139], v178 offset:27648\n\t" \
  "buffer_load_dwordx4 v193, %1, s40 offen lds\n\t" \
  "v_mfma_f32_32x32x16_bf16 v[80:95], v[156:159], a[180:183], v[80:95]\n\t" \
  "ds_read_b64_tr_b16 v[140:141], v179 offset:26624\n\t" \
  "ds_read_b64_tr_b16 v[142:143], v179 offset:27648\n\t" \
  "v_cvt_pk_bf16_f32 v219, v22, v23\n\t" \
  "s_waitcnt lgkmcnt(8)\n\t" \
  "v_mfma_f32_32x32x16_bf16 v[64:79], v[160:163], a[184:187], v[64:79]\n\t" \
  "ds_read_b64_tr_b16 v[144:145], v178 offset:28672\n\t" \
  "ds_read_b64_tr_b16 v[146:147], v178 offset:29696\n\t" \
  "buffer_store_dwordx4 v[216:219], v195, %3, s42 offen offset:2048\n\t" \
  "v_mfma_f32_32x32x16_bf16 v[80:95], v[164:167], a[184:187], v[80:95]\n\t" \
  "ds_read_b64_tr_b16 v[148:149], v179 offset:28672\n\t" \
  "ds_read_b64_tr_b16 v[150:151], v179 offset:29696\n\t" \
  "v_cvt_pk_bf16_f32 v220, v24, v25\n\t" \
  "v_mfma_f32_32x32x16_bf16 v[64:79], v[168:171], a[188:191], v[64:79]\n\t" \
  "ds_read_b64_tr_b16 v[152:153], v178 offset:30720\n\t" \
  "ds_read_b64_tr_b16 v[154:155], v178 offset:31744\n\t" \
  "v_cvt_pk_bf16_f32 v221, v26, v27\n\t" \
  "v_mfma_f32_32x32x16_bf16 v[80:95], v[172:175], a[188:191], v[80:95]\n\t" \
  "ds_read_b64_tr_b16 v[156:157], v179 offset:30720\n\t" \
  "ds_read_b64_tr_b16 v[158:159], v179 offset:31744\n\t" \
  "v_cvt_pk_bf16_f32 v222, v28, v29\n\t" \
  "s_waitcnt lgkmcnt(8)\n\t" \
  "v_mfma_f32_32x32x16_bf16 v[64:79], v[128:131], a[192:195], v[64:79]\n\t" \
  "ds_read_b64_tr_b16 v[160:161], v178 offset:32768\n\t" \
  "ds_read_b64_tr_b16 v[162:163], v178 offset:33792\n\t" \
  "ds_read_b64_tr_b16 v[164:165], v179 offset:32768\n\t" \
  "ds_read_b64_tr_b16 v[166:167], v179 offset:33792\n\t" \
  "v_mfma_f32_32x32x16_bf16 v[80:95], v[132:135], a[192:195], v[80:95]\n\t" \
  "ds_read_b64_tr_b16 v[168:169], v178 offset:34816\n\t" \
  "ds_read_b64_tr_b16 v[170:171], v178 offset:35840\n\t" \
  "ds_read_b64_tr_b16 v[172:173], v179 offset:34816\n\t" \
  "ds_read_b64_tr_b16 v[174:175], v179 offset:35840\n\t" \
  "v_cvt_pk_bf16_f32 v223, v30, v31\n\t" \
  "v_mfma_f32_32x32x16_bf16 v[64:79], v[136:139], a[196:199], v[64:79]\n\t" \
  "buffer_store_dwordx4 v[220:223], v195, %3, s42 offen offset:3072\n\t" \
  "v_mfma_f32_32x32x16_bf16 v[80:95], v[140:143], a[196:199], v[80:95]\n\t" \
  "s_waitcnt lgkmcnt(8)\n\t" \
  "v_mfma_f32_32x32x16_bf16 v[64:79], v[144:147], a[200:203], v[64:79]\n\t" \
  "s_waitcnt vmcnt(15) lgkmcnt(0)\n\ts_barrier\n\t" \
  "ds_read_b64_tr_b16 v[128:129], v180 offset:0\n\t" \
  "ds_read_b64_tr_b16 v[130:131], v180 offset:1024\n\t" \
  "v_mfma_f32_32x32x16_bf16 v[80:95], v[148:151], a[200:203], v[80:95]\n\t" \
  "ds_read_b64_tr_b16 v[132:133], v181 offset:0\n\t" \
  "ds_read_b64_tr_b16 v[134:135], v181 offset:1024\n\t" \
  "s_add_u32 s41, s41, 128\n\t" \
  "s_cmp_eq_u32 s41, 1152\n\t" \
  "s_cselect_b32 s41, 0, s41\n\t" \
  "s_add_u32 s40, %9, s41\n\t" \
  "s_add_u32 m0, %16, 49152\n\t" \
  "s_nop 0\n\t" \
  "buffer_load_dwordx4 v193, %1, s40 offen lds\n\t" \
  "v_mfma_f32_32x32x16_bf16 v[64:79], v[152:155], a[204:207], v[64:79]\n\t" \
  "ds_read_b64_tr_b16 v[136:137], v180 offset:2048\n\t" \
  "ds_read_b64_tr_b16 v[138:139], v180 offset:3072\n\t" \
  "buffer_load_dwordx4 v194, %1, s40 offen offset:1024 lds\n\t" \
  "v_mfma_f32_32x32x16_bf16 v[80:95], v[156:159], a[204:207], v[80:95]\n\t" \
  "ds_read_b64_tr_b16 v[140:141], v181 offset:2048\n\t" \
  "ds_read_b64_tr_b16 v[142:143], v181 offset:3072\n\t" \
  "s_waitcnt lgkmcnt(8)\n\t" \
  "v_mfma_f32_32x32x16_bf16 v[64:79], v[160:163], a[208:211], v[64:79]\n\t" \
  "ds_read_b64_tr_b16 v[144:145], v180 offset:4096\n\t" \
  "ds_read_b64_tr_b16 v[146:147], v180 offset:5120\n\t" \
  "v_mfma_f32_32x32x16_bf16 v[80:95], v[164:167], a[208:211], v[80:95]\n\t" \
  "ds_read_b64_tr_b16 v[148:149], v181 offset:4096\n\t" \
  "ds_read_b64_tr_b16 v[150:151], v181 offset:5120\n\t" \
  "s_add_u32 s40, %10, s41\n\t" \
  "s_add_u32 m0, %16, 57344\n\t" \
  "v_mfma_f32_32x32x16_bf16 v[64:79], v[168:171], a[212:215], v[64:79]\n\t" \
  "ds_read_b64_tr_b16 v[152:153], v180 offset:6144\n\t" \
  "ds_read_b64_tr_b16 v[154:155], v180 offset:7168\n\t" \
  "buffer_load_dwordx4 v193, %1, s40 offen lds\n\t" \
  "v_mfma_f32_32x32x16_bf16 v[80:95], v[172:175], a[212:215], v[80:95]\n\t" \
  "ds_read_b64_tr_b16 v[156:157], v181 offset:6144\n\t" \
  "ds_read_b64_tr_b16 v[158:159], v181 offset:7168\n\t" \
  "s_waitcnt lgkmcnt(8)\n\t" \
  "v_mfma_f32_32x32x16_bf16 v[0:15], v[128:131], a[144:147], 0\n\t" \
  "ds_read_b64_tr_b16 v[160:161], v180 offset:8192\n\t" \
  "ds_read_b64_tr_b16 v[162:163], v180 offset:9216\n\t" \
  "v_mfma_f32_32x32x16_bf16 v[16:31], v[132:135], a[144:147], 0\n\t" \
  "ds_read_b64_tr_b16 v[164:165], v181 offset:8192\n\t" \
  "ds_read_b64_tr_b16 v[166:167], v181 offset:9216\n\t" \
  "s_add_u32 s42, s42, 4096\n\t" \
  "s_cmp_eq_u32 s42, 36864\n\t" \
  "s_cselect_b32 s42, 0, s42\n\t" \
  "v_mfma_f32_32x32x16_bf16 v[0:15], v[136:139], a[148:151], v[0:15]\n\t" \
  "ds_read_b64_tr_b16 v[168:169], v180 offset:10240\n\t" \
  "ds_read_b64_tr_b16 v[170:171], v180 offset:11264\n\t" \
  "buffer_load_dwordx4 v194, %1, s40 offen offset:1024 lds\n\t" \
  "v_mfma_f32_32x32x16_bf16 v[16:31], v[140:143], a[148:151], v[16:31]\n\t" \
  "ds_read_b64_tr_b16 v[172:173], v181 offset:10240\n\t" \
  "ds_read_b64_tr_b16 v[174:175], v181 offset:11264\n\t" \
  "s_waitcnt lgkmcnt(8)\n\t" \
  "v_mfma_f32_32x32x16_bf16 v[0:15], v[144:147], a[152:155], v[0:15]\n\t" \
  "ds_read_b64_tr_b16 v[128:129], v180 offset:12288\n\t" \
  "ds_read_b64_tr_b16 v[130:131], v180 offset:13312\n\t" \
  "s_add_u32 s40, %11, s41\n\t" \
  "s_add_u32 m0, %16, 65536\n\t" \
  "v_cvt_pk_bf16_f32 v208, v64, v65\n\t" \
  "v_cvt_pk_bf16_f32 v209, v66, v67\n\t" \
  "v_mfma_f32_32x32x16_bf16 v[16:31], v[148:151], a[152:155], v[16:31]\n\t" \
  "ds_read_b64_tr_b16 v[132:133], v181 offset:12288\n\t" \
  "ds_read_b64_tr_b16 v[134:135], v181 offset:13312\n\t" \
  "buffer_load_dwordx4 v193, %1, s40 offen lds\n\t" \
  "v_cvt_pk_bf16_f32 v210, v68, v69\n\t" \
  "v_mfma_f32_32x32x16_bf16 v[0:15], v[152:155], a[156:159], v[0:15]\n\t" \
  "ds_read_b64_tr_b16 v[136:137], v180 offset:14336\n\t" \
  "ds_read_b64_tr_b16 v[138:139], v180 offset:15360\n\t" \
  "v_mfma_f32_32x32x16_bf16 v[16:31], v[156:159], a[156:159], v[16:31]\n\t" \
  "ds_read_b64_tr_b16 v[140:141], v181 offset:14336\n\t" \
  "ds_read_b64_tr_b16 v[142:143], v181 offset:15360\n\t" \
  "v_cvt_pk_bf16_f32 v211, v70, v71\n\t" \
  "s_waitcnt lgkmcnt(8)\n\t" \
  "v_mfma_f32_32x32x16_bf16 v[0:15], v[160:163], a[160:163], v[0:15]\n\t" \
  "ds_read_b64_tr_b16 v[144:145], v180 offset:16384\n\t" \
  "ds_read_b64_tr_b16 v[146:147], v180 offset:17408\n\t" \
  "buffer_store_dwordx4 v[208:211], v195, %3, s42 offen\n\t" \
  "v_mfma_f32_32x32x16_bf16 v[16:31], v[164:167], a[160:163], v[16:31]\n\t" \
  "ds_read_b64_tr_b16 v[148:149], v181 offset:16384\n\t" \
  "ds_read_b64_tr_b16 v[150:151], v181 offset:17408\n\t" \
  "buffer_load_dwordx4 v194, %1, s40 offen offset:1024 lds\n\t" \
  "v_cvt_pk_bf16_f32 v212, v72, v73\n\t" \
  "v_mfma_f32_32x32x16_bf16 v[0:15], v[168:171], a[164:167], v[0:15]\n\t" \
  "ds_read_b64_tr_b16 v[152:153], v180 offset:18432\n\t" \
  "ds_read_b64_tr_b16 v[154:155], v180 offset:19456\n\t" \
  "v_cvt_pk_bf16_f32 v213, v74, v75\n\t" \
  "v_mfma_f32_32x32x16_bf16 v[16:31], v[172:175], a[164:167], v[16:31]\n\t" \
  "ds_read_b64_tr_b16 v[156:157], v181 offset:18432\n\t" \
  "ds_read_b64_tr_b16 v[158:159], v181 offset:19456\n\t" \
  "v_cvt_pk_bf16_f32 v214, v76, v77\n\t" \
  "s_waitcnt lgkmcnt(8)\n\t" \
  "v_mfma_f32_32x32x16_bf16 v[0:15], v[128:131], a[168:171], v[0:15]\n\t" \
  "ds_read_b64_tr_b16 v[160:161], v180 offset:20480\n\t" \
  "ds_read_b64_tr_b16 v[162:163], v180 offset:21504\n\t" \
  "s_add_u32 s40, %12, s41\n\t" \
  "s_add_u32 m0, %16, 73728\n\t" \
  "v_mfma_f32_32x32x16_bf16 v[16:31], v[132:135], a[168:171], v[16:31]\n\t" \
  "ds_read_b64_tr_b16 v[164:165], v181 offset:20480\n\t" \
  "ds_read_b64_tr_b16 v[166:167], v181 offset:21504\n\t" \
  "buffer_load_dwordx4 v193, %1, s40 offen lds\n\t" \
  "v_cvt_pk_bf16_f32 v215, v78, v79\n\t" \
  "v_mfma_f32_32x32x16_bf16 v[0:15], v[136:139], a[172:175], v[0:15]\n\t" \
  "ds_read_b64_tr_b16 v[168:169], v180 offset:22528\n\t" \
  "ds_read_b64_tr_b16 v[170:171], v180 offset:23552\n\t" \
  "buffer_load_dwordx4 v194, %1, s40 offen offset:1024 lds\n\t" \
  "buffer_store_dwordx4 v[212:215], v195, %3, s42 offen offset:1024\n\t" \
  "v_mfma_f32_32x32x16_bf16 v[16:31], v[140:143], a[172:175], v[16:31]\n\t" \
  "ds_read_b64_tr_b16 v[172:173], v181 offset:22528\n\t" \
  "ds_read_b64_tr_b16 v[174:175], v181 offset:23552\n\t" \
  "v_cvt_pk_bf16_f32 v216, v80, v81\n\t" \
  "s_waitcnt lgkmcnt(8)\n\t" \
  "v_mfma_f32_32x32x16_bf16 v[0:15], v[144:147], a[176:179], v[0:15]\n\t" \
  "ds_read_b64_tr_b16 v[128:129], v180 offset:24576\n\t" \
  "ds_read_b64_tr_b16 v[130:131], v180 offset:25600\n\t" \
  "v_cvt_pk_bf16_f32 v217, v82, v83\n\t" \
  "v_mfma_f32_32x32x16_bf16 v[16:31], v[148:151], a[176:179], v[16:31]\n\t" \
  "ds_read_b64_tr_b16 v[132:133], v181 offset:24576\n\t" \
  "ds_read_b64_tr_b16 v[134:135], v181 offset:25600\n\t" \
  "s_add_u32 s40, %13, s41\n\t" \
  "s_add_u32 m0, %25, 49152\n\t" \
  "v_cvt_pk_bf16_f32 v218, v84, v85\n\t" \
  "v_mfma_f32_32x32x16_bf16 v[0:15], v[152:155], a[180:183], v[0:15]\n\t" \
  "ds_read_b64_tr_b16 v[136:137], v180 offset:26624\n\t" \
  "ds_read_b64_tr_b16 v[138:139], v180 offset:27648\n\t" \
  "buffer_load_dwordx4 v193, %1, s40 offen lds\n\t" \
  "v_mfma_f32_32x32x16_bf16 v[16:31], v[156:159], a[180:183], v[16:31]\n\t" \
  "ds_read_b64_tr_b16 v[140:141], v181 offset:26624\n\t" \
  "ds_read_b64_tr_b16 v[142:143], v181 offset:27648\n\t" \
  "v_cvt_pk_bf16_f32 v219, v86, v87\n\t" \
  "s_waitcnt lgkmcnt(8)\n\t" \
  "v_mfma_f32_32x32x16_bf16 v[0:15], v[160:163], a[184:187], v[0:15]\n\t" \
  "ds_read_b64_tr_b16 v[144:145], v180 offset:28672\n\t" \
  "ds_read_b64_tr_b16 v[146:147], v180 offset:29696\n\t" \
  "buffer_store_dwordx4 v[216:219], v195, %3, s42 offen offset:2048\n\t" \
  "v_mfma_f32_32x32x16_bf16 v[16:31], v[164:167], a[184:187], v[16:31]\n\t" \
  "ds_read_b64_tr_b16 v[148:149], v181 offset:28672\n\t" \
  "ds_read_b64_tr_b16 v[150:151], v181 offset:29696\n\t" \
  "v_cvt_pk_bf16_f32 v220, v88, v89\n\t" \
  "v_mfma_f32_32x32x16_bf16 v[0:15], v[168:171], a[188:191], v[0:15]\n\t" \
  "ds_read_b64_tr_b16 v[152:153], v180 offset:30720\n\t" \
  "ds_read_b64_tr_b16 v[154:155], v180 offset:31744\n\t" \
  "v_cvt_pk_bf16_f32 v221, v90, v91\n\t" \
  "v_mfma_f32_32x32x16_bf16 v[16:31], v[172:175], a[188:191], v[16:31]\n\t" \
  "ds_read_b64_tr_b16 v[156:157], v181 offset:30720\n\t" \
  "ds_read_b64_tr_b16 v[158:159], v181 offset:31744\n\t" \
  "v_cvt_pk_bf16_f32 v222, v92, v93\n\t" \
  "s_waitcnt lgkmcnt(8)\n\t" \
  "v_mfma_f32_32x32x16_bf16 v[0:15], v[128:131], a[192:195], v[0:15]\n\t" \
  "ds_read_b64_tr_b16 v[160:161], v180 offset:32768\n\t" \
  "ds_read_b64_tr_b16 v[162:163], v180 offset:33792\n\t" \
  "ds_read_b64_tr_b16 v[164:165], v181 offset:32768\n\t" \
  "ds_read_b64_tr_b16 v[166:167], v181 offset:33792\n\t" \
  "v_mfma_f32_32x32x16_bf16 v[16:31], v[132:135], a[192:195], v[16:31]\n\t" \
  "ds_read_b64_tr_b16 v[168:169], v180 offset:34816\n\t" \
  "ds_read_b64_tr_b16 v[170:171], v180 offset:35840\n\t" \
  "ds_read_b64_tr_b16 v[172:173], v181 offset:34816\n\t" \
  "ds_read_b64_tr_b16 v[174:175], v181 offset:35840\n\t" \
  "v_cvt_pk_bf16_f32 v223, v94, v95\n\t" \
  "v_mfma_f32_32x32x16_bf16 v[0:15], v[136:139], a[196:199], v[0:15]\n\t" \
  "buffer_store_dwordx4 v[220:223], v195, %3, s42 offen offset:3072\n\t" \
  "v_mfma_f32_32x32x16_bf16 v[16:31], v[140:143], a[196:199], v[16:31]\n\t" \
  "s_waitcnt lgkmcnt(8)\n\t" \
  "v_mfma_f32_32x32x16_bf16 v[0:15], v[144:147], a[200:203], v[0:15]\n\t" \
  "s_waitcnt vmcnt(15) lgkmcnt(0)\n\ts_barrier\n\t" \
  "ds_read_b64_tr_b16 v[128:129], v176 offset:0\n\t" \
  "ds_read_b64_tr_b16 v[130:131], v176 offset:1024\n\t" \
  "v_mfma_f32_32x32x16_bf16 v[16:31], v[148:151], a[200:203], v[16:31]\n\t" \
  "ds_read_b64_tr_b16 v[132:133], v177 offset:0\n\t" \
  "ds_read_b64_tr_b16 v[134:135], v177 offset:1024\n\t" \
  "s_add_u32 s41, s41, 128\n\t" \
  "s_cmp_eq_u32 s41, 1152\n\t" \
  "s_cselect_b32 s41, 0, s41\n\t" \
  "s_add_u32 s40, %9, s41\n\t" \
  "s_add_u32 m0, %16, 98304\n\t" \
  "s_nop 0\n\t" \
  "buffer_load_dwordx4 v193, %1, s40 offen lds\n\t" \
  "v_mfma_f32_32x32x16_bf16 v[0:15], v[152:155], a[204:207], v[0:15]\n\t" \
  "ds_read_b64_tr_b16 v[136:137], v176 offset:2048\n\t" \
  "ds_read_b64_tr_b16 v[138:139], v176 offset:3072\n\t" \
  "buffer_load_dwordx4 v194, %1, s40 offen offset:1024 lds\n\t" \
  "v_mfma_f32_32x32x16_bf16 v[16:31], v[156:159], a[204:207], v[16:31]\n\t" \
  "ds_read_b64_tr_b16 v[140:141], v177 offset:2048\n\t" \
  "ds_read_b64_tr_b16 v[142:143], v177 offset:3072\n\t" \
  "s_waitcnt lgkmcnt(8)\n\t" \
  "v_mfma_f32_32x32x16_bf16 v[0:15], v[160:163], a[208:211], v[0:15]\n\t" \
  "ds_read_b64_tr_b16 v[144:145], v176 offset:4096\n\t" \
  "ds_read_b64_tr_b16 v[146:147], v176 offset:5120\n\t" \
  "v_mfma_f32_32x32x16_bf16 v[16:31], v[164:167], a[208:211], v[16:31]\n\t" \
  "ds_read_b64_tr_b16 v[148:149], v177 offset:4096\n\t" \
  "ds_read_b64_tr_b16 v[150:151], v177 offset:5120\n\t" \
  "s_add_u32 s40, %10, s41\n\t" \
  "s_add_u32 m0, %16, 106496\n\t" \
  "v_mfma_f32_32x32x16_bf16 v[0:15], v[168:171], a[212:215], v[0:15]\n\t" \
  "ds_read_b64_tr_b16 v[152:153], v176 offset:6144\n\t" \
  "ds_read_b64_tr_b16 v[154:155], v176 offset:7168\n\t" \
  "buffer_load_dwordx4 v193, %1, s40 offen lds\n\t" \
  "v_mfma_f32_32x32x16_bf16 v[16:31], v[172:175], a[212:215], v[16:31]\n\t" \
  "ds_read_b64_tr_b16 v[156:157], v177 offset:6144\n\t" \
  "ds_read_b64_tr_b16 v[158:159], v177 offset:7168\n\t" \
  "s_waitcnt lgkmcnt(8)\n\t" \
  "v_mfma_f32_32x32x16_bf16 v[64:79], v[128:131], a[144:147], 0\n\t" \
  "ds_read_b64_tr_b16 v[160:161], v176 offset:8192\n\t" \
  "ds_read_b64_tr_b16 v[162:163], v176 offset:9216\n\t" \
  "v_mfma_f32_32x32x16_bf16 v[80:95], v[132:135], a[144:147], 0\n\t" \
  "ds_read_b64_tr_b16 v[164:165], v177 offset:8192\n\t" \
  "ds_read_b64_tr_b16 v[166:167], v177 offset:9216\n\t" \
  "s_add_u32 s42, s42, 4096\n\t" \
  "s_cmp_eq_u32 s42, 36864\n\t" \
  "s_cselect_b32 s42, 0, s42\n\t" \
  "v_mfma_f32_32x32x16_bf16 v[64:79], v[136:139], a[148:151], v[64:79]\n\t" \
  "ds_read_b64_tr_b16 v[168:169], v176 offset:10240\n\t" \
  "ds_read_b64_tr_b16 v[170:171], v176 offset:11264\n\t" \
  "buffer_load_dwordx4 v194, %1, s40 offen offset:1024 lds\n\t" \
  "v_mfma_f32_32x32x16_bf16 v[80:95], v[140:143], a[148:151], v[80:95]\n\t" \
  "ds_read_b64_tr_b16 v[172:173], v177 offset:10240\n\t" \
  "ds_read_b64_tr_b16 v[174:175], v177 offset:11264\n\t" \
  "s_waitcnt lgkmcnt(8)\n\t" \
  "v_mfma_f32_32x32x16_bf16 v[64:79], v[144:147], a[152:155], v[64:79]\n\t" \
  "ds_read_b64_tr_b16 v[128:129], v176 offset:12288\n\t" \
  "ds_read_b64_tr_b16 v[130:131], v176 offset:13312\n\t" \
  "s_add_u32 s40, %11, s41\n\t" \
  "s_add_u32 m0, %16, 114688\n\t" \
  "v_cvt_pk_bf16_f32 v208, v0, v1\n\t" \
  "v_cvt_pk_bf16_f32 v209, v2, v3\n\t" \
  "v_mfma_f32_32x32x16_bf16 v[80:95], v[148:151], a[152:155], v[80:95]\n\t" \
  "ds_read_b64_tr_b16 v[132:133], v177 offset:12288\n\t" \
  "ds_read_b64_tr_b16 v[134:135], v177 offset:13312\n\t" \
  "buffer_load_dwordx4 v193, %1, s40 offen lds\n\t" \
  "v_cvt_pk_bf16_f32 v210, v4, v5\n\t" \
  "v_mfma_f32_32x32x16_bf16 v[64:79], v[152:155], a[156:159], v[64:79]\n\t" \
  "ds_read_b64_tr_b16 v[136:137], v176 offset:14336\n\t" \
  "ds_read_b64_tr_b16 v[138:139], v176 offset:15360\n\t" \
  "v_mfma_f32_32x32x16_bf16 v[80:95], v[156:159], a[156:159], v[80:95]\n\t" \
  "ds_read_b64_tr_b16 v[140:141], v177 offset:14336\n\t" \
  "ds_read_b64_tr_b16 v[142:143], v177 offset:15360\n\t" \
  "v_cvt_pk_bf16_f32 v211, v6, v7\n\t" \
  "s_waitcnt lgkmcnt(8)\n\t" \
  "v_mfma_f32_32x32x16_bf16 v[64:79], v[160:163], a[160:163], v[64:79]\n\t" \
  "ds_read_b64_tr_b16 v[144:145], v176 offset:16384\n\t" \
  "ds_read_b64_tr_b16 v[146:147], v176 offset:17408\n\t" \
  "buffer_store_dwordx4 v[208:211], v195, %3, s42 offen\n\t" \
  "v_mfma_f32_32x32x16_bf16 v[80:95], v[164:167], a[160:163], v[80:95]\n\t" \
  "ds_read_b64_tr_b16 v[148:149], v177 offset:16384\n\t" \
  "ds_read_b64_tr_b16 v[150:151], v177 offset:17408\n\t" \
  "buffer_load_dwordx4 v194, %1, s40 offen offset:1024 lds\n\t" \
  "v_cvt_pk_bf16_f32 v212, v8, v9\n\t" \
  "v_mfma_f32_32x32x16_bf16 v[64:79], v[168:171], a[164:167], v[64:79]\n\t" \
  "ds_read_b64_tr_b16 v[152:153], v176 offset:18432\n\t" \
  "ds_read_b64_tr_b16 v[154:155], v176 offset:19456\n\t" \
  "v_cvt_pk_bf16_f32 v213, v10, v11\n\t" \
  "v_mfma_f32_32x32x16_bf16 v[80:95], v[172:175], a[164:167], v[80:95]\n\t" \
  "ds_read_b64_tr_b16 v[156:157], v177 offset:18432\n\t" \
  "ds_read_b64_tr_b16 v[158:159], v177 offset:19456\n\t" \
  "v_cvt_pk_bf16_f32 v214, v12, v13\n\t" \
  "s_waitcnt lgkmcnt(8)\n\t" \
  "v_mfma_f32_32x32x16_bf16 v[64:79], v[128:131], a[168:171], v[64:79]\n\t" \
  "ds_read_b64_tr_b16 v[160:161], v176 offset:20480\n\t" \
  "ds_read_b64_tr_b16 v[162:163], v176 offset:21504\n\t" \
  "s_add_u32 s40, %12, s41\n\t" \
  "s_add_u32 m0, %16, 122880\n\t" \
  "v_mfma_f32_32x32x16_bf16 v[80:95], v[132:135], a[168:171], v[80:95]\n\t" \
  "ds_read_b64_tr_b16 v[164:165], v177 offset:20480\n\t" \
  "ds_read_b64_tr_b16 v[166:167], v177 offset:21504\n\t" \
  "buffer_load_dwordx4 v193, %1, s40 offen lds\n\t" \
  "v_cvt_pk_bf16_f32 v215, v14, v15\n\t" \
  "v_mfma_f32_32x32x16_bf16 v[64:79], v[136:139], a[172:175], v[64:79]\n\t" \
  "ds_read_b64_tr_b16 v[168:169], v176 offset:22528\n\t" \
  "ds_read_b64_tr_b16 v[170:171], v176 offset:23552\n\t" \
  "buffer_load_dwordx4 v194, %1, s40 offen offset:1024 lds\n\t" \
  "buffer_store_dwordx4 v[212:215], v195, %3, s42 offen offset:1024\n\t" \
  "v_mfma_f32_32x32x16_bf16 v[80:95], v[140:143], a[172:175], v[80:95]\n\t" \
  "ds_read_b64_tr_b16 v[172:173], v177 offset:22528\n\t" \
  "ds_read_b64_tr_b16 v[174:175], v177 offset:23552\n\t" \
  "v_cvt_pk_bf16_f32 v216, v16, v17\n\t" \
  "s_waitcnt lgkmcnt(8)\n\t" \
  "v_mfma_f32_32x32x16_bf16 v[64:79], v[144:147], a[176:179], v[64:79]\n\t" \
  "ds_read_b64_tr_b16 v[128:129], v176 offset:24576\n\t" \
  "ds_read_b64_tr_b16 v[130:131], v176 offset:25600\n\t" \
  "v_cvt_pk_bf16_f32 v217, v18, v19\n\t" \
  "v_mfma_f32_32x32x16_bf16 v[80:95], v[148:151], a[176:179], v[80:95]\n\t" \
  "ds_read_b64_tr_b16 v[132:133], v177 offset:24576\n\t" \
  "ds_read_b64_tr_b16 v[134:135], v177 offset:25600\n\t" \
  "s_add_u32 s40, %13, s41\n\t" \
  "s_add_u32 m0, %25, 98304\n\t" \
  "v_cvt_pk_bf16_f32 v218, v20, v21\n\t" \
  "v_mfma_f32_32x32x16_bf16 v[64:79], v[152:155], a[180:183], v[64:79]\n\t" \
  "ds_read_b64_tr_b16 v[136:137], v176 offset:26624\n\t" \
  "ds_read_b64_tr_b16 v[138:139], v176 offset:27648\n\t" \
  "buffer_load_dwordx4 v193, %1, s40 offen lds\n\t" \
  "v_mfma_f32_32x32x16_bf16 v[80:95], v[156:159], a[180:183], v[80:95]\n\t" \
  "ds_read_b64_tr_b16 v[140:141], v177 offset:26624\n\t" \
  "ds_read_b64_tr_b16 v[142:143], v177 offset:27648\n\t" \
  "v_cvt_pk_bf16_f32 v219, v22, v23\n\t" \
  "s_waitcnt lgkmcnt(8)\n\t" \
  "v_mfma_f32_32x32x16_bf16 v[64:79], v[160:163], a[184:187], v[64:79]\n\t" \
  "ds_read_b64_tr_b16 v[144:145], v176 offset:28672\n\t" \
  "ds_read_b64_tr_b16 v[146:147], v176 offset:29696\n\t" \
  "buffer_store_dwordx4 v[216:219], v195, %3, s42 offen offset:2048\n\t" \
  "v_mfma_f32_32x32x16_bf16 v[80:95], v[164:167], a[184:187], v[80:95]\n\t" \
  "ds_read_b64_tr_b16 v[148:149], v177 offset:28672\n\t" \
  "ds_read_b64_tr_b16 v[150:151], v177 offset:29696\n\t" \
  "v_cvt_pk_bf16_f32 v220, v24, v25\n\t" \
  "v_mfma_f32_32x32x16_bf16 v[64:79], v[168:171], a[188:191], v[64:79]\n\t" \
  "ds_read_b64_tr_b16 v[152:153], v176 offset:30720\n\t" \
  "ds_read_b64_tr_b16 v[154:155], v176 offset:31744\n\t" \
  "v_cvt_pk_bf16_f32 v221, v26, v27\n\t" \
  "v_mfma_f32_32x32x16_bf16 v[80:95], v[172:175], a[188:191], v[80:95]\n\t" \
  "ds_read_b64_tr_b16 v[156:157], v177 offset:30720\n\t" \
  "ds_read_b64_tr_b16 v[158:159], v177 offset:31744\n\t" \
  "v_cvt_pk_bf16_f32 v222, v28, v29\n\t" \
  "s_waitcnt lgkmcnt(8)\n\t" \
  "v_mfma_f32_32x32x16_bf16 v[64:79], v[128:131], a[192:195], v[64:79]\n\t" \
  "ds_read_b64_tr_b16 v[160:161], v176 offset:32768\n\t" \
  "ds_read_b64_tr_b16 v[162:163], v176 offset:33792\n\t" \
  "ds_read_b64_tr_b16 v[164:165], v177 offset:32768\n\t" \
  "ds_read_b64_tr_b16 v[166:167], v177 offset:33792\n\t" \
  "v_mfma_f32_32x32x16_bf16 v[80:95], v[132:135], a[192:195], v[80:95]\n\t" \
  "ds_read_b64_tr_b16 v[168:169], v176 offset:34816\n\t" \
  "ds_read_b64_tr_b16 v[170:171], v176 offset:35840\n\t" \
  "ds_read_b64_tr_b16 v[172:173], v177 offset:34816\n\t" \
  "ds_read_b64_tr_b16 v[174:175], v177 offset:35840\n\t" \
  "v_cvt_pk_bf16_f32 v223, v30, v31\n\t" \
  "v_mfma_f32_32x32x16_bf16 v[64:79], v[136:139], a[196:199], v[64:79]\n\t" \
  "buffer_store_dwordx4 v[220:223], v195, %3, s42 offen offset:3072\n\t" \
  "v_mfma_f32_32x32x16_bf16 v[80:95], v[140:143], a[196:199], v[80:95]\n\t" \
  "s_waitcnt lgkmcnt(8)\n\t" \
  "v_mfma_f32_32x32x16_bf16 v[64:79], v[144:147], a[200:203], v[64:79]\n\t" \
  "s_waitcnt vmcnt(15) lgkmcnt(0)\n\ts_barrier\n\t" \
  "ds_read_b64_tr_b16 v[128:129], v178 offset:0\n\t" \
  "ds_read_b64_tr_b16 v[130:131], v178 offset:1024\n\t" \
  "v_mfma_f32_32x32x16_bf16 v[80:95], v[148:151], a[200:203], v[80:95]\n\t" \
  "ds_read_b64_tr_b16 v[132:133], v179 offset:0\n\t" \
  "ds_read_b64_tr_b16 v[134:135], v179 offset:1024\n\t" \
  "v_mfma_f32_32x32x16_bf16 v[64:79], v[152:155], a[204:207], v[64:79]\n\t" \
  "ds_read_b64_tr_b16 v[136:137], v178 offset:2048\n\t" \
  "ds_read_b64_tr_b16 v[138:139], v178 offset:3072\n\t" \
  "v_mfma_f32_32x32x16_bf16 v[80:95], v[156:159], a[204:207], v[80:95]\n\t" \
  "ds_read_b64_tr_b16 v[140:141], v179 offset:2048\n\t" \
  "ds_read_b64_tr_b16 v[142:143], v179 offset:3072\n\t" \
  "s_waitcnt lgkmcnt(8)\n\t" \
  "v_mfma_f32_32x32x16_bf16 v[64:79], v[160:163], a[208:211], v[64:79]\n\t" \
  "ds_read_b64_tr_b16 v[144:145], v178 offset:4096\n\t" \
  "ds_read_b64_tr_b16 v[146:147], v178 offset:5120\n\t" \
  "v_mfma_f32_32x32x16_bf16 v[80:95], v[164:167], a[208:211], v[80:95]\n\t" \
  "ds_read_b64_tr_b16 v[148:149], v179 offset:4096\n\t" \
  "ds_read_b64_tr_b16 v[150:151], v179 offset:5120\n\t" \
  "v_mfma_f32_32x32x16_bf16 v[64:79], v[168:171], a[212:215], v[64:79]\n\t" \
  "ds_read_b64_tr_b16 v[152:153], v178 offset:6144\n\t" \
  "ds_read_b64_tr_b16 v[154:155], v178 offset:7168\n\t" \
  "v_mfma_f32_32x32x16_bf16 v[80:95], v[172:175], a[212:215], v[80:95]\n\t" \
  "ds_read_b64_tr_b16 v[156:157], v179 offset:6144\n\t" \
  "ds_read_b64_tr_b16 v[158:159], v179 offset:7168\n\t" \
  "s_waitcnt lgkmcnt(8)\n\t" \
  "v_mfma_f32_32x32x16_bf16 v[0:15], v[128:131], a[144:147], 0\n\t" \
  "ds_read_b64_tr_b16 v[160:161], v178 offset:8192\n\t" \
  "ds_read_b64_tr_b16 v[162:163], v178 offset:9216\n\t" \
  "v_mfma_f32_32x32x16_bf16 v[16:31], v[132:135], a[144:147], 0\n\t" \
  "ds_read_b64_tr_b16 v[164:165], v179 offset:8192\n\t" \
  "ds_read_b64_tr_b16 v[166:167], v179 offset:9216\n\t" \
  "s_add_u32 s42, s42, 4096\n\t" \
  "s_cmp_eq_u32 s42, 36864\n\t" \
  "s_cselect_b32 s42, 0, s42\n\t" \
  "v_mfma_f32_32x32x16_bf16 v[0:15], v[136:139], a[148:151], v[0:15]\n\t" \
  "ds_read_b64_tr_b16 v[168:169], v178 offset:10240\n\t" \
  "ds_read_b64_tr_b16 v[170:171], v178 offset:11264\n\t" \
  "v_mfma_f32_32x32x16_bf16 v[16:31], v[140:143], a[148:151], v[16:31]\n\t" \
  "ds_read_b64_tr_b16 v[172:173], v179 offset:10240\n\t" \
  "ds_read_b64_tr_b16 v[174:175], v179 offset:11264\n\t" \
  "s_waitcnt lgkmcnt(8)\n\t" \
  "v_mfma_f32_32x32x16_bf16 v[0:15], v[144:147], a[152:155], v[0:15]\n\t" \
  "ds_read_b64_tr_b16 v[128:129], v178 offset:12288\n\t" \
  "ds_read_b64_tr_b16 v[130:131], v178 offset:13312\n\t" \
  "v_cvt_pk_bf16_f32 v208, v64, v65\n\t" \
  "v_cvt_pk_bf16_f32 v209, v66, v67\n\t" \
  "v_mfma_f32_32x32x16_bf16 v[16:31], v[148:151], a[152:155], v[16:31]\n\t" \
  "ds_read_b64_tr_b16 v[132:133], v179 offset:12288\n\t" \
  "ds_read_b64_tr_b16 v[134:135], v179 offset:13312\n\t" \
  "v_cvt_pk_bf16_f32 v210, v68, v69\n\t" \
  "v_mfma_f32_32x32x16_bf16 v[0:15], v[152:155], a[156:159], v[0:15]\n\t" \
  "ds_read_b64_tr_b16 v[136:137], v178 offset:14336\n\t" \
  "ds_read_b64_tr_b16 v[138:139], v178 offset:15360\n\t" \
  "v_mfma_f32_32x32x16_bf16 v[16:31], v[156:159], a[156:159], v[16:31]\n\t" \
  "ds_read_b64_tr_b16 v[140:141], v179 offset:14336\n\t" \
  "ds_read_b64_tr_b16 v[142:143], v179 offset:15360\n\t" \
  "v_cvt_pk_bf16_f32 v211, v70, v71\n\t" \
  "s_waitcnt lgkmcnt(8)\n\t" \
  "v_mfma_f32_32x32x16_bf16 v[0:15], v[160:163], a[160:163], v[0:15]\n\t" \
  "ds_read_b64_tr_b16 v[144:145], v178 offset:16384\n\t" \
  "ds_read_b64_tr_b16 v[146:147], v178 offset:17408\n\t" \
  "buffer_store_dwordx4 v[208:211], v195, %3, s42 offen\n\t" \
  "v_mfma_f32_32x32x16_bf16 v[16:31], v[164:167], a[160:163], v[16:31]\n\t" \
  "ds_read_b64_tr_b16 v[148:149], v179 offset:16384\n\t" \
  "ds_read_b64_tr_b16 v[150:151], v179 offset:17408\n\t" \
  "v_cvt_pk_bf16_f32 v212, v72, v73\n\t" \
  "v_mfma_f32_32x32x16_bf16 v[0:15], v[168:171], a[164:167], v[0:15]\n\t" \
  "ds_read_b64_tr_b16 v[152:153], v178 offset:18432\n\t" \
  "ds_read_b64_tr_b16 v[154:155], v178 offset:19456\n\t" \
  "v_cvt_pk_bf16_f32 v213, v74, v75\n\t" \
  "v_mfma_f32_32x32x16_bf16 v[16:31], v[172:175], a[164:167], v[16:31]\n\t" \
  "ds_read_b64_tr_b16 v[156:157], v179 offset:18432\n\t" \
  "ds_read_b64_tr_b16 v[158:159], v179 offset:19456\n\t" \
  "v_cvt_pk_bf16_f32 v214, v76, v77\n\t" \
  "s_waitcnt lgkmcnt(8)\n\t" \
  "v_mfma_f32_32x32x16_bf16 v[0:15], v[128:131], a[168:171], v[0:15]\n\t" \
  "ds_read_b64_tr_b16 v[160:161], v178 offset:20480\n\t" \
  "ds_read_b64_tr_b16 v[162:163], v178 offset:21504\n\t" \
  "v_mfma_f32_32x32x16_bf16 v[16:31], v[132:135], a[168:171], v[16:31]\n\t" \
  "ds_read_b64_tr_b16 v[164:165], v179 offset:20480\n\t" \
  "ds_read_b64_tr_b16 v[166:167], v179 offset:21504\n\t" \
  "v_cvt_pk_bf16_f32 v215, v78, v79\n\t" \
  "v_mfma_f32_32x32x16_bf16 v[0:15], v[136:139], a[172:175], v[0:15]\n\t" \
  "ds_read_b64_tr_b16 v[168:169], v178 offset:22528\n\t" \
  "ds_read_b64_tr_b16 v[170:171], v178 offset:23552\n\t" \
  "buffer_store_dwordx4 v[212:215], v195, %3, s42 offen offset:1024\n\t" \
  "v_mfma_f32_32x32x16_bf16 v[16:31], v[140:143], a[172:175], v[16:31]\n\t" \
  "ds_read_b64_tr_b16 v[172:173], v179 offset:22528\n\t" \
  "ds_read_b64_tr_b16 v[174:175], v179 offset:23552\n\t" \
  "v_cvt_pk_bf16_f32 v216, v80, v81\n\t" \
  "s_waitcnt lgkmcnt(8)\n\t" \
  "v_mfma_f32_32x32x16_bf16 v[0:15], v[144:147], a[176:179], v[0:15]\n\t" \
  "ds_read_b64_tr_b16 v[128:129], v178 offset:24576\n\t" \
  "ds_read_b64_tr_b16 v[130:131], v178 offset:25600\n\t" \
  "v_cvt_pk_bf16_f32 v217, v82, v83\n\t" \
  "v_mfma_f32_32x32x16_bf16 v[16:31], v[148:151], a[176:179], v[16:31]\n\t" \
  "ds_read_b64_tr_b16 v[132:133], v179 offset:24576\n\t" \
  "ds_read_b64_tr_b16 v[134:135], v179 offset:25600\n\t" \
  "v_cvt_pk_bf16_f32 v218, v84, v85\n\t" \
  "v_mfma_f32_32x32x16_bf16 v[0:15], v[152:155], a[180:183], v[0:15]\n\t" \
  "ds_read_b64_tr_b16 v[136:137], v178 offset:26624\n\t" \
  "ds_read_b64_tr_b16 v[138:139], v178 offset:27648\n\t" \
  "v_mfma_f32_32x32x16_bf16 v[16:31], v[156:159], a[180:183], v[16:31]\n\t" \
  "ds_read_b64_tr_b16 v[140:141], v179 offset:26624\n\t" \
  "ds_read_b64_tr_b16 v[142:143], v179 offset:27648\n\t" \
  "v_cvt_pk_bf16_f32 v219, v86, v87\n\t" \
  "s_waitcnt lgkmcnt(8)\n\t" \
  "v_mfma_f32_32x32x16_bf16 v[0:15], v[160:163], a[184:187], v[0:15]\n\t" \
  "ds_read_b64_tr_b16 v[144:145], v178 offset:28672\n\t" \
  "ds_read_b64_tr_b16 v[146:147], v178 offset:29696\n\t" \
  "buffer_store_dwordx4 v[216:219], v195, %3, s42 offen offset:2048\n\t" \
  "v_mfma_f32_32x32x16_bf16 v[16:31], v[164:167], a[184:187], v[16:31]\n\t" \
  "ds_read_b64_tr_b16 v[148:149], v179 offset:28672\n\t" \
  "ds_read_b64_tr_b16 v[150:151], v179 offset:29696\n\t" \
  "v_cvt_pk_bf16_f32 v220, v88, v89\n\t" \
  "v_mfma_f32_32x32x16_bf16 v[0:15], v[168:171], a[188:191], v[0:15]\n\t" \
  "ds_read_b64_tr_b16 v[152:153], v178 offset:30720\n\t" \
  "ds_read_b64_tr_b16 v[154:155], v178 offset:31744\n\t" \
  "v_cvt_pk_bf16_f32 v221, v90, v91\n\t" \
  "v_mfma_f32_32x32x16_bf16 v[16:31], v[172:175], a[188:191], v[16:31]\n\t" \
  "ds_read_b64_tr_b16 v[156:157], v179 offset:30720\n\t" \
  "ds_read_b64_tr_b16 v[158:159], v179 offset:31744\n\t" \
  "v_cvt_pk_bf16_f32 v222, v92, v93\n\t" \
  "s_waitcnt lgkmcnt(8)\n\t" \
  "v_mfma_f32_32x32x16_bf16 v[0:15], v[128:131], a[192:195], v[0:15]\n\t" \
  "ds_read_b64_tr_b16 v[160:161], v178 offset:32768\n\t" \
  "ds_read_b64_tr_b16 v[162:163], v178 offset:33792\n\t" \
  "ds_read_b64_tr_b16 v[164:165], v179 offset:32768\n\t" \
  "ds_read_b64_tr_b16 v[166:167], v179 offset:33792\n\t" \
  "v_mfma_f32_32x32x16_bf16 v[16:31], v[132:135], a[192:195], v[16:31]\n\t" \
  "ds_read_b64_tr_b16 v[168:169], v178 offset:34816\n\t" \
  "ds_read_b64_tr_b16 v[170:171], v178 offset:35840\n\t" \
  "ds_read_b64_tr_b16 v[172:173], v179 offset:34816\n\t" \
  "ds_read_b64_tr_b16 v[174:175], v179 offset:35840\n\t" \
  "v_cvt_pk_bf16_f32 v223, v94, v95\n\t" \
  "v_mfma_f32_32x32x16_bf16 v[0:15], v[136:139], a[196:199], v[0:15]\n\t" \
  "buffer_store_dwordx4 v[220:223], v195, %3, s42 offen offset:3072\n\t" \
  "v_mfma_f32_32x32x16_bf16 v[16:31], v[140:143], a[196:199], v[16:31]\n\t" \
  "s_waitcnt lgkmcnt(8)\n\t" \
  "v_mfma_f32_32x32x16_bf16 v[0:15], v[144:147], a[200:203], v[0:15]\n\t" \
  "s_waitcnt vmcnt(6) lgkmcnt(0)\n\ts_barrier\n\t" \
  "ds_read_b64_tr_b16 v[128:129], v180 offset:0\n\t" \
  "ds_read_b64_tr_b16 v[130:131], v180 offset:1024\n\t" \
  "v_mfma_f32_32x32x16_bf16 v[16:31], v[148:151], a[200:203], v[16:31]\n\t" \
  "ds_read_b64_tr_b16 v[132:133], v181 offset:0\n\t" \
  "ds_read_b64_tr_b16 v[134:135], v181 offset:1024\n\t" \
  "v_mfma_f32_32x32x16_bf16 v[0:15], v[152:155], a[204:207], v[0:15]\n\t" \
  "ds_read_b64_tr_b16 v[136:137], v180 offset:2048\n\t" \
  "ds_read_b64_tr_b16 v[138:139], v180 offset:3072\n\t" \
  "v_mfma_f32_32x32x16_bf16 v[16:31], v[156:159], a[204:207], v[16:31]\n\t" \
  "ds_read_b64_tr_b16 v[140:141], v181 offset:2048\n\t" \
  "ds_read_b64_tr_b16 v[142:143], v181 offset:3072\n\t" \
  "s_waitcnt lgkmcnt(8)\n\t" \
  "v_mfma_f32_32x32x16_bf16 v[0:15], v[160:163], a[208:211], v[0:15]\n\t" \
  "ds_read_b64_tr_b16 v[144:145], v180 offset:4096\n\t" \
  "ds_read_b64_tr_b16 v[146:147], v180 offset:5120\n\t" \
  "v_mfma_f32_32x32x16_bf16 v[16:31], v[164:167], a[208:211], v[16:31]\n\t" \
  "ds_read_b64_tr_b16 v[148:149], v181 offset:4096\n\t" \
  "ds_read_b64_tr_b16 v[150:151], v181 offset:5120\n\t" \
  "v_mfma_f32_32x32x16_bf16 v[0:15], v[168:171], a[212:215], v[0:15]\n\t" \
  "ds_read_b64_tr_b16 v[152:153], v180 offset:6144\n\t" \
  "ds_read_b64_tr_b16 v[154:155], v180 offset:7168\n\t" \
  "v_mfma_f32_32x32x16_bf16 v[16:31], v[172:175], a[212:215], v[16:31]\n\t" \
  "ds_read_b64_tr_b16 v[156:157], v181 offset:6144\n\t" \
  "ds_read_b64_tr_b16 v[158:159], v181 offset:7168\n\t" \
  "s_waitcnt lgkmcnt(8)\n\t" \
  "v_mfma_f32_32x32x16_bf16 v[64:79], v[128:131], a[144:147], 0\n\t" \
  "ds_read_b64_tr_b16 v[160:161], v180 offset:8192\n\t" \
  "ds_read_b64_tr_b16 v[162:163], v180 offset:9216\n\t" \
  "v_mfma_f32_32x32x16_bf16 v[80:95], v[132:135], a[144:147], 0\n\t" \
  "ds_read_b64_tr_b16 v[164:165], v181 offset:8192\n\t" \
  "ds_read_b64_tr_b16 v[166:167], v181 offset:9216\n\t" \
  "s_add_u32 s42, s42, 4096\n\t" \
  "s_cmp_eq_u32 s42, 36864\n\t" \
  "s_cselect_b32 s42, 0, s42\n\t" \
  "v_mfma_f32_32x32x16_bf16 v[64:79], v[136:139], a[148:151], v[64:79]\n\t" \
  "ds_read_b64_tr_b16 v[168:169], v180 offset:10240\n\t" \
  "ds_read_b64_tr_b16 v[170:171], v180 offset:11264\n\t" \
  "v_mfma_f32_32x32x16_bf16 v[80:95], v[140:143], a[148:151], v[80:95]\n\t" \
  "ds_read_b64_tr_b16 v[172:173], v181 offset:10240\n\t" \
  "ds_read_b64_tr_b16 v[174:175], v181 offset:11264\n\t" \
  "s_waitcnt lgkmcnt(8)\n\t" \
  "v_mfma_f32_32x32x16_bf16 v[64:79], v[144:147], a[152:155], v[64:79]\n\t" \
  "ds_read_b64_tr_b16 v[128:129], v180 offset:12288\n\t" \
  "ds_read_b64_tr_b16 v[130:131], v180 offset:13312\n\t" \
  "v_cvt_pk_bf16_f32 v208, v0, v1\n\t" \
  "v_cvt_pk_bf16_f32 v209, v2, v3\n\t" \
  "v_mfma_f32_32x32x16_bf16 v[80:95], v[148:151], a[152:155], v[80:95]\n\t" \
  "ds_read_b64_tr_b16 v[132:133], v181 offset:12288\n\t" \
  "ds_read_b64_tr_b16 v[134:135], v181 offset:13312\n\t" \
  "v_cvt_pk_bf16_f32 v210, v4, v5\n\t" \
  "v_mfma_f32_32x32x16_bf16 v[64:79], v[152:155], a[156:159], v[64:79]\n\t" \
  "ds_read_b64_tr_b16 v[136:137], v180 offset:14336\n\t" \
  "ds_read_b64_tr_b16 v[138:139], v180 offset:15360\n\t" \
  "v_mfma_f32_32x32x16_bf16 v[80:95], v[156:159], a[156:159], v[80:95]\n\t" \
  "ds_read_b64_tr_b16 v[140:141], v181 offset:14336\n\t" \
  "ds_read_b64_tr_b16 v[142:143], v181 offset:15360\n\t" \
  "v_cvt_pk_bf16_f32 v211, v6, v7\n\t" \
  "s_waitcnt lgkmcnt(8)\n\t" \
  "v_mfma_f32_32x32x16_bf16 v[64:79], v[160:163], a[160:163], v[64:79]\n\t" \
  "ds_read_b64_tr_b16 v[144:145], v180 offset:16384\n\t" \
  "ds_read_b64_tr_b16 v[146:147], v180 offset:17408\n\t" \
  "buffer_store_dwordx4 v[208:211], v195, %3, s42 offen\n\t" \
  "v_mfma_f32_32x32x16_bf16 v[80:95], v[164:167], a[160:163], v[80:95]\n\t" \
  "ds_read_b64_tr_b16 v[148:149], v181 offset:16384\n\t" \
  "ds_read_b64_tr_b16 v[150:151], v181 offset:17408\n\t" \
  "v_cvt_pk_bf16_f32 v212, v8, v9\n\t" \
  "v_mfma_f32_32x32x16_bf16 v[64:79], v[168:171], a[164:167], v[64:79]\n\t" \
  "ds_read_b64_tr_b16 v[152:153], v180 offset:18432\n\t" \
  "ds_read_b64_tr_b16 v[154:155], v180 offset:19456\n\t" \
  "v_cvt_pk_bf16_f32 v213, v10, v11\n\t" \
  "v_mfma_f32_32x32x16_bf16 v[80:95], v[172:175], a[164:167], v[80:95]\n\t" \
  "ds_read_b64_tr_b16 v[156:157], v181 offset:18432\n\t" \
  "ds_read_b64_tr_b16 v[158:159], v181 offset:19456\n\t" \
  "v_cvt_pk_bf16_f32 v214, v12, v13\n\t" \
  "s_waitcnt lgkmcnt(8)\n\t" \
  "v_mfma_f32_32x32x16_bf16 v[64:79], v[128:131], a[168:171], v[64:79]\n\t" \
  "ds_read_b64_tr_b16 v[160:161], v180 offset:20480\n\t" \
  "ds_read_b64_tr_b16 v[162:163], v180 offset:21504\n\t" \
  "v_mfma_f32_32x32x16_bf16 v[80:95], v[132:135], a[168:171], v[80:95]\n\t" \
  "ds_read_b64_tr_b16 v[164:165], v181 offset:20480\n\t" \
  "ds_read_b64_tr_b16 v[166:167], v181 offset:21504\n\t" \
  "v_cvt_pk_bf16_f32 v215, v14, v15\n\t" \
  "v_mfma_f32_32x32x16_bf16 v[64:79], v[136:139], a[172:175], v[64:79]\n\t" \
  "ds_read_b64_tr_b16 v[168:169], v180 offset:22528\n\t" \
  "ds_read_b64_tr_b16 v[170:171], v180 offset:23552\n\t" \
  "buffer_store_dwordx4 v[212:215], v195, %3, s42 offen offset:1024\n\t" \
  "v_mfma_f32_32x32x16_bf16 v[80:95], v[140:143], a[172:175], v[80:95]\n\t" \
  "ds_read_b64_tr_b16 v[172:173], v181 offset:22528\n\t" \
  "ds_read_b64_tr_b16 v[174:175], v181 offset:23552\n\t" \
  "v_cvt_pk_bf16_f32 v216, v16, v17\n\t" \
  "s_waitcnt lgkmcnt(8)\n\t" \
  "v_mfma_f32_32x32x16_bf16 v[64:79], v[144:147], a[176:179], v[64:79]\n\t" \
  "ds_read_b64_tr_b16 v[128:129], v180 offset:24576\n\t" \
  "ds_read_b64_tr_b16 v[130:131], v180 offset:25600\n\t" \
  "v_cvt_pk_bf16_f32 v217, v18, v19\n\t" \
  "v_mfma_f32_32x32x16_bf16 v[80:95], v[148:151], a[176:179], v[80:95]\n\t" \
  "ds_read_b64_tr_b16 v[132:133], v181 offset:24576\n\t" \
  "ds_read_b64_tr_b16 v[134:135], v181 offset:25600\n\t" \
  "v_cvt_pk_bf16_f32 v218, v20, v21\n\t" \
  "v_mfma_f32_32x32x16_bf16 v[64:79], v[152:155], a[180:183], v[64:79]\n\t" \
  "ds_read_b64_tr_b16 v[136:137], v180 offset:26624\n\t" \
  "ds_read_b64_tr_b16 v[138:139], v180 offset:27648\n\t" \
  "v_mfma_f32_32x32x16_bf16 v[80:95], v[156:159], a[180:183], v[80:95]\n\t" \
  "ds_read_b64_tr_b16 v[140:141], v181 offset:26624\n\t" \
  "ds_read_b64_tr_b16 v[142:143], v181 offset:27648\n\t" \
  "v_cvt_pk_bf16_f32 v219, v22, v23\n\t" \
  "s_waitcnt lgkmcnt(8)\n\t" \
  "v_mfma_f32_32x32x16_bf16 v[64:79], v[160:163], a[184:187], v[64:79]\n\t" \
  "ds_read_b64_tr_b16 v[144:145], v180 offset:28672\n\t" \
  "ds_read_b64_tr_b16 v[146:147], v180 offset:29696\n\t" \
  "buffer_store_dwordx4 v[216:219], v195, %3, s42 offen offset:2048\n\t" \
  "v_mfma_f32_32x32x16_bf16 v[80:95], v[164:167], a[184:187], v[80:95]\n\t" \
  "ds_read_b64_tr_b16 v[148:149], v181 offset:28672\n\t" \
  "ds_read_b64_tr_b16 v[150:151], v181 offset:29696\n\t" \
  "v_cvt_pk_bf16_f32 v220, v24, v25\n\t" \
  "v_mfma_f32_32x32x16_bf16 v[64:79], v[168:171], a[188:191], v[64:79]\n\t" \
  "ds_read_b64_tr_b16 v[152:153], v180 offset:30720\n\t" \
  "ds_read_b64_tr_b16 v[154:155], v180 offset:31744\n\t" \
  "v_cvt_pk_bf16_f32 v221, v26, v27\n\t" \
  "v_mfma_f32_32x32x16_bf16 v[80:95], v[172:175], a[188:191], v[80:95]\n\t" \
  "ds_read_b64_tr_b16 v[156:157], v181 offset:30720\n\t" \
  "ds_read_b64_tr_b16 v[158:159], v181 offset:31744\n\t" \
  "v_cvt_pk_bf16_f32 v222, v28, v29\n\t" \
  "s_waitcnt lgkmcnt(8)\n\t" \
  "v_mfma_f32_32x32x16_bf16 v[64:79], v[128:131], a[192:195], v[64:79]\n\t" \
  "ds_read_b64_tr_b16 v[160:161], v180 offset:32768\n\t" \
  "ds_read_b64_tr_b16 v[162:163], v180 offset:33792\n\t" \
  "v_mfma_f32_32x32x16_bf16 v[80:95], v[132:135], a[192:195], v[80:95]\n\t" \
  "ds_read_b64_tr_b16 v[164:165], v181 offset:32768\n\t" \
  "ds_read_b64_tr_b16 v[166:167], v181 offset:33792\n\t" \
  "v_cvt_pk_bf16_f32 v223, v30, v31\n\t" \
  "v_mfma_f32_32x32x16_bf16 v[64:79], v[136:139], a[196:199], v[64:79]\n\t" \
  "ds_read_b64_tr_b16 v[168:169], v180 offset:34816\n\t" \
  "ds_read_b64_tr_b16 v[170:171], v180 offset:35840\n\t" \
  "buffer_store_dwordx4 v[220:223], v195, %3, s42 offen offset:3072\n\t" \
  "v_mfma_f32_32x32x16_bf16 v[80:95], v[140:143], a[196:199], v[80:95]\n\t" \
  "ds_read_b64_tr_b16 v[172:173], v181 offset:34816\n\t" \
  "ds_read_b64_tr_b16 v[174:175], v181 offset:35840\n\t" \
  "s_waitcnt lgkmcnt(8)\n\t" \
  "v_mfma_f32_32x32x16_bf16 v[64:79], v[144:147], a[200:203], v[64:79]\n\t" \
  "v_mfma_f32_32x32x16_bf16 v[80:95], v[148:151], a[200:203], v[80:95]\n\t" \
  "v_mfma_f32_32x32x16_bf16 v[64:79], v[152:155], a[204:207], v[64:79]\n\t" \
  "v_mfma_f32_32x32x16_bf16 v[80:95], v[156:159], a[204:207], v[80:95]\n\t" \
  "s_waitcnt lgkmcnt(0)\n\t" \
  "v_mfma_f32_32x32x16_bf16 v[64:79], v[160:163], a[208:211], v[64:79]\n\t" \
  "v_mfma_f32_32x32x16_bf16 v[80:95], v[164:167], a[208:211], v[80:95]\n\t" \
  "v_mfma_f32_32x32x16_bf16 v[64:79], v[168:171], a[212:215], v[64:79]\n\t" \
  "v_mfma_f32_32x32x16_bf16 v[80:95], v[172:175], a[212:215], v[80:95]\n\t" \
  "s_add_u32 s42, s42, 4096\n\t" \
  "s_cmp_eq_u32 s42, 36864\n\t" \
  "s_cselect_b32 s42, 0, s42\n\t" \
  "s_nop 7\n\t" \
  "v_cvt_pk_bf16_f32 v208, v64, v65\n\t" \
  "v_cvt_pk_bf16_f32 v209, v66, v67\n\t" \
  "v_cvt_pk_bf16_f32 v210, v68, v69\n\t" \
  "v_cvt_pk_bf16_f32 v211, v70, v71\n\t" \
  "buffer_store_dwordx4 v[208:211], v195, %3, s42 offen\n\t" \
  "v_cvt_pk_bf16_f32 v212, v72, v73\n\t" \
  "v_cvt_pk_bf16_f32 v213, v74, v75\n\t" \
  "v_cvt_pk_bf16_f32 v214, v76, v77\n\t" \
  "v_cvt_pk_bf16_f32 v215, v78, v79\n\t" \
  "buffer_store_dwordx4 v[212:215], v195, %3, s42 offen offset:1024\n\t" \
  "v_cvt_pk_bf16_f32 v216, v80, v81\n\t" \
  "v_cvt_pk_bf16_f32 v217, v82, v83\n\t" \
  "v_cvt_pk_bf16_f32 v218, v84, v85\n\t" \
  "v_cvt_pk_bf16_f32 v219, v86, v87\n\t" \
  "buffer_store_dwordx4 v[216:219], v195, %3, s42 offen offset:2048\n\t" \
  "v_cvt_pk_bf16_f32 v220, v88, v89\n\t" \
  "v_cvt_pk_bf16_f32 v221, v90, v91\n\t" \
  "v_cvt_pk_bf16_f32 v222, v92, v93\n\t" \
  "v_cvt_pk_bf16_f32 v223, v94, v95\n\t" \
  "buffer_store_dwordx4 v[220:223], v195, %3, s42 offen offset:3072\n\t" \
  ""
#define K3_ASM_CLOBBERS "v0", "v1", "v2", "v3", "v4", "v5", "v6", "v7", "v8", "v9", "v10", "v11", "v12", "v13", "v14", "v15", "v16", "v17", "v18", "v19", "v20", "v21", "v22", "v23", "v24", "v25", "v26", "v27", "v28", "v29", "v30", "v31", "v32", "v33", "v34", "v35", "v36", "v37", "v38", "v39", "v40", "v41", "v42", "v43", "v44", "v45", "v46", "v47", "v48", "v49", "v50", "v51", "v52", "v53", "v54", "v55", "v56", "v57", "v58", "v59", "v60", "v61", "v62", "v63", "v64", "v65", "v66", "v67", "v68", "v69", "v70", "v71", "v72", "v73", "v74", "v75", "v76", "v77", "v78", "v79", "v80", "v81", "v82", "v83", "v84", "v85", "v86", "v87", "v88", "v89", "v90", "v91", "v92", "v93", "v94", "v95", "v96", "v97", "v98", "v99", "v100", "v101", "v102", "v103", "v104", "v105", "v106", "v107", "v108", "v109", "v110", "v111", "v112", "v113", "v114", "v115", "v116", "v117", "v118", "v119", "v120", "v121", "v122", "v123", "v124", "v125", "v126", "v127", "v128", "v129", "v130", "v131", "v132", "v133", "v134", "v135", "v136", "v137", "v138", "v139", "v140", "v141", "v142", "v143", "v144", "v145", "v146", "v147", "v148", "v149", "v150", "v151", "v152", "v153", "v154", "v155", "v156", "v157", "v158", "v159", "v160", "v161", "v162", "v163", "v164", "v165", "v166", "v167", "v168", "v169", "v170", "v171", "v172", "v173", "v174", "v175", "v176", "v177", "v178", "v179", "v180", "v181", "v182", "v183", "v184", "v185", "v186", "v187", "v188", "v189", "v190", "v191", "v192", "v193", "v194", "v195", "v196", "v197", "v198", "v199", "v200", "v201", "v202", "v203", "v204", "v205", "v206", "v207", "v208", "v209", "v210", "v211", "v212", "v213", "v214", "v215", "v216", "v217", "v218", "v219", "v220", "v221", "v222", "v223", "a0", "a1", "a2", "a3", "a4", "a5", "a6", "a7", "a8", "a9", "a10", "a11", "a12", "a13", "a14", "a15", "a16", "a17", "a18", "a19", "a20", "a21", "a22", "a23", "a24", "a25", "a26", "a27", "a28", "a29", "a30", "a31", "a32", "a33", "a34", "a35", "a36", "a37", "a38", "a39", "a40", "a41", "a42", "a43", "a44", "a45", "a46", "a47", "a48", "a49", "a50", "a51", "a52", "a53", "a54", "a55", "a56", "a57", "a58", "a59", "a60", "a61", "a62", "a63", "a64", "a65", "a66", "a67", "a68", "a69", "a70", "a71", "a72", "a73", "a74", "a75", "a76", "a77", "a78", "a79", "a80", "a81", "a82", "a83", "a84", "a85", "a86", "a87", "a88", "a89", "a90", "a91", "a92", "a93", "a94", "a95", "a96", "a97", "a98", "a99", "a100", "a101", "a102", "a103", "a104", "a105", "a106", "a107", "a108", "a109", "a110", "a111", "a112", "a113", "a114", "a115", "a116", "a117", "a118", "a119", "a120", "a121", "a122", "a123", "a124", "a125", "a126", "a127", "a128", "a129", "a130", "a131", "a132", "a133", "a134", "a135", "a136", "a137", "a138", "a139", "a140", "a141", "a142", "a143", "a144", "a145", "a146", "a147", "a148", "a149", "a150", "a151", "a152", "a153", "a154", "a155", "a156", "a157", "a158", "a159", "a160", "a161", "a162", "a163", "a164", "a165", "a166", "a167", "a168", "a169", "a170", "a171", "a172", "a173", "a174", "a175", "a176", "a177", "a178", "a179", "a180", "a181", "a182", "a183", "a184", "a185", "a186", "a187", "a188", "a189", "a190", "a191", "a192", "a193", "a194", "a195", "a196", "a197", "a198", "a199", "a200", "a201", "a202", "a203", "a204", "a205", "a206", "a207", "a208", "a209", "a210", "a211", "a212", "a213", "a214", "a215", "a216", "a217", "a218", "a219", "a220", "a221", "a222", "a223", "a224", "a225", "a226", "a227", "a228", "a229", "a230", "a231", "a232", "a233", "a234", "a235", "a236", "a237", "a238", "a239", "a240", "a241", "a242", "a243", "a244", "a245", "a246", "a247", "a248", "a249", "a250", "a251", "a252", "a253", "a254", "a255", "s40", "s41", "s42", "s43", "s44", "s45", "s46", "s47", "vcc", "scc", "memory"
#define P1_ASM_BODY \
  "v_add_u32 v192, 40960, %15\n\t" \
  "v_add_u32 v193, 40960, %16\n\t" \
  "v_add_u32 v194, 40960, %17\n\t" \
  "v_add_u32 v195, 40960, %18\n\t" \
  "v_add_u32 v196, 40960, %19\n\t" \
  "v_add_u32 v197, 81920, %15\n\t" \
  "v_add_u32 v198, 81920, %16\n\t" \
  "v_add_u32 v199, 81920, %17\n\t" \
  "v_add_u32 v200, 81920, %18\n\t" \
  "v_add_u32 v201, 81920, %19\n\t" \
  "v_xor_b32 v202, 0, %20\n\t" \
  "v_xor_b32 v203, 32, %20\n\t" \
  "v_xor_b32 v204, 64, %20\n\t" \
  "v_xor_b32 v205, 96, %20\n\t" \
  "v_add_u32 v206, 40960, v202\n\t" \
  "v_add_u32 v207, 40960, v203\n\t" \
  "v_add_u32 v208, 40960, v204\n\t" \
  "v_add_u32 v209, 40960, v205\n\t" \
  "v_add_u32 v210, 81920, v202\n\t" \
  "v_add_u32 v211, 81920, v203\n\t" \
  "v_add_u32 v212, 81920, v204\n\t" \
  "v_add_u32 v213, 81920, v205\n\t" \
  "v_mov_b32 v214, %21\n\t" \
  "v_add_u32 v215, 4294966272, %22\n\t" \
  "v_add_u32 v216, 16384, %21\n\t" \
  "v_add_u32 v217, 15360, %22\n\t" \
  "v_lshrrev_b32 v224, 4, %29\n\t" \
  "v_and_b32 v224, 1, v224\n\t" \
  "v_and_b32 v225, 15, %29\n\t" \
  "v_cmp_eq_u32 vcc, v224, v225\n\t" \
  "v_mov_b32 v224, 0x3f803f80\n\t" \
  "s_nop 1\n\t" \
  "v_cndmask_b32 v188, 0, v224, vcc\n\t" \
  "v_mov_b32 v189, v188\n\t" \
  "v_mov_b32 v190, v188\n\t" \
  "v_mov_b32 v191, v188\n\t" \
  "v_mov_b32 v160, 0\n\t" \
  "v_mov_b32 v161, 0\n\t" \
  "v_mov_b32 v162, 0\n\t" \
  "v_mov_b32 v163, 0\n\t" \
  "v_mov_b32 v164, 0\n\t" \
  "v_mov_b32 v165, 0\n\t" \
  "v_mov_b32 v166, 0\n\t" \
  "v_mov_b32 v167, 0\n\t" \
  "v_mov_b32 v168, 0\n\t" \
  "v_mov_b32 v169, 0\n\t" \
  "v_mov_b32 v170, 0\n\t" \
  "v_mov_b32 v171, 0\n\t" \
  "v_mov_b32 v172, 0\n\t" \
  "v_mov_b32 v173, 0\n\t" \
  "v_mov_b32 v174, 0\n\t" \
  "v_mov_b32 v175, 0\n\t" \
  "v_mov_b32 v176, 0\n\t" \
  "v_mov_b32 v177, 0\n\t" \
  "v_mov_b32 v178, 0\n\t" \
  "v_mov_b32 v179, 0\n\t" \
  "s_waitcnt vmcnt(11) lgkmcnt(0)\n\ts_barrier\n\t" \
  "ds_read_b128 v[128:131], v202\n\t" \
  "ds_read_b64_tr_b16 a[0:1], %15 offset:0\n\t" \
  "ds_read_b64_tr_b16 a[2:3], %15 offset:512\n\t" \
  "ds_read_b64_tr_b16 a[4:5], %16 offset:0\n\t" \
  "s_add_u32 s40, %10, 256\n\t" \
  "s_add_u32 m0, %8, 81920\n\t" \
  "s_nop 0\n\t" \
  "buffer_load_dwordx4 v214, %6, s40 offen lds\n\t" \
  "ds_read_b64_tr_b16 a[6:7], %16 offset:512\n\t" \
  "ds_read_b64_tr_b16 a[8:9], %17 offset:0\n\t" \
  "ds_read_b64_tr_b16 a[10:11], %17 offset:512\n\t" \
  "ds_read_b64_tr_b16 a[12:13], %18 offset:0\n\t" \
  "buffer_load_dwordx4 v215, %6, s40 offen offset:1024 lds\n\t" \
  "ds_read_b64_tr_b16 a[14:15], %18 offset:512\n\t" \
  "ds_read_b64_tr_b16 a[16:17], %19 offset:0\n\t" \
  "ds_read_b64_tr_b16 a[18:19], %19 offset:512\n\t" \
  "ds_read_b128 v[132:135], v203\n\t" \
  "buffer_load_dwordx4 v216, %6, s40 offen offset:2048 lds\n\t" \
  "ds_read_b64_tr_b16 a[20:21], %15 offset:2048\n\t" \
  "ds_read_b64_tr_b16 a[22:23], %15 offset:2560\n\t" \
  "ds_read_b64_tr_b16 a[24:25], %16 offset:2048\n\t" \
  "ds_read_b64_tr_b16 a[26:27], %16 offset:2560\n\t" \
  "buffer_load_dwordx4 v217, %6, s40 offen offset:3072 lds\n\t" \
  "ds_read_b64_tr_b16 a[28:29], %17 offset:2048\n\t" \
  "ds_read_b64_tr_b16 a[30:31], %17 offset:2560\n\t" \
  "ds_read_b64_tr_b16 a[32:33], %18 offset:2048\n\t" \
  "ds_read_b64_tr_b16 a[34:35], %18 offset:2560\n\t" \
  "s_add_u32 s40, %11, 442368\n\t" \
  "s_add_u32 m0, %9, 81920\n\t" \
  "s_nop 0\n\t" \
  "buffer_load_dwordx4 %23, %7, s40 offen lds\n\t" \
  "ds_read_b64_tr_b16 a[36:37], %19 offset:2048\n\t" \
  "ds_read_b64_tr_b16 a[38:39], %19 offset:2560\n\t" \
  "ds_read_b128 v[136:139], v204\n\t" \
  "ds_read_b64_tr_b16 a[40:41], %15 offset:4096\n\t" \
  "s_add_u32 m0, %9, 82944\n\t" \
  "s_nop 0\n\t" \
  "buffer_load_dwordx4 %24, %7, s40 offen lds\n\t" \
  "ds_read_b64_tr_b16 a[42:43], %15 offset:4608\n\t" \
  "ds_read_b64_tr_b16 a[44:45], %16 offset:4096\n\t" \
  "ds_read_b64_tr_b16 a[46:47], %16 offset:4608\n\t" \
  "ds_read_b64_tr_b16 a[48:49], %17 offset:4096\n\t" \
  "s_add_u32 m0, %9, 83968\n\t" \
  "s_nop 0\n\t" \
  "buffer_load_dwordx4 %25, %7, s40 offen lds\n\t" \
  "ds_read_b64_tr_b16 a[50:51], %17 offset:4608\n\t" \
  "ds_read_b64_tr_b16 a[52:53], %18 offset:4096\n\t" \
  "ds_read_b64_tr_b16 a[54:55], %18 offset:4608\n\t" \
  "ds_read_b64_tr_b16 a[56:57], %19 offset:4096\n\t" \
  "s_add_u32 m0, %9, 84992\n\t" \
  "s_nop 0\n\t" \
  "buffer_load_dwordx4 %26, %7, s40 offen lds\n\t" \
  "ds_read_b64_tr_b16 a[58:59], %19 offset:4608\n\t" \
  "ds_read_b128 v[140:143], v205\n\t" \
  "ds_read_b64_tr_b16 a[60:61], %15 offset:6144\n\t" \
  "ds_read_b64_tr_b16 a[62:63], %15 offset:6656\n\t" \
  "s_add_u32 m0, %9, 86016\n\t" \
  "s_nop 0\n\t" \
  "buffer_load_dwordx4 %27, %7, s40 offen lds\n\t" \
  "ds_read_b64_tr_b16 a[64:65], %16 offset:6144\n\t" \
  "ds_read_b64_tr_b16 a[66:67], %16 offset:6656\n\t" \
  "ds_read_b64_tr_b16 a[68:69], %17 offset:6144\n\t" \
  "ds_read_b64_tr_b16 a[70:71], %17 offset:6656\n\t" \
  "s_add_u32 m0, %9, 87040\n\t" \
  "s_nop 0\n\t" \
  "buffer_load_dwordx4 %28, %7, s40 offen lds\n\t" \
  "ds_read_b64_tr_b16 a[72:73], %18 offset:6144\n\t" \
  "ds_read_b64_tr_b16 a[74:75], %18 offset:6656\n\t" \
  "ds_read_b64_tr_b16 a[76:77], %19 offset:6144\n\t" \
  "ds_read_b64_tr_b16 a[78:79], %19 offset:6656\n\t" \
  "s_waitcnt vmcnt(11) lgkmcnt(0)\n\ts_barrier\n\t" \
  "v_mfma_f32_32x32x16_bf16 %0, a[0:3], v[128:131], 0\n\t" \
  "ds_read_b128 v[144:147], v206\n\t" \
  "ds_read_b64_tr_b16 a[80:81], v192 offset:0\n\t" \
  "v_mfma_f32_32x32x16_bf16 %1, a[4:7], v[128:131], 0\n\t" \
  "ds_read_b64_tr_b16 a[82:83], v192 offset:512\n\t" \
  "ds_read_b64_tr_b16 a[84:85], v193 offset:0\n\t" \
  "ds_read_b64_tr_b16 a[86:87], v193 offset:512\n\t" \
  "s_add_u32 s40, %10, 384\n\t" \
  "s_add_u32 m0, %8, 0\n\t" \
  "s_nop 0\n\t" \
  "buffer_load_dwordx4 v214, %6, s40 offen lds\n\t" \
  "v_mfma_f32_32x32x16_bf16 %2, a[8:11], v[128:131], 0\n\t" \
  "v_mfma_f32_16x16x32_bf16 v[236:239], v[188:191], v[128:131], 0\n\t" \
  "ds_read_b64_tr_b16 a[88:89], v194 offset:0\n\t" \
  "ds_read_b64_tr_b16 a[90:91], v194 offset:512\n\t" \
  "ds_read_b64_tr_b16 a[92:93], v195 offset:0\n\t" \
  "v_mfma_f32_32x32x16_bf16 %3, v[128:131], a[12:15], 0\n\t" \
  "ds_read_b64_tr_b16 a[94:95], v195 offset:512\n\t" \
  "ds_read_b64_tr_b16 a[96:97], v196 offset:0\n\t" \
  "ds_read_b64_tr_b16 a[98:99], v196 offset:512\n\t" \
  "buffer_load_dwordx4 v215, %6, s40 offen offset:1024 lds\n\t" \
  "v_mfma_f32_32x32x16_bf16 %4, v[128:131], a[16:19], 0\n\t" \
  "v_mfma_f32_32x32x16_bf16 %5, v[128:131], v[128:131], 0\n\t" \
  "ds_read_b128 v[148:151], v207\n\t" \
  "ds_read_b64_tr_b16 a[100:101], v192 offset:2048\n\t" \
  "v_mfma_f32_32x32x16_bf16 %0, a[20:23], v[132:135], %0\n\t" \
  "ds_read_b64_tr_b16 a[102:103], v192 offset:2560\n\t" \
  "ds_read_b64_tr_b16 a[104:105], v193 offset:2048\n\t" \
  "ds_read_b64_tr_b16 a[106:107], v193 offset:2560\n\t" \
  "buffer_load_dwordx4 v216, %6, s40 offen offset:2048 lds\n\t" \
  "v_mfma_f32_32x32x16_bf16 %1, a[24:27], v[132:135], %1\n\t" \
  "ds_read_b64_tr_b16 a[108:109], v194 offset:2048\n\t" \
  "ds_read_b64_tr_b16 a[110:111], v194 offset:2560\n\t" \
  "ds_read_b64_tr_b16 a[112:113], v195 offset:2048\n\t" \
  "v_mfma_f32_32x32x16_bf16 %2, a[28:31], v[132:135], %2\n\t" \
  "v_mfma_f32_16x16x32_bf16 v[236:239], v[188:191], v[132:135], v[236:239]\n\t" \
  "ds_read_b64_tr_b16 a[114:115], v195 offset:2560\n\t" \
  "ds_read_b64_tr_b16 a[116:117], v196 offset:2048\n\t" \
  "ds_read_b64_tr_b16 a[118:119], v196 offset:2560\n\t" \
  "buffer_load_dwordx4 v217, %6, s40 offen offset:3072 lds\n\t" \
  "v_mfma_f32_32x32x16_bf16 %3, v[132:135], a[32:35], %3\n\t" \
  "ds_read_b128 v[152:155], v208\n\t" \
  "ds_read_b64_tr_b16 a[120:121], v192 offset:4096\n\t" \
  "v_mfma_f32_32x32x16_bf16 %4, v[132:135], a[36:39], %4\n\t" \
  "v_mfma_f32_32x32x16_bf16 %5, v[132:135], v[132:135], %5\n\t" \
  "ds_read_b64_tr_b16 a[122:123], v192 offset:4608\n\t" \
  "ds_read_b64_tr_b16 a[124:125], v193 offset:4096\n\t" \
  "ds_read_b64_tr_b16 a[126:127], v193 offset:4608\n\t" \
  "s_add_u32 s40, %11, 663552\n\t" \
  "s_add_u32 m0, %9, 0\n\t" \
  "s_nop 0\n\t" \
  "buffer_load_dwordx4 %23, %7, s40 offen lds\n\t" \
  "v_mfma_f32_32x32x16_bf16 %0, a[40:43], v[136:139], %0\n\t" \
  "ds_read_b64_tr_b16 a[128:129], v194 offset:4096\n\t" \
  "ds_read_b64_tr_b16 a[130:131], v194 offset:4608\n\t" \
  "ds_read_b64_tr_b16 a[132:133], v195 offset:4096\n\t" \
  "v_mfma_f32_32x32x16_bf16 %1, a[44:47], v[136:139], %1\n\t" \
  "ds_read_b64_tr_b16 a[134:135], v195 offset:4608\n\t" \
  "ds_read_b64_tr_b16 a[136:137], v196 offset:4096\n\t" \
  "ds_read_b64_tr_b16 a[138:139], v196 offset:4608\n\t" \
  "s_add_u32 m0, %9, 1024\n\t" \
  "s_nop 0\n\t" \
  "buffer_load_dwordx4 %24, %7, s40 offen lds\n\t" \
  "v_mfma_f32_32x32x16_bf16 %2, a[48:51], v[136:139], %2\n\t" \
  "v_mfma_f32_16x16x32_bf16 v[236:239], v[188:191], v[136:139], v[236:239]\n\t" \
  "ds_read_b128 v[156:159], v209\n\t" \
  "ds_read_b64_tr_b16 a[140:141], v192 offset:6144\n\t" \
  "v_mfma_f32_32x32x16_bf16 %3, v[136:139], a[52:55], %3\n\t" \
  "ds_read_b64_tr_b16 a[142:143], v192 offset:6656\n\t" \
  "ds_read_b64_tr_b16 a[144:145], v193 offset:6144\n\t" \
  "ds_read_b64_tr_b16 a[146:147], v193 offset:6656\n\t" \
  "s_add_u32 m0, %9, 2048\n\t" \
  "s_nop 0\n\t" \
  "buffer_load_dwordx4 %25, %7, s40 offen lds\n\t" \
  "v_mfma_f32_32x32x16_bf16 %4, v[136:139], a[56:59], %4\n\t" \
  "v_mfma_f32_32x32x16_bf16 %5, v[136:139], v[136:139], %5\n\t" \
  "ds_read_b64_tr_b16 a[148:149], v194 offset:6144\n\t" \
  "ds_read_b64_tr_b16 a[150:151], v194 offset:6656\n\t" \
  "ds_read_b64_tr_b16 a[152:153], v195 offset:6144\n\t" \
  "v_mfma_f32_32x32x16_bf16 %0, a[60:63], v[140:143], %0\n\t" \
  "ds_read_b64_tr_b16 a[154:155], v195 offset:6656\n\t" \
  "ds_read_b64_tr_b16 a[156:157], v196 offset:6144\n\t" \
  "ds_read_b64_tr_b16 a[158:159], v196 offset:6656\n\t" \
  "s_add_u32 m0, %9, 3072\n\t" \
  "s_nop 0\n\t" \
  "buffer_load_dwordx4 %26, %7, s40 offen lds\n\t" \
  "v_mfma_f32_32x32x16_bf16 %1, a[64:67], v[140:143], %1\n\t" \
  "v_mfma_f32_32x32x16_bf16 %2, a[68:71], v[140:143], %2\n\t" \
  "v_mfma_f32_16x16x32_bf16 v[236:239], v[188:191], v[140:143], v[236:239]\n\t" \
  "s_add_u32 m0, %9, 4096\n\t" \
  "s_nop 0\n\t" \
  "buffer_load_dwordx4 %27, %7, s40 offen lds\n\t" \
  "v_mfma_f32_32x32x16_bf16 %3, v[140:143], a[72:75], %3\n\t" \
  "v_mfma_f32_32x32x16_bf16 %4, v[140:143], a[76:79], %4\n\t" \
  "v_mfma_f32_32x32x16_bf16 %5, v[140:143], v[140:143], %5\n\t" \
  "s_add_u32 m0, %9, 5120\n\t" \
  "s_nop 0\n\t" \
  "buffer_load_dwordx4 %28, %7, s40 offen lds\n\t" \
  "s_cmp_lg_u32 %12, 0\n\t" \
  "s_cbranch_scc1 KA1_0_%=\n\t" \
  "v_mfma_f32_16x16x32_bf16 v[160:163], v[188:191], a[0:3], v[160:163]\n\t" \
  "v_mfma_f32_16x16x32_bf16 v[164:167], v[188:191], a[4:7], v[164:167]\n\t" \
  "v_mfma_f32_16x16x32_bf16 v[168:171], v[188:191], a[8:11], v[168:171]\n\t" \
  "v_mfma_f32_16x16x32_bf16 v[172:175], v[188:191], a[12:15], v[172:175]\n\t" \
  "v_mfma_f32_16x16x32_bf16 v[176:179], v[188:191], a[16:19], v[176:179]\n\t" \
  "KA1_0_%=:\n\t" \
  "s_cmp_lg_u32 %12, 1\n\t" \
  "s_cbranch_scc1 KA1_1_%=\n\t" \
  "v_mfma_f32_16x16x32_bf16 v[160:163], v[188:191], a[20:23], v[160:163]\n\t" \
  "v_mfma_f32_16x16x32_bf16 v[164:167], v[188:191], a[24:27], v[164:167]\n\t" \
  "v_mfma_f32_16x16x32_bf16 v[168:171], v[188:191], a[28:31], v[168:171]\n\t" \
  "v_mfma_f32_16x16x32_bf16 v[172:175], v[188:191], a[32:35], v[172:175]\n\t" \
  "v_mfma_f32_16x16x32_bf16 v[176:179], v[188:191], a[36:39], v[176:179]\n\t" \
  "KA1_1_%=:\n\t" \
  "s_cmp_lg_u32 %12, 2\n\t" \
  "s_cbranch_scc1 KA1_2_%=\n\t" \
  "v_mfma_f32_16x16x32_bf16 v[160:163], v[188:191], a[40:43], v[160:163]\n\t" \
  "v_mfma_f32_16x16x32_bf16 v[164:167], v[188:191], a[44:47], v[164:167]\n\t" \
  "v_mfma_f32_16x16x32_bf16 v[168:171], v[188:191], a[48:51], v[168:171]\n\t" \
  "v_mfma_f32_16x16x32_bf16 v[172:175], v[188:191], a[52:55], v[172:175]\n\t" \
  "v_mfma_f32_16x16x32_bf16 v[176:179], v[188:191], a[56:59], v[176:179]\n\t" \
  "KA1_2_%=:\n\t" \
  "s_cmp_lg_u32 %12, 3\n\t" \
  "s_cbranch_scc1 KA1_3_%=\n\t" \
  "v_mfma_f32_16x16x32_bf16 v[160:163], v[188:191], a[60:63], v[160:163]\n\t" \
  "v_mfma_f32_16x16x32_bf16 v[164:167], v[188:191], a[64:67], v[164:167]\n\t" \
  "v_mfma_f32_16x16x32_bf16 v[168:171], v[188:191], a[68:71], v[168:171]\n\t" \
  "v_mfma_f32_16x16x32_bf16 v[172:175], v[188:191], a[72:75], v[172:175]\n\t" \
  "v_mfma_f32_16x16x32_bf16 v[176:179], v[188:191], a[76:79], v[176:179]\n\t" \
  "KA1_3_%=:\n\t" \
  "s_waitcnt vmcnt(10) lgkmcnt(0)\n\ts_barrier\n\t" \
  "v_mfma_f32_32x32x16_bf16 %0, a[80:83], v[144:147], %0\n\t" \
  "ds_read_b128 v[128:131], v210\n\t" \
  "ds_read_b64_tr_b16 a[0:1], v197 offset:0\n\t" \
  "v_mfma_f32_32x32x16_bf16 %1, a[84:87], v[144:147], %1\n\t" \
  "ds_read_b64_tr_b16 a[2:3], v197 offset:512\n\t" \
  "ds_read_b64_tr_b16 a[4:5], v198 offset:0\n\t" \
  "ds_read_b64_tr_b16 a[6:7], v198 offset:512\n\t" \
  "s_add_u32 s40, %10, 512\n\t" \
  "s_add_u32 m0, %8, 40960\n\t" \
  "s_nop 0\n\t" \
  "buffer_load_dwordx4 v214, %6, s40 offen lds\n\t" \
  "v_mfma_f32_32x32x16_bf16 %2, a[88:91], v[144:147], %2\n\t" \
  "v_mfma_f32_16x16x32_bf16 v[236:239], v[188:191], v[144:147], v[236:239]\n\t" \
  "ds_read_b64_tr_b16 a[8:9], v199 offset:0\n\t" \
  "ds_read_b64_tr_b16 a[10:11], v199 offset:512\n\t" \
  "ds_read_b64_tr_b16 a[12:13], v200 offset:0\n\t" \
  "v_mfma_f32_32x32x16_bf16 %3, v[144:147], a[92:95], %3\n\t" \
  "ds_read_b64_tr_b16 a[14:15], v200 offset:512\n\t" \
  "ds_read_b64_tr_b16 a[16:17], v201 offset:0\n\t" \
  "ds_read_b64_tr_b16 a[18:19], v201 offset:512\n\t" \
  "buffer_load_dwordx4 v215, %6, s40 offen offset:1024 lds\n\t" \
  "v_mfma_f32_32x32x16_bf16 %4, v[144:147], a[96:99], %4\n\t" \
  "v_mfma_f32_32x32x16_bf16 %5, v[144:147], v[144:147], %5\n\t" \
  "ds_read_b128 v[132:135], v211\n\t" \
  "ds_read_b64_tr_b16 a[20:21], v197 offset:2048\n\t" \
  "v_mfma_f32_32x32x16_bf16 %0, a[100:103], v[148:151], %0\n\t" \
  "ds_read_b64_tr_b16 a[22:23], v197 offset:2560\n\t" \
  "ds_read_b64_tr_b16 a[24:25], v198 offset:2048\n\t" \
  "ds_read_b64_tr_b16 a[26:27], v198 offset:2560\n\t" \
  "buffer_load_dwordx4 v216, %6, s40 offen offset:2048 lds\n\t" \
  "v_mfma_f32_32x32x16_bf16 %1, a[104:107], v[148:151], %1\n\t" \
  "ds_read_b64_tr_b16 a[28:29], v199 offset:2048\n\t" \
  "ds_read_b64_tr_b16 a[30:31], v199 offset:2560\n\t" \
  "ds_read_b64_tr_b16 a[32:33], v200 offset:2048\n\t" \
  "v_mfma_f32_32x32x16_bf16 %2, a[108:111], v[148:151], %2\n\t" \
  "v_mfma_f32_16x16x32_bf16 v[236:239], v[188:191], v[148:151], v[236:239]\n\t" \
  "ds_read_b64_tr_b16 a[34:35], v200 offset:2560\n\t" \
  "ds_read_b64_tr_b16 a[36:37], v201 offset:2048\n\t" \
  "ds_read_b64_tr_b16 a[38:39], v201 offset:2560\n\t" \
  "buffer_load_dwordx4 v217, %6, s40 offen offset:3072 lds\n\t" \
  "v_mfma_f32_32x32x16_bf16 %3, v[148:151], a[112:115], %3\n\t" \
  "ds_read_b128 v[136:139], v212\n\t" \
  "ds_read_b64_tr_b16 a[40:41], v197 offset:4096\n\t" \
  "v_mfma_f32_32x32x16_bf16 %4, v[148:151], a[116:119], %4\n\t" \
  "v_mfma_f32_32x32x16_bf16 %5, v[148:151], v[148:151], %5\n\t" \
  "ds_read_b64_tr_b16 a[42:43], v197 offset:4608\n\t" \
  "ds_read_b64_tr_b16 a[44:45], v198 offset:4096\n\t" \
  "ds_read_b64_tr_b16 a[46:47], v198 offset:4608\n\t" \
  "s_add_u32 s40, %11, 884736\n\t" \
  "s_add_u32 m0, %9, 40960\n\t" \
  "s_nop 0\n\t" \
  "buffer_load_dwordx4 %23, %7, s40 offen lds\n\t" \
  "v_mfma_f32_32x32x16_bf16 %0, a[120:123], v[152:155], %0\n\t" \
  "ds_read_b64_tr_b16 a[48:49], v199 offset:4096\n\t" \
  "ds_read_b64_tr_b16 a[50:51], v199 offset:4608\n\t" \
  "ds_read_b64_tr_b16 a[52:53], v200 offset:4096\n\t" \
  "v_mfma_f32_32x32x16_bf16 %1, a[124:127], v[152:155], %1\n\t" \
  "ds_read_b64_tr_b16 a[54:55], v200 offset:4608\n\t" \
  "ds_read_b64_tr_b16 a[56:57], v201 offset:4096\n\t" \
  "ds_read_b64_tr_b16 a[58:59], v201 offset:4608\n\t" \
  "s_add_u32 m0, %9, 41984\n\t" \
  "s_nop 0\n\t" \
  "buffer_load_dwordx4 %24, %7, s40 offen lds\n\t" \
  "v_mfma_f32_32x32x16_bf16 %2, a[128:131], v[152:155], %2\n\t" \
  "v_mfma_f32_16x16x32_bf16 v[236:239], v[188:191], v[152:155], v[236:239]\n\t" \
  "ds_read_b128 v[140:143], v213\n\t" \
  "ds_read_b64_tr_b16 a[60:61], v197 offset:6144\n\t" \
  "v_mfma_f32_32x32x16_bf16 %3, v[152:155], a[132:135], %3\n\t" \
  "ds_read_b64_tr_b16 a[62:63], v197 offset:6656\n\t" \
  "ds_read_b64_tr_b16 a[64:65], v198 offset:6144\n\t" \
  "ds_read_b64_tr_b16 a[66:67], v198 offset:6656\n\t" \
  "s_add_u32 m0, %9, 43008\n\t" \
  "s_nop 0\n\t" \
  "buffer_load_dwordx4 %25, %7, s40 offen lds\n\t" \
  "v_mfma_f32_32x32x16_bf16 %4, v[152:155], a[136:139], %4\n\t" \
  "v_mfma_f32_32x32x16_bf16 %5, v[152:155], v[152:155], %5\n\t" \
  "ds_read_b64_tr_b16 a[68:69], v199 offset:6144\n\t" \
  "ds_read_b64_tr_b16 a[70:71], v199 offset:6656\n\t" \
  "ds_read_b64_tr_b16 a[72:73], v200 offset:6144\n\t" \
  "v_mfma_f32_32x32x16_bf16 %0, a[140:143], v[156:159], %0\n\t" \
  "ds_read_b64_tr_b16 a[74:75], v200 offset:6656\n\t" \
  "ds_read_b64_tr_b16 a[76:77], v201 offset:6144\n\t" \
  "ds_read_b64_tr_b16 a[78:79], v201 offset:6656\n\t" \
  "s_add_u32 m0, %9, 44032\n\t" \
  "s_nop 0\n\t" \
  "buffer_load_dwordx4 %26, %7, s40 offen lds\n\t" \
  "v_mfma_f32_32x32x16_bf16 %1, a[144:147], v[156:159], %1\n\t" \
  "v_mfma_f32_32x32x16_bf16 %2, a[148:151], v[156:159], %2\n\t" \
  "v_mfma_f32_16x16x32_bf16 v[236:239], v[188:191], v[156:159], v[236:239]\n\t" \
  "s_add_u32 m0, %9, 45056\n\t" \
  "s_nop 0\n\t" \
  "buffer_load_dwordx4 %27, %7, s40 offen lds\n\t" \
  "v_mfma_f32_32x32x16_bf16 %3, v[156:159], a[152:155], %3\n\t" \
  "v_mfma_f32_32x32x16_bf16 %4, v[156:159], a[156:159], %4\n\t" \
  "v_mfma_f32_32x32x16_bf16 %5, v[156:159], v[156:159], %5\n\t" \
  "s_add_u32 m0, %9, 46080\n\t" \
  "s_nop 0\n\t" \
  "buffer_load_dwordx4 %28, %7, s40 offen lds\n\t" \
  "s_cmp_lg_u32 %12, 0\n\t" \
  "s_cbranch_scc1 KA2_0_%=\n\t" \
  "v_mfma_f32_16x16x32_bf16 v[160:163], v[188:191], a[80:83], v[160:163]\n\t" \
  "v_mfma_f32_16x16x32_bf16 v[164:167], v[188:191], a[84:87], v[164:167]\n\t" \
  "v_mfma_f32_16x16x32_bf16 v[168:171], v[188:191], a[88:91], v[168:171]\n\t" \
  "v_mfma_f32_16x16x32_bf16 v[172:175], v[188:191], a[92:95], v[172:175]\n\t" \
  "v_mfma_f32_16x16x32_bf16 v[176:179], v[188:191], a[96:99], v[176:179]\n\t" \
  "KA2_0_%=:\n\t" \
  "s_cmp_lg_u32 %12, 1\n\t" \
  "s_cbranch_scc1 KA2_1_%=\n\t" \
  "v_mfma_f32_16x16x32_bf16 v[160:163], v[188:191], a[100:103], v[160:163]\n\t" \
  "v_mfma_f32_16x16x32_bf16 v[164:167], v[188:191], a[104:107], v[164:167]\n\t" \
  "v_mfma_f32_16x16x32_bf16 v[168:171], v[188:191], a[108:111], v[168:171]\n\t" \
  "v_mfma_f32_16x16x32_bf16 v[172:175], v[188:191], a[112:115], v[172:175]\n\t" \
  "v_mfma_f32_16x16x32_bf16 v[176:179], v[188:191], a[116:119], v[176:179]\n\t" \
  "KA2_1_%=:\n\t" \
  "s_cmp_lg_u32 %12, 2\n\t" \
  "s_cbranch_scc1 KA2_2_%=\n\t" \
  "v_mfma_f32_16x16x32_bf16 v[160:163], v[188:191], a[120:123], v[160:163]\n\t" \
  "v_mfma_f32_16x16x32_bf16 v[164:167], v[188:191], a[124:127], v[164:167]\n\t" \
  "v_mfma_f32_16x16x32_bf16 v[168:171], v[188:191], a[128:131], v[168:171]\n\t" \
  "v_mfma_f32_16x16x32_bf16 v[172:175], v[188:191], a[132:135], v[172:175]\n\t" \
  "v_mfma_f32_16x16x32_bf16 v[176:179], v[188:191], a[136:139], v[176:179]\n\t" \
  "KA2_2_%=:\n\t" \
  "s_cmp_lg_u32 %12, 3\n\t" \
  "s_cbranch_scc1 KA2_3_%=\n\t" \
  "v_mfma_f32_16x16x32_bf16 v[160:163], v[188:191], a[140:143], v[160:163]\n\t" \
  "v_mfma_f32_16x16x32_bf16 v[164:167], v[188:191], a[144:147], v[164:167]\n\t" \
  "v_mfma_f32_16x16x32_bf16 v[168:171], v[188:191], a[148:151], v[168:171]\n\t" \
  "v_mfma_f32_16x16x32_bf16 v[172:175], v[188:191], a[152:155], v[172:175]\n\t" \
  "v_mfma_f32_16x16x32_bf16 v[176:179], v[188:191], a[156:159], v[176:179]\n\t" \
  "KA2_3_%=:\n\t" \
  "s_waitcnt vmcnt(10) lgkmcnt(0)\n\ts_barrier\n\t" \
  "v_mfma_f32_32x32x16_bf16 %0, a[0:3], v[128:131], %0\n\t" \
  "ds_read_b128 v[144:147], v202\n\t" \
  "ds_read_b64_tr_b16 a[80:81], %15 offset:0\n\t" \
  "v_mfma_f32_32x32x16_bf16 %1, a[4:7], v[128:131], %1\n\t" \
  "ds_read_b64_tr_b16 a[82:83], %15 offset:512\n\t" \
  "ds_read_b64_tr_b16 a[84:85], %16 offset:0\n\t" \
  "ds_read_b64_tr_b16 a[86:87], %16 offset:512\n\t" \
  "s_add_u32 s40, %10, 640\n\t" \
  "s_add_u32 m0, %8, 81920\n\t" \
  "s_nop 0\n\t" \
  "buffer_load_dwordx4 v214, %6, s40 offen lds\n\t" \
  "v_mfma_f32_32x32x16_bf16 %2, a[8:11], v[128:131], %2\n\t" \
  "v_mfma_f32_16x16x32_bf16 v[236:239], v[188:191], v[128:131], v[236:239]\n\t" \
  "ds_read_b64_tr_b16 a[88:89], %17 offset:0\n\t" \
  "ds_read_b64_tr_b16 a[90:91], %17 offset:512\n\t" \
  "ds_read_b64_tr_b16 a[92:93], %18 offset:0\n\t" \
  "v_mfma_f32_32x32x16_bf16 %3, v[128:131], a[12:15], %3\n\t" \
  "ds_read_b64_tr_b16 a[94:95], %18 offset:512\n\t" \
  "ds_read_b64_tr_b16 a[96:97], %19 offset:0\n\t" \
  "ds_read_b64_tr_b16 a[98:99], %19 offset:512\n\t" \
  "buffer_load_dwordx4 v215, %6, s40 offen offset:1024 lds\n\t" \
  "v_mfma_f32_32x32x16_bf16 %4, v[128:131], a[16:19], %4\n\t" \
  "v_mfma_f32_32x32x16_bf16 %5, v[128:131], v[128:131], %5\n\t" \
  "ds_read_b128 v[148:151], v203\n\t" \
  "ds_read_b64_tr_b16 a[100:101], %15 offset:2048\n\t" \
  "v_mfma_f32_32x32x16_bf16 %0, a[20:23], v[132:135], %0\n\t" \
  "ds_read_b64_tr_b16 a[102:103], %15 offset:2560\n\t" \
  "ds_read_b64_tr_b16 a[104:105], %16 offset:2048\n\t" \
  "ds_read_b64_tr_b16 a[106:107], %16 offset:2560\n\t" \
  "buffer_load_dwordx4 v216, %6, s40 offen offset:2048 lds\n\t" \
  "v_mfma_f32_32x32x16_bf16 %1, a[24:27], v[132:135], %1\n\t" \
  "ds_read_b64_tr_b16 a[108:109], %17 offset:2048\n\t" \
  "ds_read_b64_tr_b16 a[110:111], %17 offset:2560\n\t" \
  "ds_read_b64_tr_b16 a[112:113], %18 offset:2048\n\t" \
  "v_mfma_f32_32x32x16_bf16 %2, a[28:31], v[132:135], %2\n\t" \
  "v_mfma_f32_16x16x32_bf16 v[236:239], v[188:191], v[132:135], v[236:239]\n\t" \
  "ds_read_b64_tr_b16 a[114:115], %18 offset:2560\n\t" \
  "ds_read_b64_tr_b16 a[116:117], %19 offset:2048\n\t" \
  "ds_read_b64_tr_b16 a[118:119], %19 offset:2560\n\t" \
  "buffer_load_dwordx4 v217, %6, s40 offen offset:3072 lds\n\t" \
  "v_mfma_f32_32x32x16_bf16 %3, v[132:135], a[32:35], %3\n\t" \
  "ds_read_b128 v[152:155], v204\n\t" \
  "ds_read_b64_tr_b16 a[120:121], %15 offset:4096\n\t" \
  "v_mfma_f32_32x32x16_bf16 %4, v[132:135], a[36:39], %4\n\t" \
  "v_mfma_f32_32x32x16_bf16 %5, v[132:135], v[132:135], %5\n\t" \
  "ds_read_b64_tr_b16 a[122:123], %15 offset:4608\n\t" \
  "ds_read_b64_tr_b16 a[124:125], %16 offset:4096\n\t" \
  "ds_read_b64_tr_b16 a[126:127], %16 offset:4608\n\t" \
  "s_add_u32 s40, %11, 1105920\n\t" \
  "s_add_u32 m0, %9, 81920\n\t" \
  "s_nop 0\n\t" \
  "buffer_load_dwordx4 %23, %7, s40 offen lds\n\t" \
  "v_mfma_f32_32x32x16_bf16 %0, a[40:43], v[136:139], %0\n\t" \
  "ds_read_b64_tr_b16 a[128:129], %17 offset:4096\n\t" \
  "ds_read_b64_tr_b16 a[130:131], %17 offset:4608\n\t" \
  "ds_read_b64_tr_b16 a[132:133], %18 offset:4096\n\t" \
  "v_mfma_f32_32x32x16_bf16 %1, a[44:47], v[136:139], %1\n\t" \
  "ds_read_b64_tr_b16 a[134:135], %18 offset:4608\n\t" \
  "ds_read_b64_tr_b16 a[136:137], %19 offset:4096\n\t" \
  "ds_read_b64_tr_b16 a[138:139], %19 offset:4608\n\t" \
  "s_add_u32 m0, %9, 82944\n\t" \
  "s_nop 0\n\t" \
  "buffer_load_dwordx4 %24, %7, s40 offen lds\n\t" \
  "v_mfma_f32_32x32x16_bf16 %2, a[48:51], v[136:139], %2\n\t" \
  "v_mfma_f32_16x16x32_bf16 v[236:239], v[188:191], v[136:139], v[236:239]\n\t" \
  "ds_read_b128 v[156:159], v205\n\t" \
  "ds_read_b64_tr_b16 a[140:141], %15 offset:6144\n\t" \
  "v_mfma_f32_32x32x16_bf16 %3, v[136:139], a[52:55], %3\n\t" \
  "ds_read_b64_tr_b16 a[142:143], %15 offset:6656\n\t" \
  "ds_read_b64_tr_b16 a[144:145], %16 offset:6144\n\t" \
  "ds_read_b64_tr_b16 a[146:147], %16 offset:6656\n\t" \
  "s_add_u32 m0, %9, 83968\n\t" \
  "s_nop 0\n\t" \
  "buffer_load_dwordx4 %25, %7, s40 offen lds\n\t" \
  "v_mfma_f32_32x32x16_bf16 %4, v[136:139], a[56:59], %4\n\t" \
  "v_mfma_f32_32x32x16_bf16 %5, v[136:139], v[136:139], %5\n\t" \
  "ds_read_b64_tr_b16 a[148:149], %17 offset:6144\n\t" \
  "ds_read_b64_tr_b16 a[150:151], %17 offset:6656\n\t" \
  "ds_read_b64_tr_b16 a[152:153], %18 offset:6144\n\t" \
  "v_mfma_f32_32x32x16_bf16 %0, a[60:63], v[140:143], %0\n\t" \
  "ds_read_b64_tr_b16 a[154:155], %18 offset:6656\n\t" \
  "ds_read_b64_tr_b16 a[156:157], %19 offset:6144\n\t" \
  "ds_read_b64_tr_b16 a[158:159], %19 offset:6656\n\t" \
  "s_add_u32 m0, %9, 84992\n\t" \
  "s_nop 0\n\t" \
  "buffer_load_dwordx4 %26, %7, s40 offen lds\n\t" \
  "v_mfma_f32_32x32x16_bf16 %1, a[64:67], v[140:143], %1\n\t" \
  "v_mfma_f32_32x32x16_bf16 %2, a[68:71], v[140:143], %2\n\t" \
  "v_mfma_f32_16x16x32_bf16 v[236:239], v[188:191], v[140:143], v[236:239]\n\t" \
  "s_add_u32 m0, %9, 86016\n\t" \
  "s_nop 0\n\t" \
  "buffer_load_dwordx4 %27, %7, s40 offen lds\n\t" \
  "v_mfma_f32_32x32x16_bf16 %3, v[140:143], a[72:75], %3\n\t" \
  "v_mfma_f32_32x32x16_bf16 %4, v[140:143], a[76:79], %4\n\t" \
  "v_mfma_f32_32x32x16_bf16 %5, v[140:143], v[140:143], %5\n\t" \
  "s_add_u32 m0, %9, 87040\n\t" \
  "s_nop 0\n\t" \
  "buffer_load_dwordx4 %28, %7, s40 offen lds\n\t" \
  "s_cmp_lg_u32 %12, 0\n\t" \
  "s_cbranch_scc1 KA3_0_%=\n\t" \
  "v_mfma_f32_16x16x32_bf16 v[160:163], v[188:191], a[0:3], v[160:163]\n\t" \
  "v_mfma_f32_16x16x32_bf16 v[164:167], v[188:191], a[4:7], v[164:167]\n\t" \
  "v_mfma_f32_16x16x32_bf16 v[168:171], v[188:191], a[8:11], v[168:171]\n\t" \
  "v_mfma_f32_16x16x32_bf16 v[172:175], v[188:191], a[12:15], v[172:175]\n\t" \
  "v_mfma_f32_16x16x32_bf16 v[176:179], v[188:191], a[16:19], v[176:179]\n\t" \
  "KA3_0_%=:\n\t" \
  "s_cmp_lg_u32 %12, 1\n\t" \
  "s_cbranch_scc1 KA3_1_%=\n\t" \
  "v_mfma_f32_16x16x32_bf16 v[160:163], v[188:191], a[20:23], v[160:163]\n\t" \
  "v_mfma_f32_16x16x32_bf16 v[164:167], v[188:191], a[24:27], v[164:167]\n\t" \
  "v_mfma_f32_16x16x32_bf16 v[168:171], v[188:191], a[28:31], v[168:171]\n\t" \
  "v_mfma_f32_16x16x32_bf16 v[172:175], v[188:191], a[32:35], v[172:175]\n\t" \
  "v_mfma_f32_16x16x32_bf16 v[176:179], v[188:191], a[36:39], v[176:179]\n\t" \
  "KA3_1_%=:\n\t" \
  "s_cmp_lg_u32 %12, 2\n\t" \
  "s_cbranch_scc1 KA3_2_%=\n\t" \
  "v_mfma_f32_16x16x32_bf16 v[160:163], v[188:191], a[40:43], v[160:163]\n\t" \
  "v_mfma_f32_16x16x32_bf16 v[164:167], v[188:191], a[44:47], v[164:167]\n\t" \
  "v_mfma_f32_16x16x32_bf16 v[168:171], v[188:191], a[48:51], v[168:171]\n\t" \
  "v_mfma_f32_16x16x32_bf16 v[172:175], v[188:191], a[52:55], v[172:175]\n\t" \
  "v_mfma_f32_16x16x32_bf16 v[176:179], v[188:191], a[56:59], v[176:179]\n\t" \
  "KA3_2_%=:\n\t" \
  "s_cmp_lg_u32 %12, 3\n\t" \
  "s_cbranch_scc1 KA3_3_%=\n\t" \
  "v_mfma_f32_16x16x32_bf16 v[160:163], v[188:191], a[60:63], v[160:163]\n\t" \
  "v_mfma_f32_16x16x32_bf16 v[164:167], v[188:191], a[64:67], v[164:167]\n\t" \
  "v_mfma_f32_16x16x32_bf16 v[168:171], v[188:191], a[68:71], v[168:171]\n\t" \
  "v_mfma_f32_16x16x32_bf16 v[172:175], v[188:191], a[72:75], v[172:175]\n\t" \
  "v_mfma_f32_16x16x32_bf16 v[176:179], v[188:191], a[76:79], v[176:179]\n\t" \
  "KA3_3_%=:\n\t" \
  "s_waitcnt vmcnt(10) lgkmcnt(0)\n\ts_barrier\n\t" \
  "v_mfma_f32_32x32x16_bf16 %0, a[80:83], v[144:147], %0\n\t" \
  "ds_read_b128 v[128:131], v206\n\t" \
  "ds_read_b64_tr_b16 a[0:1], v192 offset:0\n\t" \
  "v_mfma_f32_32x32x16_bf16 %1, a[84:87], v[144:147], %1\n\t" \
  "ds_read_b64_tr_b16 a[2:3], v192 offset:512\n\t" \
  "ds_read_b64_tr_b16 a[4:5], v193 offset:0\n\t" \
  "ds_read_b64_tr_b16 a[6:7], v193 offset:512\n\t" \
  "s_add_u32 s40, %10, 768\n\t" \
  "s_add_u32 m0, %8, 0\n\t" \
  "s_nop 0\n\t" \
  "buffer_load_dwordx4 v214, %6, s40 offen lds\n\t" \
  "v_mfma_f32_32x32x16_bf16 %2, a[88:91], v[144:147], %2\n\t" \
  "v_mfma_f32_16x16x32_bf16 v[236:239], v[188:191], v[144:147], v[236:239]\n\t" \
  "ds_read_b64_tr_b16 a[8:9], v194 offset:0\n\t" \
  "ds_read_b64_tr_b16 a[10:11], v194 offset:512\n\t" \
  "ds_read_b64_tr_b16 a[12:13], v195 offset:0\n\t" \
  "v_mfma_f32_32x32x16_bf16 %3, v[144:147], a[92:95], %3\n\t" \
  "ds_read_b64_tr_b16 a[14:15], v195 offset:512\n\t" \
  "ds_read_b64_tr_b16 a[16:17], v196 offset:0\n\t" \
  "ds_read_b64_tr_b16 a[18:19], v196 offset:512\n\t" \
  "buffer_load_dwordx4 v215, %6, s40 offen offset:1024 lds\n\t" \
  "v_mfma_f32_32x32x16_bf16 %4, v[144:147], a[96:99], %4\n\t" \
  "v_mfma_f32_32x32x16_bf16 %5, v[144:147], v[144:147], %5\n\t" \
  "ds_read_b128 v[132:135], v207\n\t" \
  "ds_read_b64_tr_b16 a[20:21], v192 offset:2048\n\t" \
  "v_mfma_f32_32x32x16_bf16 %0, a[100:103], v[148:151], %0\n\t" \
  "ds_read_b64_tr_b16 a[22:23], v192 offset:2560\n\t" \
  "ds_read_b64_tr_b16 a[24:25], v193 offset:2048\n\t" \
  "ds_read_b64_tr_b16 a[26:27], v193 offset:2560\n\t" \
  "buffer_load_dwordx4 v216, %6, s40 offen offset:2048 lds\n\t" \
  "v_mfma_f32_32x32x16_bf16 %1, a[104:107], v[148:151], %1\n\t" \
  "ds_read_b64_tr_b16 a[28:29], v194 offset:2048\n\t" \
  "ds_read_b64_tr_b16 a[30:31], v194 offset:2560\n\t" \
  "ds_read_b64_tr_b16 a[32:33], v195 offset:2048\n\t" \
  "v_mfma_f32_32x32x16_bf16 %2, a[108:111], v[148:151], %2\n\t" \
  "v_mfma_f32_16x16x32_bf16 v[236:239], v[188:191], v[148:151], v[236:239]\n\t" \
  "ds_read_b64_tr_b16 a[34:35], v195 offset:2560\n\t" \
  "ds_read_b64_tr_b16 a[36:37], v196 offset:2048\n\t" \
  "ds_read_b64_tr_b16 a[38:39], v196 offset:2560\n\t" \
  "buffer_load_dwordx4 v217, %6, s40 offen offset:3072 lds\n\t" \
  "v_mfma_f32_32x32x16_bf16 %3, v[148:151], a[112:115], %3\n\t" \
  "ds_read_b128 v[136:139], v208\n\t" \
  "ds_read_b64_tr_b16 a[40:41], v192 offset:4096\n\t" \
  "v_mfma_f32_32x32x16_bf16 %4, v[148:151], a[116:119], %4\n\t" \
  "v_mfma_f32_32x32x16_bf16 %5, v[148:151], v[148:151], %5\n\t" \
  "ds_read_b64_tr_b16 a[42:43], v192 offset:4608\n\t" \
  "ds_read_b64_tr_b16 a[44:45], v193 offset:4096\n\t" \
  "ds_read_b64_tr_b16 a[46:47], v193 offset:4608\n\t" \
  "s_add_u32 s40, %11, 1327104\n\t" \
  "s_add_u32 m0, %9, 0\n\t" \
  "s_nop 0\n\t" \
  "buffer_load_dwordx4 %23, %7, s40 offen lds\n\t" \
  "v_mfma_f32_32x32x16_bf16 %0, a[120:123], v[152:155], %0\n\t" \
  "ds_read_b64_tr_b16 a[48:49], v194 offset:4096\n\t" \
  "ds_read_b64_tr_b16 a[50:51], v194 offset:4608\n\t" \
  "ds_read_b64_tr_b16 a[52:53], v195 offset:4096\n\t" \
  "v_mfma_f32_32x32x16_bf16 %1, a[124:127], v[152:155], %1\n\t" \
  "ds_read_b64_tr_b16 a[54:55], v195 offset:4608\n\t" \
  "ds_read_b64_tr_b16 a[56:57], v196 offset:4096\n\t" \
  "ds_read_b64_tr_b16 a[58:59], v196 offset:4608\n\t" \
  "s_add_u32 m0, %9, 1024\n\t" \
  "s_nop 0\n\t" \
  "buffer_load_dwordx4 %24, %7, s40 offen lds\n\t" \
  "v_mfma_f32_32x32x16_bf16 %2, a[128:131], v[152:155], %2\n\t" \
  "v_mfma_f32_16x16x32_bf16 v[236:239], v[188:191], v[152:155], v[236:239]\n\t" \
  "ds_read_b128 v[140:143], v209\n\t" \
  "ds_read_b64_tr_b16 a[60:61], v192 offset:6144\n\t" \
  "v_mfma_f32_32x32x16_bf16 %3, v[152:155], a[132:135], %3\n\t" \
  "ds_read_b64_tr_b16 a[62:63], v192 offset:6656\n\t" \
  "ds_read_b64_tr_b16 a[64:65], v193 offset:6144\n\t" \
  "ds_read_b64_tr_b16 a[66:67], v193 offset:6656\n\t" \
  "s_add_u32 m0, %9, 2048\n\t" \
  "s_nop 0\n\t" \
  "buffer_load_dwordx4 %25, %7, s40 offen lds\n\t" \
  "v_mfma_f32_32x32x16_bf16 %4, v[152:155], a[136:139], %4\n\t" \
  "v_mfma_f32_32x32x16_bf16 %5, v[152:155], v[152:155], %5\n\t" \
  "ds_read_b64_tr_b16 a[68:69], v194 offset:6144\n\t" \
  "ds_read_b64_tr_b16 a[70:71], v194 offset:6656\n\t" \
  "ds_read_b64_tr_b16 a[72:73], v195 offset:6144\n\t" \
  "v_mfma_f32_32x32x16_bf16 %0, a[140:143], v[156:159], %0\n\t" \
  "ds_read_b64_tr_b16 a[74:75], v195 offset:6656\n\t" \
  "ds_read_b64_tr_b16 a[76:77], v196 offset:6144\n\t" \
  "ds_read_b64_tr_b16 a[78:79], v196 offset:6656\n\t" \
  "s_add_u32 m0, %9, 3072\n\t" \
  "s_nop 0\n\t" \
  "buffer_load_dwordx4 %26, %7, s40 offen lds\n\t" \
  "v_mfma_f32_32x32x16_bf16 %1, a[144:147], v[156:159], %1\n\t" \
  "v_mfma_f32_32x32x16_bf16 %2, a[148:151], v[156:159], %2\n\t" \
  "v_mfma_f32_16x16x32_bf16 v[236:239], v[188:191], v[156:159], v[236:239]\n\t" \
  "s_add_u32 m0, %9, 4096\n\t" \
  "s_nop 0\n\t" \
  "buffer_load_dwordx4 %27, %7, s40 offen lds\n\t" \
  "v_mfma_f32_32x32x16_bf16 %3, v[156:159], a[152:155], %3\n\t" \
  "v_mfma_f32_32x32x16_bf16 %4, v[156:159], a[156:159], %4\n\t" \
  "v_mfma_f32_32x32x16_bf16 %5, v[156:159], v[156:159], %5\n\t" \
  "s_add_u32 m0, %9, 5120\n\t" \
  "s_nop 0\n\t" \
  "buffer_load_dwordx4 %28, %7, s40 offen lds\n\t" \
  "s_cmp_lg_u32 %12, 0\n\t" \
  "s_cbranch_scc1 KA4_0_%=\n\t" \
  "v_mfma_f32_16x16x32_bf16 v[160:163], v[188:191], a[80:83], v[160:163]\n\t" \
  "v_mfma_f32_16x16x32_bf16 v[164:167], v[188:191], a[84:87], v[164:167]\n\t" \
  "v_mfma_f32_16x16x32_bf16 v[168:171], v[188:191], a[88:91], v[168:171]\n\t" \
  "v_mfma_f32_16x16x32_bf16 v[172:175], v[188:191], a[92:95], v[172:175]\n\t" \
  "v_mfma_f32_16x16x32_bf16 v[176:179], v[188:191], a[96:99], v[176:179]\n\t" \
  "KA4_0_%=:\n\t" \
  "s_cmp_lg_u32 %12, 1\n\t" \
  "s_cbranch_scc1 KA4_1_%=\n\t" \
  "v_mfma_f32_16x16x32_bf16 v[160:163], v[188:191], a[100:103], v[160:163]\n\t" \
  "v_mfma_f32_16x16x32_bf16 v[164:167], v[188:191], a[104:107], v[164:167]\n\t" \
  "v_mfma_f32_16x16x32_bf16 v[168:171], v[188:191], a[108:111], v[168:171]\n\t" \
  "v_mfma_f32_16x16x32_bf16 v[172:175], v[188:191], a[112:115], v[172:175]\n\t" \
  "v_mfma_f32_16x16x32_bf16 v[176:179], v[188:191], a[116:119], v[176:179]\n\t" \
  "KA4_1_%=:\n\t" \
  "s_cmp_lg_u32 %12, 2\n\t" \
  "s_cbranch_scc1 KA4_2_%=\n\t" \
  "v_mfma_f32_16x16x32_bf16 v[160:163], v[188:191], a[120:123], v[160:163]\n\t" \
  "v_mfma_f32_16x16x32_bf16 v[164:167], v[188:191], a[124:127], v[164:167]\n\t" \
  "v_mfma_f32_16x16x32_bf16 v[168:171], v[188:191], a[128:131], v[168:171]\n\t" \
  "v_mfma_f32_16x16x32_bf16 v[172:175], v[188:191], a[132:135], v[172:175]\n\t" \
  "v_mfma_f32_16x16x32_bf16 v[176:179], v[188:191], a[136:139], v[176:179]\n\t" \
  "KA4_2_%=:\n\t" \
  "s_cmp_lg_u32 %12, 3\n\t" \
  "s_cbranch_scc1 KA4_3_%=\n\t" \
  "v_mfma_f32_16x16x32_bf16 v[160:163], v[188:191], a[140:143], v[160:163]\n\t" \
  "v_mfma_f32_16x16x32_bf16 v[164:167], v[188:191], a[144:147], v[164:167]\n\t" \
  "v_mfma_f32_16x16x32_bf16 v[168:171], v[188:191], a[148:151], v[168:171]\n\t" \
  "v_mfma_f32_16x16x32_bf16 v[172:175], v[188:191], a[152:155], v[172:175]\n\t" \
  "v_mfma_f32_16x16x32_bf16 v[176:179], v[188:191], a[156:159], v[176:179]\n\t" \
  "KA4_3_%=:\n\t" \
  "s_waitcnt vmcnt(10) lgkmcnt(0)\n\ts_barrier\n\t" \
  "v_mfma_f32_32x32x16_bf16 %0, a[0:3], v[128:131], %0\n\t" \
  "ds_read_b128 v[144:147], v210\n\t" \
  "ds_read_b64_tr_b16 a[80:81], v197 offset:0\n\t" \
  "v_mfma_f32_32x32x16_bf16 %1, a[4:7], v[128:131], %1\n\t" \
  "ds_read_b64_tr_b16 a[82:83], v197 offset:512\n\t" \
  "ds_read_b64_tr_b16 a[84:85], v198 offset:0\n\t" \
  "ds_read_b64_tr_b16 a[86:87], v198 offset:512\n\t" \
  "s_add_u32 s40, %10, 896\n\t" \
  "s_add_u32 m0, %8, 40960\n\t" \
  "s_nop 0\n\t" \
  "buffer_load_dwordx4 v214, %6, s40 offen lds\n\t" \
  "v_mfma_f32_32x32x16_bf16 %2, a[8:11], v[128:131], %2\n\t" \
  "v_mfma_f32_16x16x32_bf16 v[236:239], v[188:191], v[128:131], v[236:239]\n\t" \
  "ds_read_b64_tr_b16 a[88:89], v199 offset:0\n\t" \
  "ds_read_b64_tr_b16 a[90:91], v199 offset:512\n\t" \
  "ds_read_b64_tr_b16 a[92:93], v200 offset:0\n\t" \
  "v_mfma_f32_32x32x16_bf16 %3, v[128:131], a[12:15], %3\n\t" \
  "ds_read_b64_tr_b16 a[94:95], v200 offset:512\n\t" \
  "ds_read_b64_tr_b16 a[96:97], v201 offset:0\n\t" \
  "ds_read_b64_tr_b16 a[98:99], v201 offset:512\n\t" \
  "buffer_load_dwordx4 v215, %6, s40 offen offset:1024 lds\n\t" \
  "v_mfma_f32_32x32x16_bf16 %4, v[128:131], a[16:19], %4\n\t" \
  "v_mfma_f32_32x32x16_bf16 %5, v[128:131], v[128:131], %5\n\t" \
  "ds_read_b128 v[148:151], v211\n\t" \
  "ds_read_b64_tr_b16 a[100:101], v197 offset:2048\n\t" \
  "v_mfma_f32_32x32x16_bf16 %0, a[20:23], v[132:135], %0\n\t" \
  "ds_read_b64_tr_b16 a[102:103], v197 offset:2560\n\t" \
  "ds_read_b64_tr_b16 a[104:105], v198 offset:2048\n\t" \
  "ds_read_b64_tr_b16 a[106:107], v198 offset:2560\n\t" \
  "buffer_load_dwordx4 v216, %6, s40 offen offset:2048 lds\n\t" \
  "v_mfma_f32_32x32x16_bf16 %1, a[24:27], v[132:135], %1\n\t" \
  "ds_read_b64_tr_b16 a[108:109], v199 offset:2048\n\t" \
  "ds_read_b64_tr_b16 a[110:111], v199 offset:2560\n\t" \
  "ds_read_b64_tr_b16 a[112:113], v200 offset:2048\n\t" \
  "v_mfma_f32_32x32x16_bf16 %2, a[28:31], v[132:135], %2\n\t" \
  "v_mfma_f32_16x16x32_bf16 v[236:239], v[188:191], v[132:135], v[236:239]\n\t" \
  "ds_read_b64_tr_b16 a[114:115], v200 offset:2560\n\t" \
  "ds_read_b64_tr_b16 a[116:117], v201 offset:2048\n\t" \
  "ds_read_b64_tr_b16 a[118:119], v201 offset:2560\n\t" \
  "buffer_load_dwordx4 v217, %6, s40 offen offset:3072 lds\n\t" \
  "v_mfma_f32_32x32x16_bf16 %3, v[132:135], a[32:35], %3\n\t" \
  "ds_read_b128 v[152:155], v212\n\t" \
  "ds_read_b64_tr_b16 a[120:121], v197 offset:4096\n\t" \
  "v_mfma_f32_32x32x16_bf16 %4, v[132:135], a[36:39], %4\n\t" \
  "v_mfma_f32_32x32x16_bf16 %5, v[132:135], v[132:135], %5\n\t" \
  "ds_read_b64_tr_b16 a[122:123], v197 offset:4608\n\t" \
  "ds_read_b64_tr_b16 a[124:125], v198 offset:4096\n\t" \
  "ds_read_b64_tr_b16 a[126:127], v198 offset:4608\n\t" \
  "s_add_u32 s40, %11, 1548288\n\t" \
  "s_add_u32 m0, %9, 40960\n\t" \
  "s_nop 0\n\t" \
  "buffer_load_dwordx4 %23, %7, s40 offen lds\n\t" \
  "v_mfma_f32_32x32x16_bf16 %0, a[40:43], v[136:139], %0\n\t" \
  "ds_read_b64_tr_b16 a[128:129], v199 offset:4096\n\t" \
  "ds_read_b64_tr_b16 a[130:131], v199 offset:4608\n\t" \
  "ds_read_b64_tr_b16 a[132:133], v200 offset:4096\n\t" \
  "v_mfma_f32_32x32x16_bf16 %1, a[44:47], v[136:139], %1\n\t" \
  "ds_read_b64_tr_b16 a[134:135], v200 offset:4608\n\t" \
  "ds_read_b64_tr_b16 a[136:137], v201 offset:4096\n\t" \
  "ds_read_b64_tr_b16 a[138:139], v201 offset:4608\n\t" \
  "s_add_u32 m0, %9, 41984\n\t" \
  "s_nop 0\n\t" \
  "buffer_load_dwordx4 %24, %7, s40 offen lds\n\t" \
  "v_mfma_f32_32x32x16_bf16 %2, a[48:51], v[136:139], %2\n\t" \
  "v_mfma_f32_16x16x32_bf16 v[236:239], v[188:191], v[136:139], v[236:239]\n\t" \
  "ds_read_b128 v[156:159], v213\n\t" \
  "ds_read_b64_tr_b16 a[140:141], v197 offset:6144\n\t" \
  "v_mfma_f32_32x32x16_bf16 %3, v[136:139], a[52:55], %3\n\t" \
  "ds_read_b64_tr_b16 a[142:143], v197 offset:6656\n\t" \
  "ds_read_b64_tr_b16 a[144:145], v198 offset:6144\n\t" \
  "ds_read_b64_tr_b16 a[146:147], v198 offset:6656\n\t" \
  "s_add_u32 m0, %9, 43008\n\t" \
  "s_nop 0\n\t" \
  "buffer_load_dwordx4 %25, %7, s40 offen lds\n\t" \
  "v_mfma_f32_32x32x16_bf16 %4, v[136:139], a[56:59], %4\n\t" \
  "v_mfma_f32_32x32x16_bf16 %5, v[136:139], v[136:139], %5\n\t" \
  "ds_read_b64_tr_b16 a[148:149], v199 offset:6144\n\t" \
  "ds_read_b64_tr_b16 a[150:151], v199 offset:6656\n\t" \
  "ds_read_b64_tr_b16 a[152:153], v200 offset:6144\n\t" \
  "v_mfma_f32_32x32x16_bf16 %0, a[60:63], v[140:143], %0\n\t" \
  "ds_read_b64_tr_b16 a[154:155], v200 offset:6656\n\t" \
  "ds_read_b64_tr_b16 a[156:157], v201 offset:6144\n\t" \
  "ds_read_b64_tr_b16 a[158:159], v201 offset:6656\n\t" \
  "s_add_u32 m0, %9, 44032\n\t" \
  "s_nop 0\n\t" \
  "buffer_load_dwordx4 %26, %7, s40 offen lds\n\t" \
  "v_mfma_f32_32x32x16_bf16 %1, a[64:67], v[140:143], %1\n\t" \
  "v_mfma_f32_32x32x16_bf16 %2, a[68:71], v[140:143], %2\n\t" \
  "v_mfma_f32_16x16x32_bf16 v[236:239], v[188:191], v[140:143], v[236:239]\n\t" \
  "s_add_u32 m0, %9, 45056\n\t" \
  "s_nop 0\n\t" \
  "buffer_load_dwordx4 %27, %7, s40 offen lds\n\t" \
  "v_mfma_f32_32x32x16_bf16 %3, v[140:143], a[72:75], %3\n\t" \
  "v_mfma_f32_32x32x16_bf16 %4, v[140:143], a[76:79], %4\n\t" \
  "v_mfma_f32_32x32x16_bf16 %5, v[140:143], v[140:143], %5\n\t" \
  "s_add_u32 m0, %9, 46080\n\t" \
  "s_nop 0\n\t" \
  "buffer_load_dwordx4 %28, %7, s40 offen lds\n\t" \
  "s_cmp_lg_u32 %12, 0\n\t" \
  "s_cbranch_scc1 KA5_0_%=\n\t" \
  "v_mfma_f32_16x16x32_bf16 v[160:163], v[188:191], a[0:3], v[160:163]\n\t" \
  "v_mfma_f32_16x16x32_bf16 v[164:167], v[188:191], a[4:7], v[164:167]\n\t" \
  "v_mfma_f32_16x16x32_bf16 v[168:171], v[188:191], a[8:11], v[168:171]\n\t" \
  "v_mfma_f32_16x16x32_bf16 v[172:175], v[188:191], a[12:15], v[172:175]\n\t" \
  "v_mfma_f32_16x16x32_bf16 v[176:179], v[188:191], a[16:19], v[176:179]\n\t" \
  "KA5_0_%=:\n\t" \
  "s_cmp_lg_u32 %12, 1\n\t" \
  "s_cbranch_scc1 KA5_1_%=\n\t" \
  "v_mfma_f32_16x16x32_bf16 v[160:163], v[188:191], a[20:23], v[160:163]\n\t" \
  "v_mfma_f32_16x16x32_bf16 v[164:167], v[188:191], a[24:27], v[164:167]\n\t" \
  "v_mfma_f32_16x16x32_bf16 v[168:171], v[188:191], a[28:31], v[168:171]\n\t" \
  "v_mfma_f32_16x16x32_bf16 v[172:175], v[188:191], a[32:35], v[172:175]\n\t" \
  "v_mfma_f32_16x16x32_bf16 v[176:179], v[188:191], a[36:39], v[176:179]\n\t" \
  "KA5_1_%=:\n\t" \
  "s_cmp_lg_u32 %12, 2\n\t" \
  "s_cbranch_scc1 KA5_2_%=\n\t" \
  "v_mfma_f32_16x16x32_bf16 v[160:163], v[188:191], a[40:43], v[160:163]\n\t" \
  "v_mfma_f32_16x16x32_bf16 v[164:167], v[188:191], a[44:47], v[164:167]\n\t" \
  "v_mfma_f32_16x16x32_bf16 v[168:171], v[188:191], a[48:51], v[168:171]\n\t" \
  "v_mfma_f32_16x16x32_bf16 v[172:175], v[188:191], a[52:55], v[172:175]\n\t" \
  "v_mfma_f32_16x16x32_bf16 v[176:179], v[188:191], a[56:59], v[176:179]\n\t" \
  "KA5_2_%=:\n\t" \
  "s_cmp_lg_u32 %12, 3\n\t" \
  "s_cbranch_scc1 KA5_3_%=\n\t" \
  "v_mfma_f32_16x16x32_bf16 v[160:163], v[188:191], a[60:63], v[160:163]\n\t" \
  "v_mfma_f32_16x16x32_bf16 v[164:167], v[188:191], a[64:67], v[164:167]\n\t" \
  "v_mfma_f32_16x16x32_bf16 v[168:171], v[188:191], a[68:71], v[168:171]\n\t" \
  "v_mfma_f32_16x16x32_bf16 v[172:175], v[188:191], a[72:75], v[172:175]\n\t" \
  "v_mfma_f32_16x16x32_bf16 v[176:179], v[188:191], a[76:79], v[176:179]\n\t" \
  "KA5_3_%=:\n\t" \
  "s_waitcnt vmcnt(10) lgkmcnt(0)\n\ts_barrier\n\t" \
  "v_mfma_f32_32x32x16_bf16 %0, a[80:83], v[144:147], %0\n\t" \
  "ds_read_b128 v[128:131], v202\n\t" \
  "ds_read_b64_tr_b16 a[0:1], %15 offset:0\n\t" \
  "v_mfma_f32_32x32x16_bf16 %1, a[84:87], v[144:147], %1\n\t" \
  "ds_read_b64_tr_b16 a[2:3], %15 offset:512\n\t" \
  "ds_read_b64_tr_b16 a[4:5], %16 offset:0\n\t" \
  "ds_read_b64_tr_b16 a[6:7], %16 offset:512\n\t" \
  "s_add_u32 s40, %10, 1024\n\t" \
  "s_add_u32 m0, %8, 81920\n\t" \
  "s_nop 0\n\t" \
  "buffer_load_dwordx4 v214, %6, s40 offen lds\n\t" \
  "v_mfma_f32_32x32x16_bf16 %2, a[88:91], v[144:147], %2\n\t" \
  "v_mfma_f32_16x16x32_bf16 v[236:239], v[188:191], v[144:147], v[236:239]\n\t" \
  "ds_read_b64_tr_b16 a[8:9], %17 offset:0\n\t" \
  "ds_read_b64_tr_b16 a[10:11], %17 offset:512\n\t" \
  "ds_read_b64_tr_b16 a[12:13], %18 offset:0\n\t" \
  "v_mfma_f32_32x32x16_bf16 %3, v[144:147], a[92:95], %3\n\t" \
  "ds_read_b64_tr_b16 a[14:15], %18 offset:512\n\t" \
  "ds_read_b64_tr_b16 a[16:17], %19 offset:0\n\t" \
  "ds_read_b64_tr_b16 a[18:19], %19 offset:512\n\t" \
  "buffer_load_dwordx4 v215, %6, s40 offen offset:1024 lds\n\t" \
  "v_mfma_f32_32x32x16_bf16 %4, v[144:147], a[96:99], %4\n\t" \
  "v_mfma_f32_32x32x16_bf16 %5, v[144:147], v[144:147], %5\n\t" \
  "ds_read_b128 v[132:135], v203\n\t" \
  "ds_read_b64_tr_b16 a[20:21], %15 offset:2048\n\t" \
  "v_mfma_f32_32x32x16_bf16 %0, a[100:103], v[148:151], %0\n\t" \
  "ds_read_b64_tr_b16 a[22:23], %15 offset:2560\n\t" \
  "ds_read_b64_tr_b16 a[24:25], %16 offset:2048\n\t" \
  "ds_read_b64_tr_b16 a[26:27], %16 offset:2560\n\t" \
  "buffer_load_dwordx4 v216, %6, s40 offen offset:2048 lds\n\t" \
  "v_mfma_f32_32x32x16_bf16 %1, a[104:107], v[148:151], %1\n\t" \
  "ds_read_b64_tr_b16 a[28:29], %17 offset:2048\n\t" \
  "ds_read_b64_tr_b16 a[30:31], %17 offset:2560\n\t" \
  "ds_read_b64_tr_b16 a[32:33], %18 offset:2048\n\t" \
  "v_mfma_f32_32x32x16_bf16 %2, a[108:111], v[148:151], %2\n\t" \
  "v_mfma_f32_16x16x32_bf16 v[236:239], v[188:191], v[148:151], v[236:239]\n\t" \
  "ds_read_b64_tr_b16 a[34:35], %18 offset:2560\n\t" \
  "ds_read_b64_tr_b16 a[36:37], %19 offset:2048\n\t" \
  "ds_read_b64_tr_b16 a[38:39], %19 offset:2560\n\t" \
  "buffer_load_dwordx4 v217, %6, s40 offen offset:3072 lds\n\t" \
  "v_mfma_f32_32x32x16_bf16 %3, v[148:151], a[112:115], %3\n\t" \
  "ds_read_b128 v[136:139], v204\n\t" \
  "ds_read_b64_tr_b16 a[40:41], %15 offset:4096\n\t" \
  "v_mfma_f32_32x32x16_bf16 %4, v[148:151], a[116:119], %4\n\t" \
  "v_mfma_f32_32x32x16_bf16 %5, v[148:151], v[148:151], %5\n\t" \
  "ds_read_b64_tr_b16 a[42:43], %15 offset:4608\n\t" \
  "ds_read_b64_tr_b16 a[44:45], %16 offset:4096\n\t" \
  "ds_read_b64_tr_b16 a[46:47], %16 offset:4608\n\t" \
  "s_add_u32 s40, %11, 1769472\n\t" \
  "s_add_u32 m0, %9, 81920\n\t" \
  "s_nop 0\n\t" \
  "buffer_load_dwordx4 %23, %7, s40 offen lds\n\t" \
  "v_mfma_f32_32x32x16_bf16 %0, a[120:123], v[152:155], %0\n\t" \
  "ds_read_b64_tr_b16 a[48:49], %17 offset:4096\n\t" \
  "ds_read_b64_tr_b16 a[50:51], %17 offset:4608\n\t" \
  "ds_read_b64_tr_b16 a[52:53], %18 offset:4096\n\t" \
  "v_mfma_f32_32x32x16_bf16 %1, a[124:127], v[152:155], %1\n\t" \
  "ds_read_b64_tr_b16 a[54:55], %18 offset:4608\n\t" \
  "ds_read_b64_tr_b16 a[56:57], %19 offset:4096\n\t" \
  "ds_read_b64_tr_b16 a[58:59], %19 offset:4608\n\t" \
  "s_add_u32 m0, %9, 82944\n\t" \
  "s_nop 0\n\t" \
  "buffer_load_dwordx4 %24, %7, s40 offen lds\n\t" \
  "v_mfma_f32_32x32x16_bf16 %2, a[128:131], v[152:155], %2\n\t" \
  "v_mfma_f32_16x16x32_bf16 v[236:239], v[188:191], v[152:155], v[236:239]\n\t" \
  "ds_read_b128 v[140:143], v205\n\t" \
  "ds_read_b64_tr_b16 a[60:61], %15 offset:6144\n\t" \
  "v_mfma_f32_32x32x16_bf16 %3, v[152:155], a[132:135], %3\n\t" \
  "ds_read_b64_tr_b16 a[62:63], %15 offset:6656\n\t" \
  "ds_read_b64_tr_b16 a[64:65], %16 offset:6144\n\t" \
  "ds_read_b64_tr_b16 a[66:67], %16 offset:6656\n\t" \
  "s_add_u32 m0, %9, 83968\n\t" \
  "s_nop 0\n\t" \
  "buffer_load_dwordx4 %25, %7, s40 offen lds\n\t" \
  "v_mfma_f32_32x32x16_bf16 %4, v[152:155], a[136:139], %4\n\t" \
  "v_mfma_f32_32x32x16_bf16 %5, v[152:155], v[152:155], %5\n\t" \
  "ds_read_b64_tr_b16 a[68:69], %17 offset:6144\n\t" \
  "ds_read_b64_tr_b16 a[70:71], %17 offset:6656\n\t" \
  "ds_read_b64_tr_b16 a[72:73], %18 offset:6144\n\t" \
  "v_mfma_f32_32x32x16_bf16 %0, a[140:143], v[156:159], %0\n\t" \
  "ds_read_b64_tr_b16 a[74:75], %18 offset:6656\n\t" \
  "ds_read_b64_tr_b16 a[76:77], %19 offset:6144\n\t" \
  "ds_read_b64_tr_b16 a[78:79], %19 offset:6656\n\t" \
  "s_add_u32 m0, %9, 84992\n\t" \
  "s_nop 0\n\t" \
  "buffer_load_dwordx4 %26, %7, s40 offen lds\n\t" \
  "v_mfma_f32_32x32x16_bf16 %1, a[144:147], v[156:159], %1\n\t" \
  "v_mfma_f32_32x32x16_bf16 %2, a[148:151], v[156:159], %2\n\t" \
  "v_mfma_f32_16x16x32_bf16 v[236:239], v[188:191], v[156:159], v[236:239]\n\t" \
  "s_add_u32 m0, %9, 86016\n\t" \
  "s_nop 0\n\t" \
  "buffer_load_dwordx4 %27, %7, s40 offen lds\n\t" \
  "v_mfma_f32_32x32x16_bf16 %3, v[156:159], a[152:155], %3\n\t" \
  "v_mfma_f32_32x32x16_bf16 %4, v[156:159], a[156:159], %4\n\t" \
  "v_mfma_f32_32x32x16_bf16 %5, v[156:159], v[156:159], %5\n\t" \
  "s_add_u32 m0, %9, 87040\n\t" \
  "s_nop 0\n\t" \
  "buffer_load_dwordx4 %28, %7, s40 offen lds\n\t" \
  "s_cmp_lg_u32 %12, 0\n\t" \
  "s_cbranch_scc1 KA6_0_%=\n\t" \
  "v_mfma_f32_16x16x32_bf16 v[160:163], v[188:191], a[80:83], v[160:163]\n\t" \
  "v_mfma_f32_16x16x32_bf16 v[164:167], v[188:191], a[84:87], v[164:167]\n\t" \
  "v_mfma_f32_16x16x32_bf16 v[168:171], v[188:191], a[88:91], v[168:171]\n\t" \
  "v_mfma_f32_16x16x32_bf16 v[172:175], v[188:191], a[92:95], v[172:175]\n\t" \
  "v_mfma_f32_16x16x32_bf16 v[176:179], v[188:191], a[96:99], v[176:179]\n\t" \
  "KA6_0_%=:\n\t" \
  "s_cmp_lg_u32 %12, 1\n\t" \
  "s_cbranch_scc1 KA6_1_%=\n\t" \
  "v_mfma_f32_16x16x32_bf16 v[160:163], v[188:191], a[100:103], v[160:163]\n\t" \
  "v_mfma_f32_16x16x32_bf16 v[164:167], v[188:191], a[104:107], v[164:167]\n\t" \
  "v_mfma_f32_16x16x32_bf16 v[168:171], v[188:191], a[108:111], v[168:171]\n\t" \
  "v_mfma_f32_16x16x32_bf16 v[172:175], v[188:191], a[112:115], v[172:175]\n\t" \
  "v_mfma_f32_16x16x32_bf16 v[176:179], v[188:191], a[116:119], v[176:179]\n\t" \
  "KA6_1_%=:\n\t" \
  "s_cmp_lg_u32 %12, 2\n\t" \
  "s_cbranch_scc1 KA6_2_%=\n\t" \
  "v_mfma_f32_16x16x32_bf16 v[160:163], v[188:191], a[120:123], v[160:163]\n\t" \
  "v_mfma_f32_16x16x32_bf16 v[164:167], v[188:191], a[124:127], v[164:167]\n\t" \
  "v_mfma_f32_16x16x32_bf16 v[168:171], v[188:191], a[128:131], v[168:171]\n\t" \
  "v_mfma_f32_16x16x32_bf16 v[172:175], v[188:191], a[132:135], v[172:175]\n\t" \
  "v_mfma_f32_16x16x32_bf16 v[176:179], v[188:191], a[136:139], v[176:179]\n\t" \
  "KA6_2_%=:\n\t" \
  "s_cmp_lg_u32 %12, 3\n\t" \
  "s_cbranch_scc1 KA6_3_%=\n\t" \
  "v_mfma_f32_16x16x32_bf16 v[160:163], v[188:191], a[140:143], v[160:163]\n\t" \
  "v_mfma_f32_16x16x32_bf16 v[164:167], v[188:191], a[144:147], v[164:167]\n\t" \
  "v_mfma_f32_16x16x32_bf16 v[168:171], v[188:191], a[148:151], v[168:171]\n\t" \
  "v_mfma_f32_16x16x32_bf16 v[172:175], v[188:191], a[152:155], v[172:175]\n\t" \
  "v_mfma_f32_16x16x32_bf16 v[176:179], v[188:191], a[156:159], v[176:179]\n\t" \
  "KA6_3_%=:\n\t" \
  "s_waitcnt vmcnt(10) lgkmcnt(0)\n\ts_barrier\n\t" \
  "v_mfma_f32_32x32x16_bf16 %0, a[0:3], v[128:131], %0\n\t" \
  "ds_read_b128 v[144:147], v206\n\t" \
  "ds_read_b64_tr_b16 a[80:81], v192 offset:0\n\t" \
  "v_mfma_f32_32x32x16_bf16 %1, a[4:7], v[128:131], %1\n\t" \
  "ds_read_b64_tr_b16 a[82:83], v192 offset:512\n\t" \
  "ds_read_b64_tr_b16 a[84:85], v193 offset:0\n\t" \
  "ds_read_b64_tr_b16 a[86:87], v193 offset:512\n\t" \
  "v_mfma_f32_32x32x16_bf16 %2, a[8:11], v[128:131], %2\n\t" \
  "v_mfma_f32_16x16x32_bf16 v[236:239], v[188:191], v[128:131], v[236:239]\n\t" \
  "ds_read_b64_tr_b16 a[88:89], v194 offset:0\n\t" \
  "ds_read_b64_tr_b16 a[90:91], v194 offset:512\n\t" \
  "ds_read_b64_tr_b16 a[92:93], v195 offset:0\n\t" \
  "v_mfma_f32_32x32x16_bf16 %3, v[128:131], a[12:15], %3\n\t" \
  "ds_read_b64_tr_b16 a[94:95], v195 offset:512\n\t" \
  "ds_read_b64_tr_b16 a[96:97], v196 offset:0\n\t" \
  "ds_read_b64_tr_b16 a[98:99], v196 offset:512\n\t" \
  "v_mfma_f32_32x32x16_bf16 %4, v[128:131], a[16:19], %4\n\t" \
  "v_mfma_f32_32x32x16_bf16 %5, v[128:131], v[128:131], %5\n\t" \
  "ds_read_b128 v[148:151], v207\n\t" \
  "ds_read_b64_tr_b16 a[100:101], v192 offset:2048\n\t" \
  "v_mfma_f32_32x32x16_bf16 %0, a[20:23], v[132:135], %0\n\t" \
  "ds_read_b64_tr_b16 a[102:103], v192 offset:2560\n\t" \
  "ds_read_b64_tr_b16 a[104:105], v193 offset:2048\n\t" \
  "ds_read_b64_tr_b16 a[106:107], v193 offset:2560\n\t" \
  "v_mfma_f32_32x32x16_bf16 %1, a[24:27], v[132:135], %1\n\t" \
  "ds_read_b64_tr_b16 a[108:109], v194 offset:2048\n\t" \
  "ds_read_b64_tr_b16 a[110:111], v194 offset:2560\n\t" \
  "ds_read_b64_tr_b16 a[112:113], v195 offset:2048\n\t" \
  "v_mfma_f32_32x32x16_bf16 %2, a[28:31], v[132:135], %2\n\t" \
  "v_mfma_f32_16x16x32_bf16 v[236:239], v[188:191], v[132:135], v[236:239]\n\t" \
  "ds_read_b64_tr_b16 a[114:115], v195 offset:2560\n\t" \
  "ds_read_b64_tr_b16 a[116:117], v196 offset:2048\n\t" \
  "ds_read_b64_tr_b16 a[118:119], v196 offset:2560\n\t" \
  "v_mfma_f32_32x32x16_bf16 %3, v[132:135], a[32:35], %3\n\t" \
  "ds_read_b128 v[152:155], v208\n\t" \
  "ds_read_b64_tr_b16 a[120:121], v192 offset:4096\n\t" \
  "v_mfma_f32_32x32x16_bf16 %4, v[132:135], a[36:39], %4\n\t" \
  "v_mfma_f32_32x32x16_bf16 %5, v[132:135], v[132:135], %5\n\t" \
  "ds_read_b64_tr_b16 a[122:123], v192 offset:4608\n\t" \
  "ds_read_b64_tr_b16 a[124:125], v193 offset:4096\n\t" \
  "ds_read_b64_tr_b16 a[126:127], v193 offset:4608\n\t" \
  "v_mfma_f32_32x32x16_bf16 %0, a[40:43], v[136:139], %0\n\t" \
  "ds_read_b64_tr_b16 a[128:129], v194 offset:4096\n\t" \
  "ds_read_b64_tr_b16 a[130:131], v194 offset:4608\n\t" \
  "ds_read_b64_tr_b16 a[132:133], v195 offset:4096\n\t" \
  "v_mfma_f32_32x32x16_bf16 %1, a[44:47], v[136:139], %1\n\t" \
  "ds_read_b64_tr_b16 a[134:135], v195 offset:4608\n\t" \
  "ds_read_b64_tr_b16 a[136:137], v196 offset:4096\n\t" \
  "ds_read_b64_tr_b16 a[138:139], v196 offset:4608\n\t" \
  "v_mfma_f32_32x32x16_bf16 %2, a[48:51], v[136:139], %2\n\t" \
  "v_mfma_f32_16x16x32_bf16 v[236:239], v[188:191], v[136:139], v[236:239]\n\t" \
  "ds_read_b128 v[156:159], v209\n\t" \
  "ds_read_b64_tr_b16 a[140:141], v192 offset:6144\n\t" \
  "v_mfma_f32_32x32x16_bf16 %3, v[136:139], a[52:55], %3\n\t" \
  "ds_read_b64_tr_b16 a[142:143], v192 offset:6656\n\t" \
  "ds_read_b64_tr_b16 a[144:145], v193 offset:6144\n\t" \
  "ds_read_b64_tr_b16 a[146:147], v193 offset:6656\n\t" \
  "v_mfma_f32_32x32x16_bf16 %4, v[136:139], a[56:59], %4\n\t" \
  "v_mfma_f32_32x32x16_bf16 %5, v[136:139], v[136:139], %5\n\t" \
  "ds_read_b64_tr_b16 a[148:149], v194 offset:6144\n\t" \
  "ds_read_b64_tr_b16 a[150:151], v194 offset:6656\n\t" \
  "ds_read_b64_tr_b16 a[152:153], v195 offset:6144\n\t" \
  "v_mfma_f32_32x32x16_bf16 %0, a[60:63], v[140:143], %0\n\t" \
  "ds_read_b64_tr_b16 a[154:155], v195 offset:6656\n\t" \
  "ds_read_b64_tr_b16 a[156:157], v196 offset:6144\n\t" \
  "ds_read_b64_tr_b16 a[158:159], v196 offset:6656\n\t" \
  "v_mfma_f32_32x32x16_bf16 %1, a[64:67], v[140:143], %1\n\t" \
  "v_mfma_f32_32x32x16_bf16 %2, a[68:71], v[140:143], %2\n\t" \
  "v_mfma_f32_16x16x32_bf16 v[236:239], v[188:191], v[140:143], v[236:239]\n\t" \
  "v_mfma_f32_32x32x16_bf16 %3, v[140:143], a[72:75], %3\n\t" \
  "v_mfma_f32_32x32x16_bf16 %4, v[140:143], a[76:79], %4\n\t" \
  "v_mfma_f32_32x32x16_bf16 %5, v[140:143], v[140:143], %5\n\t" \
  "s_cmp_lg_u32 %12, 0\n\t" \
  "s_cbranch_scc1 KA7_0_%=\n\t" \
  "v_mfma_f32_16x16x32_bf16 v[160:163], v[188:191], a[0:3], v[160:163]\n\t" \
  "v_mfma_f32_16x16x32_bf16 v[164:167], v[188:191], a[4:7], v[164:167]\n\t" \
  "v_mfma_f32_16x16x32_bf16 v[168:171], v[188:191], a[8:11], v[168:171]\n\t" \
  "v_mfma_f32_16x16x32_bf16 v[172:175], v[188:191], a[12:15], v[172:175]\n\t" \
  "v_mfma_f32_16x16x32_bf16 v[176:179], v[188:191], a[16:19], v[176:179]\n\t" \
  "KA7_0_%=:\n\t" \
  "s_cmp_lg_u32 %12, 1\n\t" \
  "s_cbranch_scc1 KA7_1_%=\n\t" \
  "v_mfma_f32_16x16x32_bf16 v[160:163], v[188:191], a[20:23], v[160:163]\n\t" \
  "v_mfma_f32_16x16x32_bf16 v[164:167], v[188:191], a[24:27], v[164:167]\n\t" \
  "v_mfma_f32_16x16x32_bf16 v[168:171], v[188:191], a[28:31], v[168:171]\n\t" \
  "v_mfma_f32_16x16x32_bf16 v[172:175], v[188:191], a[32:35], v[172:175]\n\t" \
  "v_mfma_f32_16x16x32_bf16 v[176:179], v[188:191], a[36:39], v[176:179]\n\t" \
  "KA7_1_%=:\n\t" \
  "s_cmp_lg_u32 %12, 2\n\t" \
  "s_cbranch_scc1 KA7_2_%=\n\t" \
  "v_mfma_f32_16x16x32_bf16 v[160:163], v[188:191], a[40:43], v[160:163]\n\t" \
  "v_mfma_f32_16x16x32_bf16 v[164:167], v[188:191], a[44:47], v[164:167]\n\t" \
  "v_mfma_f32_16x16x32_bf16 v[168:171], v[188:191], a[48:51], v[168:171]\n\t" \
  "v_mfma_f32_16x16x32_bf16 v[172:175], v[188:191], a[52:55], v[172:175]\n\t" \
  "v_mfma_f32_16x16x32_bf16 v[176:179], v[188:191], a[56:59], v[176:179]\n\t" \
  "KA7_2_%=:\n\t" \
  "s_cmp_lg_u32 %12, 3\n\t" \
  "s_cbranch_scc1 KA7_3_%=\n\t" \
  "v_mfma_f32_16x16x32_bf16 v[160:163], v[188:191], a[60:63], v[160:163]\n\t" \
  "v_mfma_f32_16x16x32_bf16 v[164:167], v[188:191], a[64:67], v[164:167]\n\t" \
  "v_mfma_f32_16x16x32_bf16 v[168:171], v[188:191], a[68:71], v[168:171]\n\t" \
  "v_mfma_f32_16x16x32_bf16 v[172:175], v[188:191], a[72:75], v[172:175]\n\t" \
  "v_mfma_f32_16x16x32_bf16 v[176:179], v[188:191], a[76:79], v[176:179]\n\t" \
  "KA7_3_%=:\n\t" \
  "s_waitcnt vmcnt(0) lgkmcnt(0)\n\ts_barrier\n\t" \
  "v_mfma_f32_32x32x16_bf16 %0, a[80:83], v[144:147], %0\n\t" \
  "ds_read_b128 v[128:131], v210\n\t" \
  "ds_read_b64_tr_b16 a[0:1], v197 offset:0\n\t" \
  "v_mfma_f32_32x32x16_bf16 %1, a[84:87], v[144:147], %1\n\t" \
  "ds_read_b64_tr_b16 a[2:3], v197 offset:512\n\t" \
  "ds_read_b64_tr_b16 a[4:5], v198 offset:0\n\t" \
  "ds_read_b64_tr_b16 a[6:7], v198 offset:512\n\t" \
  "v_mfma_f32_32x32x16_bf16 %2, a[88:91], v[144:147], %2\n\t" \
  "v_mfma_f32_16x16x32_bf16 v[236:239], v[188:191], v[144:147], v[236:239]\n\t" \
  "ds_read_b64_tr_b16 a[8:9], v199 offset:0\n\t" \
  "ds_read_b64_tr_b16 a[10:11], v199 offset:512\n\t" \
  "ds_read_b64_tr_b16 a[12:13], v200 offset:0\n\t" \
  "v_mfma_f32_32x32x16_bf16 %3, v[144:147], a[92:95], %3\n\t" \
  "ds_read_b64_tr_b16 a[14:15], v200 offset:512\n\t" \
  "ds_read_b64_tr_b16 a[16:17], v201 offset:0\n\t" \
  "ds_read_b64_tr_b16 a[18:19], v201 offset:512\n\t" \
  "v_mfma_f32_32x32x16_bf16 %4, v[144:147], a[96:99], %4\n\t" \
  "v_mfma_f32_32x32x16_bf16 %5, v[144:147], v[144:147], %5\n\t" \
  "ds_read_b128 v[132:135], v211\n\t" \
  "ds_read_b64_tr_b16 a[20:21], v197 offset:2048\n\t" \
  "v_mfma_f32_32x32x16_bf16 %0, a[100:103], v[148:151], %0\n\t" \
  "ds_read_b64_tr_b16 a[22:23], v197 offset:2560\n\t" \
  "ds_read_b64_tr_b16 a[24:25], v198 offset:2048\n\t" \
  "ds_read_b64_tr_b16 a[26:27], v198 offset:2560\n\t" \
  "v_mfma_f32_32x32x16_bf16 %1, a[104:107], v[148:151], %1\n\t" \
  "ds_read_b64_tr_b16 a[28:29], v199 offset:2048\n\t" \
  "ds_read_b64_tr_b16 a[30:31], v199 offset:2560\n\t" \
  "ds_read_b64_tr_b16 a[32:33], v200 offset:2048\n\t" \
  "v_mfma_f32_32x32x16_bf16 %2, a[108:111], v[148:151], %2\n\t" \
  "v_mfma_f32_16x16x32_bf16 v[236:239], v[188:191], v[148:151], v[236:239]\n\t" \
  "ds_read_b64_tr_b16 a[34:35], v200 offset:2560\n\t" \
  "ds_read_b64_tr_b16 a[36:37], v201 offset:2048\n\t" \
  "ds_read_b64_tr_b16 a[38:39], v201 offset:2560\n\t" \
  "v_mfma_f32_32x32x16_bf16 %3, v[148:151], a[112:115], %3\n\t" \
  "ds_read_b128 v[136:139], v212\n\t" \
  "ds_read_b64_tr_b16 a[40:41], v197 offset:4096\n\t" \
  "v_mfma_f32_32x32x16_bf16 %4, v[148:151], a[116:119], %4\n\t" \
  "v_mfma_f32_32x32x16_bf16 %5, v[148:151], v[148:151], %5\n\t" \
  "ds_read_b64_tr_b16 a[42:43], v197 offset:4608\n\t" \
  "ds_read_b64_tr_b16 a[44:45], v198 offset:4096\n\t" \
  "ds_read_b64_tr_b16 a[46:47], v198 offset:4608\n\t" \
  "v_mfma_f32_32x32x16_bf16 %0, a[120:123], v[152:155], %0\n\t" \
  "ds_read_b64_tr_b16 a[48:49], v199 offset:4096\n\t" \
  "ds_read_b64_tr_b16 a[50:51], v199 offset:4608\n\t" \
  "ds_read_b64_tr_b16 a[52:53], v200 offset:4096\n\t" \
  "v_mfma_f32_32x32x16_bf16 %1, a[124:127], v[152:155], %1\n\t" \
  "ds_read_b64_tr_b16 a[54:55], v200 offset:4608\n\t" \
  "ds_read_b64_tr_b16 a[56:57], v201 offset:4096\n\t" \
  "ds_read_b64_tr_b16 a[58:59], v201 offset:4608\n\t" \
  "v_mfma_f32_32x32x16_bf16 %2, a[128:131], v[152:155], %2\n\t" \
  "v_mfma_f32_16x16x32_bf16 v[236:239], v[188:191], v[152:155], v[236:239]\n\t" \
  "ds_read_b128 v[140:143], v213\n\t" \
  "ds_read_b64_tr_b16 a[60:61], v197 offset:6144\n\t" \
  "v_mfma_f32_32x32x16_bf16 %3, v[152:155], a[132:135], %3\n\t" \
  "ds_read_b64_tr_b16 a[62:63], v197 offset:6656\n\t" \
  "ds_read_b64_tr_b16 a[64:65], v198 offset:6144\n\t" \
  "ds_read_b64_tr_b16 a[66:67], v198 offset:6656\n\t" \
  "v_mfma_f32_32x32x16_bf16 %4, v[152:155], a[136:139], %4\n\t" \
  "v_mfma_f32_32x32x16_bf16 %5, v[152:155], v[152:155], %5\n\t" \
  "ds_read_b64_tr_b16 a[68:69], v199 offset:6144\n\t" \
  "ds_read_b64_tr_b16 a[70:71], v199 offset:6656\n\t" \
  "ds_read_b64_tr_b16 a[72:73], v200 offset:6144\n\t" \
  "v_mfma_f32_32x32x16_bf16 %0, a[140:143], v[156:159], %0\n\t" \
  "ds_read_b64_tr_b16 a[74:75], v200 offset:6656\n\t" \
  "ds_read_b64_tr_b16 a[76:77], v201 offset:6144\n\t" \
  "ds_read_b64_tr_b16 a[78:79], v201 offset:6656\n\t" \
  "v_mfma_f32_32x32x16_bf16 %1, a[144:147], v[156:159], %1\n\t" \
  "v_mfma_f32_32x32x16_bf16 %2, a[148:151], v[156:159], %2\n\t" \
  "v_mfma_f32_16x16x32_bf16 v[236:239], v[188:191], v[156:159], v[236:239]\n\t" \
  "v_mfma_f32_32x32x16_bf16 %3, v[156:159], a[152:155], %3\n\t" \
  "v_mfma_f32_32x32x16_bf16 %4, v[156:159], a[156:159], %4\n\t" \
  "v_mfma_f32_32x32x16_bf16 %5, v[156:159], v[156:159], %5\n\t" \
  "s_cmp_lg_u32 %12, 0\n\t" \
  "s_cbranch_scc1 KA8_0_%=\n\t" \
  "v_mfma_f32_16x16x32_bf16 v[160:163], v[188:191], a[80:83], v[160:163]\n\t" \
  "v_mfma_f32_16x16x32_bf16 v[164:167], v[188:191], a[84:87], v[164:167]\n\t" \
  "v_mfma_f32_16x16x32_bf16 v[168:171], v[188:191], a[88:91], v[168:171]\n\t" \
  "v_mfma_f32_16x16x32_bf16 v[172:175], v[188:191], a[92:95], v[172:175]\n\t" \
  "v_mfma_f32_16x16x32_bf16 v[176:179], v[188:191], a[96:99], v[176:179]\n\t" \
  "KA8_0_%=:\n\t" \
  "s_cmp_lg_u32 %12, 1\n\t" \
  "s_cbranch_scc1 KA8_1_%=\n\t" \
  "v_mfma_f32_16x16x32_bf16 v[160:163], v[188:191], a[100:103], v[160:163]\n\t" \
  "v_mfma_f32_16x16x32_bf16 v[164:167], v[188:191], a[104:107], v[164:167]\n\t" \
  "v_mfma_f32_16x16x32_bf16 v[168:171], v[188:191], a[108:111], v[168:171]\n\t" \
  "v_mfma_f32_16x16x32_bf16 v[172:175], v[188:191], a[112:115], v[172:175]\n\t" \
  "v_mfma_f32_16x16x32_bf16 v[176:179], v[188:191], a[116:119], v[176:179]\n\t" \
  "KA8_1_%=:\n\t" \
  "s_cmp_lg_u32 %12, 2\n\t" \
  "s_cbranch_scc1 KA8_2_%=\n\t" \
  "v_mfma_f32_16x16x32_bf16 v[160:163], v[188:191], a[120:123], v[160:163]\n\t" \
  "v_mfma_f32_16x16x32_bf16 v[164:167], v[188:191], a[124:127], v[164:167]\n\t" \
  "v_mfma_f32_16x16x32_bf16 v[168:171], v[188:191], a[128:131], v[168:171]\n\t" \
  "v_mfma_f32_16x16x32_bf16 v[172:175], v[188:191], a[132:135], v[172:175]\n\t" \
  "v_mfma_f32_16x16x32_bf16 v[176:179], v[188:191], a[136:139], v[176:179]\n\t" \
  "KA8_2_%=:\n\t" \
  "s_cmp_lg_u32 %12, 3\n\t" \
  "s_cbranch_scc1 KA8_3_%=\n\t" \
  "v_mfma_f32_16x16x32_bf16 v[160:163], v[188:191], a[140:143], v[160:163]\n\t" \
  "v_mfma_f32_16x16x32_bf16 v[164:167], v[188:191], a[144:147], v[164:167]\n\t" \
  "v_mfma_f32_16x16x32_bf16 v[168:171], v[188:191], a[148:151], v[168:171]\n\t" \
  "v_mfma_f32_16x16x32_bf16 v[172:175], v[188:191], a[152:155], v[172:175]\n\t" \
  "v_mfma_f32_16x16x32_bf16 v[176:179], v[188:191], a[156:159], v[176:179]\n\t" \
  "KA8_3_%=:\n\t" \
  "s_waitcnt lgkmcnt(0)\n\t" \
  "v_mfma_f32_32x32x16_bf16 %0, a[0:3], v[128:131], %0\n\t" \
  "v_mfma_f32_32x32x16_bf16 %1, a[4:7], v[128:131], %1\n\t" \
  "v_mfma_f32_32x32x16_bf16 %2, a[8:11], v[128:131], %2\n\t" \
  "v_mfma_f32_16x16x32_bf16 v[236:239], v[188:191], v[128:131], v[236:239]\n\t" \
  "v_mfma_f32_32x32x16_bf16 %3, v[128:131], a[12:15], %3\n\t" \
  "v_mfma_f32_32x32x16_bf16 %4, v[128:131], a[16:19], %4\n\t" \
  "v_mfma_f32_32x32x16_bf16 %5, v[128:131], v[128:131], %5\n\t" \
  "v_mfma_f32_32x32x16_bf16 %0, a[20:23], v[132:135], %0\n\t" \
  "v_mfma_f32_32x32x16_bf16 %1, a[24:27], v[132:135], %1\n\t" \
  "v_mfma_f32_32x32x16_bf16 %2, a[28:31], v[132:135], %2\n\t" \
  "v_mfma_f32_16x16x32_bf16 v[236:239], v[188:191], v[132:135], v[236:239]\n\t" \
  "v_mfma_f32_32x32x16_bf16 %3, v[132:135], a[32:35], %3\n\t" \
  "v_mfma_f32_32x32x16_bf16 %4, v[132:135], a[36:39], %4\n\t" \
  "v_mfma_f32_32x32x16_bf16 %5, v[132:135], v[132:135], %5\n\t" \
  "v_mfma_f32_32x32x16_bf16 %0, a[40:43], v[136:139], %0\n\t" \
  "v_mfma_f32_32x32x16_bf16 %1, a[44:47], v[136:139], %1\n\t" \
  "v_mfma_f32_32x32x16_bf16 %2, a[48:51], v[136:139], %2\n\t" \
  "v_mfma_f32_16x16x32_bf16 v[236:239], v[188:191], v[136:139], v[236:239]\n\t" \
  "v_mfma_f32_32x32x16_bf16 %3, v[136:139], a[52:55], %3\n\t" \
  "v_mfma_f32_32x32x16_bf16 %4, v[136:139], a[56:59], %4\n\t" \
  "v_mfma_f32_32x32x16_bf16 %5, v[136:139], v[136:139], %5\n\t" \
  "v_mfma_f32_32x32x16_bf16 %0, a[60:63], v[140:143], %0\n\t" \
  "v_mfma_f32_32x32x16_bf16 %1, a[64:67], v[140:143], %1\n\t" \
  "v_mfma_f32_32x32x16_bf16 %2, a[68:71], v[140:143], %2\n\t" \
  "v_mfma_f32_16x16x32_bf16 v[236:239], v[188:191], v[140:143], v[236:239]\n\t" \
  "v_mfma_f32_32x32x16_bf16 %3, v[140:143], a[72:75], %3\n\t" \
  "v_mfma_f32_32x32x16_bf16 %4, v[140:143], a[76:79], %4\n\t" \
  "v_mfma_f32_32x32x16_bf16 %5, v[140:143], v[140:143], %5\n\t" \
  "s_cmp_lg_u32 %12, 0\n\t" \
  "s_cbranch_scc1 KA9_0_%=\n\t" \
  "v_mfma_f32_16x16x32_bf16 v[160:163], v[188:191], a[0:3], v[160:163]\n\t" \
  "v_mfma_f32_16x16x32_bf16 v[164:167], v[188:191], a[4:7], v[164:167]\n\t" \
  "v_mfma_f32_16x16x32_bf16 v[168:171], v[188:191], a[8:11], v[168:171]\n\t" \
  "v_mfma_f32_16x16x32_bf16 v[172:175], v[188:191], a[12:15], v[172:175]\n\t" \
  "v_mfma_f32_16x16x32_bf16 v[176:179], v[188:191], a[16:19], v[176:179]\n\t" \
  "KA9_0_%=:\n\t" \
  "s_cmp_lg_u32 %12, 1\n\t" \
  "s_cbranch_scc1 KA9_1_%=\n\t" \
  "v_mfma_f32_16x16x32_bf16 v[160:163], v[188:191], a[20:23], v[160:163]\n\t" \
  "v_mfma_f32_16x16x32_bf16 v[164:167], v[188:191], a[24:27], v[164:167]\n\t" \
  "v_mfma_f32_16x16x32_bf16 v[168:171], v[188:191], a[28:31], v[168:171]\n\t" \
  "v_mfma_f32_16x16x32_bf16 v[172:175], v[188:191], a[32:35], v[172:175]\n\t" \
  "v_mfma_f32_16x16x32_bf16 v[176:179], v[188:191], a[36:39], v[176:179]\n\t" \
  "KA9_1_%=:\n\t" \
  "s_cmp_lg_u32 %12, 2\n\t" \
  "s_cbranch_scc1 KA9_2_%=\n\t" \
  "v_mfma_f32_16x16x32_bf16 v[160:163], v[188:191], a[40:43], v[160:163]\n\t" \
  "v_mfma_f32_16x16x32_bf16 v[164:167], v[188:191], a[44:47], v[164:167]\n\t" \
  "v_mfma_f32_16x16x32_bf16 v[168:171], v[188:191], a[48:51], v[168:171]\n\t" \
  "v_mfma_f32_16x16x32_bf16 v[172:175], v[188:191], a[52:55], v[172:175]\n\t" \
  "v_mfma_f32_16x16x32_bf16 v[176:179], v[188:191], a[56:59], v[176:179]\n\t" \
  "KA9_2_%=:\n\t" \
  "s_cmp_lg_u32 %12, 3\n\t" \
  "s_cbranch_scc1 KA9_3_%=\n\t" \
  "v_mfma_f32_16x16x32_bf16 v[160:163], v[188:191], a[60:63], v[160:163]\n\t" \
  "v_mfma_f32_16x16x32_bf16 v[164:167], v[188:191], a[64:67], v[164:167]\n\t" \
  "v_mfma_f32_16x16x32_bf16 v[168:171], v[188:191], a[68:71], v[168:171]\n\t" \
  "v_mfma_f32_16x16x32_bf16 v[172:175], v[188:191], a[72:75], v[172:175]\n\t" \
  "v_mfma_f32_16x16x32_bf16 v[176:179], v[188:191], a[76:79], v[176:179]\n\t" \
  "KA9_3_%=:\n\t" \
  "v_and_b32 v224, 31, %29\n\t" \
  "v_lshlrev_b32 v224, 3, v224\n\t" \
  "v_add_u32 v224, %14, v224\n\t" \
  "v_lshlrev_b32 v225, 2, %29\n\t" \
  "v_add_u32 v225, %13, v225\n\t" \
  "s_mov_b64 s[42:43], exec\n\t" \
  "s_mov_b64 exec, 0xffff\n\t" \
  "ds_write_b32 v224, v236\n\t" \
  "ds_write_b32 v224, v237 offset:128\n\t" \
  "ds_write_b32 v225, v160 offset:0\n\t" \
  "ds_write_b32 v225, v161 offset:64\n\t" \
  "ds_write_b32 v225, v164 offset:128\n\t" \
  "ds_write_b32 v225, v165 offset:192\n\t" \
  "ds_write_b32 v225, v168 offset:256\n\t" \
  "ds_write_b32 v225, v169 offset:320\n\t" \
  "ds_write_b32 v225, v172 offset:384\n\t" \
  "ds_write_b32 v225, v173 offset:448\n\t" \
  "ds_write_b32 v225, v176 offset:512\n\t" \
  "ds_write_b32 v225, v177 offset:576\n\t" \
  "s_mov_b64 exec, s[42:43]\n\t" \
  "s_nop 15\n\t" \
  ""
#define P1_ASM_CLOBBERS "v128", "v129", "v130", "v131", "v132", "v133", "v134", "v135", "v136", "v137", "v138", "v139", "v140", "v141", "v142", "v143", "v144", "v145", "v146", "v147", "v148", "v149", "v150", "v151", "v152", "v153", "v154", "v155", "v156", "v157", "v158", "v159", "v160", "v161", "v162", "v163", "v164", "v165", "v166", "v167", "v168", "v169", "v170", "v171", "v172", "v173", "v174", "v175", "v176", "v177", "v178", "v179", "v180", "v181", "v182", "v183", "v184", "v185", "v186", "v187", "v188", "v189", "v190", "v191", "v192", "v193", "v194", "v195", "v196", "v197", "v198", "v199", "v200", "v201", "v202", "v203", "v204", "v205", "v206", "v207", "v208", "v209", "v210", "v211", "v212", "v213", "v214", "v215", "v216", "v217", "v218", "v219", "v220", "v221", "v222", "v223", "v224", "v225", "v226", "v227", "v228", "v229", "v230", "v231", "v232", "v233", "v234", "v235", "v236", "v237", "v238", "v239", "a0", "a1", "a2", "a3", "a4", "a5", "a6", "a7", "a8", "a9", "a10", "a11", "a12", "a13", "a14", "a15", "a16", "a17", "a18", "a19", "a20", "a21", "a22", "a23", "a24", "a25", "a26", "a27", "a28", "a29", "a30", "a31", "a32", "a33", "a34", "a35", "a36", "a37", "a38", "a39", "a40", "a41", "a42", "a43", "a44", "a45", "a46", "a47", "a48", "a49", "a50", "a51", "a52", "a53", "a54", "a55", "a56", "a57", "a58", "a59", "a60", "a61", "a62", "a63", "a64", "a65", "a66", "a67", "a68", "a69", "a70", "a71", "a72", "a73", "a74", "a75", "a76", "a77", "a78", "a79", "a80", "a81", "a82", "a83", "a84", "a85", "a86", "a87", "a88", "a89", "a90", "a91", "a92", "a93", "a94", "a95", "a96", "a97", "a98", "a99", "a100", "a101", "a102", "a103", "a104", "a105", "a106", "a107", "a108", "a109", "a110", "a111", "a112", "a113", "a114", "a115", "a116", "a117", "a118", "a119", "a120", "a121", "a122", "a123", "a124", "a125", "a126", "a127", "a128", "a129", "a130", "a131", "a132", "a133", "a134", "a135", "a136", "a137", "a138", "a139", "a140", "a141", "a142", "a143", "a144", "a145", "a146", "a147", "a148", "a149", "a150", "a151", "a152", "a153", "a154", "a155", "a156", "a157", "a158", "a159", "s40", "s41", "s42", "s43", "vcc", "scc", "memory"
constexpr size_t WS_FL=0, WS_KVP=16384, WS_O=WS_KVP+16ull*64*2304, WS_TP=WS_O+64ull*48*1024, WS_MX=WS_TP+16ull*16*512, WS_HC=WS_MX+8192,
  WS_MH=WS_HC+64ull*36*1024, WS_SSQ=WS_MH+8192, WS_P=WS_SSQ+2048ull*64, WS_DBG=WS_P+16ull*64*36*1024, WS_END=WS_DBG+1024*256;
#define GAS __attribute__((address_space(1)))
typedef GAS unsigned long long gu64;
typedef GAS u32 gu32;
constexpr u32 P1_WOFF=16384, P1_SLAB=40960;
constexpr u32 P3_ROWS=584, P3_IMG_END=P3_ROWS*192;
constexpr u32 P1_SCR=P3_IMG_END  , P1_PG=149376  , P1_PHI=P1_PG+2048  , P1_CS=P1_PHI+8192  ,
              P1_CSF=P1_CS+2560, P1_ST=P1_CSF+640  , P1_END=P1_ST+1024;
constexpr u32 P2_AF=P1_SCR  , P2_WB=P1_PG  ;
constexpr u32 ATT_LDS=163840;
static_assert(P1_SCR+4*9216<=P1_PG && P1_END<=ATT_LDS,"lds map");
constexpr u32 KVP_TILE=1280;
constexpr u32 FL_F1=256, FL_F2=FL_F1+16*256, FL_F3=FL_F2+16*256, FL_END=FL_F3+16*256;

struct Soft { float pos[9], neg[9]; };
DI void dual_softmax(const f32x16& p, Soft& o){
  float mx=p[0], mn=p[0];
  #pragma unroll
  for(int i=1;i<9;i++){ mx=fmaxf(mx,p[i]); mn=fminf(mn,p[i]); }
  mx=maxhalves(mx); mn=minhalves(mn);
  const float L=1.4426950408889634f;
  const float mxl=mx*L, mnl=mn*L;
  float sp=0.f, sn=0.f;
  #pragma unroll
  for(int i=0;i<9;i++){ o.pos[i]=__builtin_amdgcn_exp2f(p[i]*L-mxl); o.neg[i]=__builtin_amdgcn_exp2f(mnl-p[i]*L); sp+=o.pos[i]; sn+=o.neg[i]; }
  sp=addhalves(sp); sn=addhalves(sn);
  const float rp=__builtin_amdgcn_rcpf(sp), rn=__builtin_amdgcn_rcpf(sn);
  #pragma unroll
  for(int i=0;i<9;i++){ o.pos[i]*=rp; o.neg[i]*=rn; }
}
DI unsigned long long flagval(u32 e){ return ((unsigned long long)e<<32)|(unsigned long long)(e^0x9E3779B9u); }
DI void publish(char* fl, u32 off, u32 e){
  asm volatile("s_waitcnt vmcnt(0) lgkmcnt(0)\n\ts_barrier":::"memory");
  if(threadIdx.x==0) __hip_atomic_store((gu64*)(fl+off), flagval(e), __ATOMIC_RELAXED, __HIP_MEMORY_SCOPE_AGENT);
}
DI void wait16(char* fl, u32 off, u32 e){
  if(threadIdx.x<64u){
    const unsigned long long want=flagval(e); const u32 l=threadIdx.x&15u;
    const unsigned long long t0=__builtin_amdgcn_s_memrealtime();
    for(;;){
      const unsigned long long v=__hip_atomic_load((gu64*)(fl+off)+l, __ATOMIC_RELAXED, __HIP_MEMORY_SCOPE_AGENT);
      if(__all(v==want)) break;
      if(__builtin_amdgcn_s_memrealtime()-t0>4000000ull) break;
      __builtin_amdgcn_s_sleep(1);
    }
  }
  asm volatile("s_waitcnt vmcnt(0) lgkmcnt(0)\n\ts_barrier":::"memory");
}
DI u32x4 ld128sc(__amdgpu_buffer_rsrc_t r, u32 voff, u32 soff){ return __builtin_amdgcn_raw_buffer_load_b128(r,voff,soff,16); }
DI u32 ld32sc(__amdgpu_buffer_rsrc_t r, u32 voff, u32 soff){ return __builtin_amdgcn_raw_buffer_load_b32(r,voff,soff,16); }
DI void st128sc(u32x4 v, __amdgpu_buffer_rsrc_t r, u32 voff, u32 soff){ __builtin_amdgcn_raw_buffer_store_b128(v,r,voff,soff,16); }
DI void st64sc(u32x2 v, __amdgpu_buffer_rsrc_t r, u32 voff, u32 soff){ __builtin_amdgcn_raw_buffer_store_b64(v,r,voff,soff,16); }
DI void st32sc(u32 v, __amdgpu_buffer_rsrc_t r, u32 voff, u32 soff){ __builtin_amdgcn_raw_buffer_store_b32(v,r,voff,soff,16); }
DI f32x4 asf4(u32x4 v){ return __builtin_bit_cast(f32x4,v); }
DI u32x4 asu4(f32x4 v){ return __builtin_bit_cast(u32x4,v); }

struct AttnWs { char* fl; float* KVP; char* O; float* TP; float* MX; char* HC; float* MH; float* SSQ; };

DI u32 attn_phases(const u16* __restrict__ x, const u16* __restrict__ wqkv, const u16* __restrict__ wphi, const u16* __restrict__ wo,
                    const u16* __restrict__ w1, const u16* __restrict__ w3, const u16* __restrict__ w2,
                    const AttnWs& W, const u32 L, const u32 i, const u32 j, const u32 w, const u32 epv){
  u32 ep;
  STAMP_DECL STAMP(0); STAMP(0);
  const u32 tt32=4u*i+w;
  const __amdgpu_buffer_rsrc_t rx=mkrsrc(x,2048u*1152u), rw=mkrsrc(wqkv,576u*3456u);
  u32x4 bqf[2][2]; u32 bq2[2];
  const u32 ONES=0x3f803f80u;
  const f32x16 Z={};
  {
    const u32 lane=lane_id_opaque(), r=lane&31u, h=lane>>5, blk=(lane>>4)&1u, q=(lane>>2)&3u, p=lane&3u, G=4u*blk+p, l8=lane>>3;
    u32 vx0=(32u*w+l8)*1152u+(((lane&7u)^(l8>>1))*16u), vx1=(32u*w+8u+l8)*1152u+(((lane&7u)^(4u+(l8>>1)))*16u);
    u32 vw[6];
    #pragma unroll
    for(int e=0;e<6;e++){ const u32 m=6u*w+e, seg=m>>3, row=(m&7u)*8u+l8; vw[e]=row*3456u+seg*1152u+(((lane&7u)^(((l8>>1)&1u)<<2))*16u); }
    const u32 sx0=128u*i*1152u, sw0=(72u*j)&~63u, mx0=L+w*4096u, mw0=L+P1_WOFF+6u*w*1024u;
    #pragma unroll
    for(int n=0;n<2;n++){
      asm volatile("s_mov_b32 m0, %0\n\ts_nop 0\n\tbuffer_load_dwordx4 %1, %5, %6 offen lds\n\tbuffer_load_dwordx4 %2, %5, %6 offen offset:1024 lds\n\t"
                   "buffer_load_dwordx4 %3, %5, %6 offen offset:2048 lds\n\tbuffer_load_dwordx4 %4, %5, %6 offen offset:3072 lds"
                   ::"s"(mx0+n*P1_SLAB),"v"(vx0),"v"(vx1-1024u),"v"(vx0+2u*9216u-2048u),"v"(vx1+2u*9216u-3072u),"s"(rx),"s"(sx0+n*128u):"memory");
      #pragma unroll
      for(int e=0;e<6;e++) DMA16(mw0+n*P1_SLAB+e*1024u, vw[e], rw, sw0+n*64u*3456u);
    }
    {
      const __amdgpu_buffer_rsrc_t rp=mkrsrc(wphi+(size_t)j*648u,1296u);
      DMA16(L+P1_PG+(w&1u)*1024u, ((w&1u)*64u+lane)*16u, rp, 0);
    }
    STAMP(1);
    const u32 ub=j&7u;
    u32 aw[5];
    { const u32 sg[5]={0u,(G==0u)?0u:1u,1u,2u,2u};
      const u32 un[5]={ub+G,(G==0u)? ub+8u : ub+G-1u, ub+7u+(G<2u?G:1u), ub+G, ub+((4u+G)>8u?8u:(4u+G))};
      #pragma unroll
      for(int t=0;t<5;t++) aw[t]=L+P1_WOFF+sg[t]*8192u+(8u*h+q)*128u+((un[t]^(((q>>1)&1u)<<3))*8u); }
    const u32 ax0=L+(32u*w+r)*128u+((h^((r>>1)&7u))*16u);
    f32x16 acc[5], gram;
    asm volatile(P1_ASM_BODY
      : "=&v"(acc[0]),"=&v"(acc[1]),"=&v"(acc[2]),"=&v"(acc[3]),"=&v"(acc[4]),"=&v"(gram)
      : "s"(rx),"s"(rw),"s"(mx0),"s"(mw0),"s"(sx0),"s"(sw0),"s"(w),"s"(L+P1_CS+w*640u),"s"(L+P1_ST+w*256u),
        "v"(aw[0]),"v"(aw[1]),"v"(aw[2]),"v"(aw[3]),"v"(aw[4]),"v"(ax0),"v"(vx0),"v"(vx1),
        "v"(vw[0]),"v"(vw[1]),"v"(vw[2]),"v"(vw[3]),"v"(vw[4]),"v"(vw[5]),"v"(lane)
      : P1_ASM_CLOBBERS);

    STAMP(2);
    ep=rfl(epv)+1u;
   {
    const u32 lane=lane_id_opaque(), tid=w*64u+lane, r=lane&31u, h=lane>>5, blk=(lane>>4)&1u, q=(lane>>2)&3u, p=lane&3u;
    const __amdgpu_buffer_rsrc_t ro=mkrsrc(wo,576u*1152u);
    u32 vwo[3];
    #pragma unroll
    for(int e=0;e<3;e++){ const u32 idx=64u*e+lane, row=(idx*43691u)>>19; vwo[e]=row*1152u+(idx-row*12u)*16u; }
    const u32 wcb=(144u*(j&7u))&~63u;
    auto wodma=[&](int k){ const u32 P=9u*w+k;
      #pragma unroll
      for(int e=0;e<3;e++) DMA16(L+P*3072u+e*1024u, vwo[e], ro, P*16u*1152u+wcb); };
    float ssx;
    { const u32 rg=(r&3u)+4u*(r>>3); float d=0.f;
      #pragma unroll
      for(int e=0;e<16;e++) d=(rg==(u32)e)?gram[e]:d;
      d=(h==((r>>2)&1u))?d:0.f; ssx=addhalves(d); }
    BAR();
    {
      const u32 id=w*64u+lane, sc=L+P1_SCR+4u*9216u;
      const u32 row=36u*i+id/6u, vo=(id<216u)? row*9216u+((576u*j)&~127u)+(id%6u)*128u : 0x7fffff00u;
      asm volatile("s_mov_b32 m0, %0\n\ts_nop 0\n\tbuffer_load_dword %1, %2, 0 offen lds"::"s"(sc),"v"(vo),"s"(mkrsrc(w1,576u*9216u)):"memory");
      asm volatile("s_mov_b32 m0, %0\n\ts_nop 0\n\tbuffer_load_dword %1, %2, 0 offen lds"::"s"(sc),"v"(vo),"s"(mkrsrc(w3,576u*9216u)):"memory");
      const u32 vo2=(id<162u)? (288u*j+18u*i)*1152u+id*128u : 0x7fffff00u;
      asm volatile("s_mov_b32 m0, %0\n\ts_nop 0\n\tbuffer_load_dword %1, %2, 0 offen lds"::"s"(sc),"v"(vo2),"s"(mkrsrc(w2,4608u*1152u)):"memory"); }
    wodma(0);
    { const int f = r<16u? (int)r : (r==16u? 16 : (r==20u? 17 : -1));
      #pragma unroll
      for(int e=0;e<2;e++){
        const int db = (w==0)? (e?16:0) : (w==1)? (e?14:-2) : (w==2)? (e?12:-4) : (e?10:-6);
        const int d0=db+4*(int)h; u32 g8[8];
        #pragma unroll
        for(int je=0;je<8;je++){ const int d=d0+(je&3)+8*(je>>2); const bool ok=(f>=0)&&(d>=0)&&(d<18);
          const u32 v=*(LAS u16*)(size_t)(L+P1_PG+(w&1u)*648u+(u32)((ok?d:0)*18+(ok?f:0))*2u); g8[je]=ok?v:0u; }
        ldsw128(L+P1_PHI+(2u*w+e)*1024u+lane*16u, u32x4{g8[0]|(g8[1]<<16),g8[2]|(g8[3]<<16),g8[4]|(g8[5]<<16),g8[6]|(g8[7]<<16)}); } }
    if(tid<160u) ldsw32f(L+P1_CSF+tid*4u, (ldsr32f(L+P1_CS+tid*4u)+ldsr32f(L+P1_CS+640u+tid*4u))+(ldsr32f(L+P1_CS+1280u+tid*4u)+ldsr32f(L+P1_CS+1920u+tid*4u)));
    float mean, rstd;
    { mean=ldsr32f(L+P1_ST+(32u*w+r)*8u)*(1.0f/576.0f); const float var=fmaxf(ssx*(1.0f/576.0f)-mean*mean,0.f); rstd=__builtin_amdgcn_rsqf(var+1e-5f);
      *(LAS f32x2*)(size_t)(L+P1_ST+(32u*w+r)*8u)=f32x2{mean,rstd};
      if(j==0u && h==0u) st32sc(__float_as_uint(mean), mkrsrc(W.MX,8192u), (tt32*32u+r)*4u, 0); }
    wodma(1);
    BAR();
    STAMP(3);
    u32x4 bq[5];
    #pragma unroll
    for(int k=0;k<5;k++){
      float v[8];
      #pragma unroll
      for(int g=0;g<2;g++){ const f32x4 c=ldsr128f(L+P1_CSF+(16u*k+8u*g+4u*h)*4u);
        #pragma unroll
        for(int e=0;e<4;e++) v[4*g+e]=rstd*(acc[k>>1][8*(k&1)+4*g+e]-mean*c[e]); }
      bq[k]=u32x4{cvtpk(v[0],v[1]),cvtpk(v[2],v[3]),cvtpk(v[4],v[5]),cvtpk(v[6],v[7])};
    }
    wodma(2);
    const u32 pa=L+P1_PHI+lane*16u;
    const f32x16 pq0=mfma32(ldsr128(pa+1024u),bq[1],mfma32(ldsr128(pa),bq[0],Z));
    const f32x16 pq1=mfma32(ldsr128(pa+3072u),bq[2],mfma32(ldsr128(pa+2048u),bq[1],Z));
    const f32x16 pk0=mfma32(ldsr128(pa+5120u),bq[3],mfma32(ldsr128(pa+4096u),bq[2],Z));
    const f32x16 pk1=mfma32(ldsr128(pa+7168u),bq[4],mfma32(ldsr128(pa+6144u),bq[3],Z));
    wodma(3);
    #pragma unroll
    for(int hh=0;hh<2;hh++){
      Soft s; dual_softmax(hh?pk1:pk0,s);
      const u32 rowb=L+P1_SCR+w*9216u+hh*4096u+r*128u, sw=((r>>1)&1u)<<6;
      wodma(4+hh);
      ldsw64(rowb+((8u*h)^sw),     u32x2{cvtpk(s.pos[0],s.pos[1]),cvtpk(s.pos[2],s.pos[3])});
      ldsw64(rowb+((16u+8u*h)^sw), u32x2{cvtpk(s.pos[4],s.pos[5]),cvtpk(s.pos[6],s.pos[7])});
      ldsw64(rowb+((64u+8u*h)^sw), u32x2{cvtpk(s.neg[0],s.neg[1]),cvtpk(s.neg[2],s.neg[3])});
      ldsw64(rowb+((80u+8u*h)^sw), u32x2{cvtpk(s.neg[4],s.neg[5]),cvtpk(s.neg[6],s.neg[7])});
      const u32 pn=cvtpk(s.pos[8],s.neg[8]);
      ldsw16(rowb+((32u+8u*h)^sw),(u16)(pn&0xffffu)); ldsw16(rowb+((96u+8u*h)^sw),(u16)(pn>>16));
    }
    STAMP(4);
    u32x4 bv[2][2];
    { const bool one=(r==27u)||(r==31u);
      const float c0=ldsr32f(L+P1_CSF+(96u+r)*4u), c1=ldsr32f(L+P1_CSF+(128u+r)*4u);
      #pragma unroll
      for(int s=0;s<2;s++){
        float v0[8], v1[8];
        #pragma unroll
        for(int g=0;g<2;g++){
          const u32 sa=L+P1_ST+(32u*w+16u*s+8u*g+4u*h)*8u;
          const f32x4 a=ldsr128f(sa), d=ldsr128f(sa+16u);
          const float m4[4]={a[0],a[2],d[0],d[2]}, r4[4]={a[1],a[3],d[1],d[3]};
          #pragma unroll
          for(int e=0;e<4;e++){ v0[4*g+e]=r4[e]*(acc[3][8*s+4*g+e]-m4[e]*c0); v1[4*g+e]=r4[e]*(acc[4][8*s+4*g+e]-m4[e]*c1); }
        }
        bv[0][s]=one? u32x4{ONES,ONES,ONES,ONES} : u32x4{cvtpk(v0[0],v0[1]),cvtpk(v0[2],v0[3]),cvtpk(v0[4],v0[5]),cvtpk(v0[6],v0[7])};
        bv[1][s]=one? u32x4{ONES,ONES,ONES,ONES} : u32x4{cvtpk(v1[0],v1[1]),cvtpk(v1[2],v1[3]),cvtpk(v1[4],v1[5]),cvtpk(v1[6],v1[7])};
      } }
    wodma(6);
    { const u32 tb=(4u*h+q)*128u+8u*(p&1u);
      f32x16 d[4];
      #pragma unroll
      for(int hh=0;hh<2;hh++)
        #pragma unroll
        for(int c=0;c<2;c++){
          const u32 a0=L+P1_SCR+w*9216u+hh*4096u+tb+(((4u*c+2u*blk+(p>>1))^(((q>>1)&1u)<<2))*16u);
          d[hh*2+c]=mfma32(trfrag(a0+2048u,a0+3072u),bv[hh][1],mfma32(trfrag(a0,a0+1024u),bv[hh][0],Z));
        }
      wodma(7);
      #pragma unroll
      for(int t=0;t<4;t++){ const u32 o=L+P1_SCR+w*9216u+t*2304u;
        ldsw128f(o+lane*16u,f32x4{d[t][0],d[t][1],d[t][2],d[t][3]}); ldsw128f(o+1024u+lane*16u,f32x4{d[t][4],d[t][5],d[t][6],d[t][7]}); ldsw32f(o+2048u+lane*4u,d[t][8]); } }
    wodma(8);
    BAR();
    { f32x4 a={}, c={}; float e=0.f;
      #pragma unroll
      for(int ww=0;ww<4;ww++){ const u32 o=L+P1_SCR+ww*9216u+w*2304u; a+=ldsr128f(o+lane*16u); c+=ldsr128f(o+1024u+lane*16u); e+=ldsr32f(o+2048u+lane*4u); }
      const __amdgpu_buffer_rsrc_t rk=mkrsrc(W.KVP,16u*64u*KVP_TILE);
      const u32 so=((i*32u+2u*j+(w>>1))*2u+(w&1u))*KVP_TILE;
      st128sc(u32x4{cvtpk(a[0],a[1]),cvtpk(a[2],a[3]),cvtpk(c[0],c[1]),cvtpk(c[2],c[3])},rk,lane*16u,so); st32sc(__float_as_uint(e),rk,1024u+lane*4u,so); }

    STAMP(5);
    publish(W.fl, FL_F1+j*256u+i*8u, ep);
    STAMP(6);
    #pragma unroll
    for(int hh=0;hh<2;hh++){
      Soft s; dual_softmax(hh?pq1:pq0,s);
      bqf[hh][0]=u32x4{cvtpk(s.pos[0],s.pos[1]),cvtpk(s.pos[2],s.pos[3]),cvtpk(s.pos[4],s.pos[5]),cvtpk(s.pos[6],s.pos[7])};
      bqf[hh][1]=u32x4{cvtpk(s.neg[0],s.neg[1]),cvtpk(s.neg[2],s.neg[3]),cvtpk(s.neg[4],s.neg[5]),cvtpk(s.neg[6],s.neg[7])};
      bq2[hh]=cvtpk(s.pos[8],s.neg[8]);
    }
   }
  }
  const u32 th=j>>3, ng=j&7u;
  {
    const u32 lane=lane_id_opaque(), tid=w*64u+lane;
    const __amdgpu_buffer_rsrc_t ro=mkrsrc(wo,576u*1152u);
    if(tid<8u*12u) ldsw128(L+576u*192u+tid*16u,u32x4{0,0,0,0});
    #pragma unroll
    for(int ps=0;ps<2;ps++){
      const u32 row=ps*32u+(tid>>3);
      if(ps==0 || w==0u){
        const bool ok=row<36u;
        float s=0.f; u32x4 c9[9];
        #pragma unroll
        for(int m=0;m<9;m++) c9[m]=__builtin_amdgcn_raw_buffer_load_b128(ro, ok? ((lane&7u)+8u*m)*16u+row*1152u : 0x7fffff00u, 36u*j*1152u, 0);
        #pragma unroll
        for(int m=0;m<9;m++){
          #pragma unroll
          for(int e=0;e<4;e++) s=dot2(c9[m][e],ONES,s); }
        s+=__shfl_xor(s,1); s+=__shfl_xor(s,2); s+=__shfl_xor(s,4);
        if((lane&7u)==0u && row<40u) ldsw32f(L+P2_WB+row*4u, s*(1.0f/576.0f));
      }
    }
  }

  STAMP(7);
  wait16(W.fl, FL_F1+j*256u, ep);
  STAMP(8);

  {
    const u32 lane=lane_id_opaque(), r=lane&31u, h=lane>>5;
    const __amdgpu_buffer_rsrc_t rk=mkrsrc(W.KVP,16u*64u*KVP_TILE);
    f32x4 a={}, c={}; float e=0.f;
    { const u32 so=((2u*j+(w>>1))*2u+(w&1u))*KVP_TILE;
      u32x4 la[16]; u32 le[16];
      #pragma unroll
      for(int t=0;t<16;t++){ la[t]=ld128sc(rk,lane*16u,so+t*64u*KVP_TILE); le[t]=ld32sc(rk,1024u+lane*4u,so+t*64u*KVP_TILE); }
      #pragma unroll
      for(int t=0;t<16;t++){ a+=f32x4{bflo(la[t][0]),bfhi(la[t][0]),bflo(la[t][1]),bfhi(la[t][1])}; c+=f32x4{bflo(la[t][2]),bfhi(la[t][2]),bflo(la[t][3]),bfhi(la[t][3])}; e+=__uint_as_float(le[t]); } }
    STAMP(9);
    const u32 hd=w>>1;
    const bool valid=(r==27u)||(r==31u)||((r>=2u*hd)&&(r<2u*hd+18u));
    if(!valid){ a=f32x4{}; c=f32x4{}; e=0.f; }
    ldsw128(L+P2_AF+(hd*2u+(w&1u))*1024u+lane*16u, u32x4{cvtpk(a[0],a[1]),cvtpk(a[2],a[3]),cvtpk(c[0],c[1]),cvtpk(c[2],c[3])});
    ldsw16(L+P2_AF+4096u+hd*256u+lane*4u+(w&1u)*2u,(u16)(cvtpk(e,0.f)&0xffffu));
    BAR();
    float o[2][12];
    #pragma unroll
    for(int hh=0;hh<2;hh++){
      const u32x4 a0=ldsr128(L+P2_AF+(hh*2u)*1024u+lane*16u), a1=ldsr128(L+P2_AF+(hh*2u+1u)*1024u+lane*16u);
      const u32 a2=*(LAS u32*)(size_t)(L+P2_AF+4096u+hh*256u+lane*4u);
      f32x16 d=mfma32(a0,bqf[hh][0],Z); d=mfma32(a1,bqf[hh][1],d); d=mfma32(u32x4{a2,0,0,0},u32x4{bq2[hh],0,0,0},d);
      const float rd=1.0f/(d[15]+1e-6f);
      #pragma unroll
      for(int k=0;k<12;k++) o[hh][k]=d[k]*rd;
    }
    const bool h0=(h==0u);
    const u32x4 f0={cvtpk(o[0][0],o[0][1]),cvtpk(o[0][2],o[0][3]),cvtpk(o[0][4],o[0][5]),cvtpk(o[0][6],o[0][7])};
    const u32x4 f1={h0?0u:cvtpk(o[1][0],o[1][1]),cvtpk(o[1][2],o[1][3]),cvtpk(o[1][4],o[1][5]),cvtpk(o[1][6],o[1][7])};
    const u32x4 f2={h0?cvtpk(o[0][8],o[0][9]):0u,h0?cvtpk(o[1][8],o[1][9]):0u,h0?cvtpk(o[1][10],o[1][11]):0u,0u};
    const __amdgpu_buffer_rsrc_t rO=mkrsrc(W.O,64u*48u*1024u);
    const u32 so=(tt32*16u+j)*3072u;
    st128sc(f0,rO,lane*16u,so); st128sc(f1,rO,1024u+lane*16u,so); st128sc(f2,rO,2048u+lane*16u,so);
    float t=0.f;
    { const u32 wb=L+P2_WB;
      const f32x4 w0=ldsr128f(wb+(4u*h)*4u), w1=ldsr128f(wb+(8u+4u*h)*4u), w2=ldsr128f(wb+(16u+4u*h)*4u), w3=ldsr128f(wb+(24u+4u*h)*4u), w4=ldsr128f(wb+32u*4u);
      t+=bflo(f0[0])*w0[0]+bfhi(f0[0])*w0[1]+bflo(f0[1])*w0[2]+bfhi(f0[1])*w0[3]+bflo(f0[2])*w1[0]+bfhi(f0[2])*w1[1]+bflo(f0[3])*w1[2]+bfhi(f0[3])*w1[3];
      t+=bflo(f1[0])*w2[0]+bfhi(f1[0])*w2[1]+bflo(f1[1])*w2[2]+bfhi(f1[1])*w2[3]+bflo(f1[2])*w3[0]+bfhi(f1[2])*w3[1]+bflo(f1[3])*w3[2]+bfhi(f1[3])*w3[3];
      t+=bflo(f2[0])*w2[0]+bfhi(f2[0])*w2[1]+bflo(f2[1])*w4[0]+bfhi(f2[1])*w4[1]+bflo(f2[2])*w4[2]+bfhi(f2[2])*w4[3];
    }
    t=addhalves(t);
    if(h0) st32sc(__float_as_uint(t), mkrsrc(W.TP,16u*16u*128u*4u), (32u*w+r)*4u, (i*16u+j)*512u);

    STAMP(10);
    publish(W.fl, FL_F2+i*256u+j*8u, ep);
    STAMP(11);
  }
  const u32 omt=w>>1, oT=w&1u, ott=4u*i+2u*th+omt;
  u32x2 xq[5];
  {
    const u32 lane=lane_id_opaque(), r=lane&31u, h=lane>>5;
    #pragma unroll
    for(int g=0;g<5;g++){ const u32 n=72u*ng+32u*oT+8u*g+4u*h; const bool ok=(g<4)||(oT==1u);
      const u32x2 v=__builtin_bit_cast(u32x2,__builtin_amdgcn_raw_buffer_load_b64(rx, ok? (ott*32u+r)*1152u+n*2u : 0x7fffff00u, 0, 0)); xq[g]=v; }
  }
  wait16(W.fl, FL_F2+i*256u, ep);
  STAMP(12);

  if(blockIdx.x==0 && threadIdx.x==0) __hip_atomic_store((gu64*)W.fl, (unsigned long long)ep, __ATOMIC_RELAXED, __HIP_MEMORY_SCOPE_AGENT);
  {
    const u32 lane=lane_id_opaque(), r=lane&31u, h=lane>>5, blk=(lane>>4)&1u, q=(lane>>2)&3u, p=lane&3u, G=4u*blk+p;
    const __amdgpu_buffer_rsrc_t rO=mkrsrc(W.O,64u*48u*1024u);
    u32x4 of[2][12];
    #pragma unroll
    for(int mt=0;mt<2;mt++)
      #pragma unroll
      for(int k=0;k<12;k++) of[mt][k]=ld128sc(rO,lane*16u,((4u*i+2u*th+mt)*48u+w*12u+k)*1024u);
    float mean;
    { const __amdgpu_buffer_rsrc_t rt=mkrsrc(W.TP,16u*16u*128u*4u);
      u32 tp[8];
      #pragma unroll
      for(int k=0;k<8;k++) tp[k]=ld32sc(rt,((2u*th+omt)*32u+r)*4u+(h*8u+k)*512u,i*16u*512u);
      float s=0.f;
      #pragma unroll
      for(int k=0;k<8;k++) s+=__uint_as_float(tp[k]);
      mean=addhalves(s)+__uint_as_float(ld32sc(mkrsrc(W.MX,8192u),(ott*32u+r)*4u,0)); }
    u32 ro[3][2];
    ro[0][0]=4u*h+q; ro[0][1]=8u+4u*h+q; ro[1][0]=16u+4u*h+q; ro[1][1]=24u+4u*h+q;
    ro[2][0]=(q<2u)? 16u+4u*h+q : 32u+4u*h+(q-2u); ro[2][1]=(q<2u)? 34u+4u*h+q : 0u;
    const u32 ub=2u*(ng&3u);
    u32 aa[3][2];
    #pragma unroll
    for(int s=0;s<3;s++)
      #pragma unroll
      for(int t=0;t<2;t++) aa[s][t]=L+(w*144u+ro[s][t])*192u;
    const u32 cu0=(ub+G)*8u, cu1=(ub+8u+G)*8u, cu2=(ub+16u+(G<2u?G:1u))*8u;
    f32x16 acc[2][3]={};
    u32x4 wf[2][3];
    auto rd=[&](int st, int bsel){ const int jj=st/3, s=st%3; const u32 a0=aa[s][0]+jj*36u*192u, a1=aa[s][1]+jj*36u*192u;
      wf[bsel][0]=trfrag(a0+cu0,a1+cu0); wf[bsel][1]=trfrag(a0+cu1,a1+cu1); wf[bsel][2]=trfrag(a0+cu2,a1+cu2); };
    rd(0,0);
    #pragma unroll
    for(int st=0;st<12;st++){
      if(st+1<12) rd(st+1,(st+1)&1);
      #pragma unroll
      for(int t=0;t<3;t++){ acc[0][t]=mfma32(wf[st&1][t],of[0][st],acc[0][t]); acc[1][t]=mfma32(wf[st&1][t],of[1][st],acc[1][t]); }
    }

    STAMP(13);
    BAR();
    { const u32 o=L+w*24576u+lane*16u;
      #pragma unroll
      for(int mt=0;mt<2;mt++)
        #pragma unroll
        for(int t=0;t<3;t++){
          const bool own=((u32)mt==omt)&&((t==0)? (oT==0u) : (oT==1u));
          if(!own){
            #pragma unroll
            for(int g4=0;g4<(t==2?1:4);g4++) ldsw128f(o+(mt*3+t)*4096u+g4*1024u,f32x4{acc[mt][t][4*g4],acc[mt][t][4*g4+1],acc[mt][t][4*g4+2],acc[mt][t][4*g4+3]}); } } }
    BAR();
    float v[20];
    #pragma unroll
    for(int e=0;e<16;e++) v[e]=(omt==0u)? ((oT==0u)?acc[0][0][e]:acc[0][1][e]) : ((oT==0u)?acc[1][0][e]:acc[1][1][e]);
    #pragma unroll
    for(int e=0;e<4;e++) v[16+e]=(omt==0u)?acc[0][2][e]:acc[1][2][e];
    #pragma unroll
    for(int ww=0;ww<4;ww++) if((u32)ww!=w){
      const u32 o=L+ww*24576u+lane*16u+(omt*3u+oT)*4096u;
      #pragma unroll
      for(int g4=0;g4<4;g4++){ const f32x4 pv=ldsr128f(o+g4*1024u);
        #pragma unroll
        for(int e=0;e<4;e++) v[4*g4+e]+=pv[e]; }
      if(oT==1u){ const f32x4 pv=ldsr128f(o+4096u);
        #pragma unroll
        for(int e=0;e<4;e++) v[16+e]+=pv[e]; }
    }
    float sq=0.f;
    const __amdgpu_buffer_rsrc_t rh=mkrsrc(W.HC,64u*36u*1024u);
    #pragma unroll
    for(int g=0;g<5;g++){
      if(g<4 || oT==1u){
        const u32 d0=cvtpk(v[4*g]+bflo(xq[g][0])-mean, v[4*g+1]+bfhi(xq[g][0])-mean), d1=cvtpk(v[4*g+2]+bflo(xq[g][1])-mean, v[4*g+3]+bfhi(xq[g][1])-mean);
        sq+=bflo(d0)*bflo(d0)+bfhi(d0)*bfhi(d0)+bflo(d1)*bflo(d1)+bfhi(d1)*bfhi(d1);
        const u32 gam=9u*ng+4u*oT+g;
        st64sc(u32x2{d0,d1}, rh, lane*16u+(gam&1u)*8u, (ott*36u+(gam>>1))*1024u);
      }
    }
    sq=addhalves(sq);
    if(h==0u){ st32sc(__float_as_uint(sq), mkrsrc(W.SSQ,2048u*64u), (ott*32u+r)*64u+(ng*2u+oT)*4u, 0);
      if(ng==0u && oT==0u) st32sc(__float_as_uint(mean), mkrsrc(W.MH,8192u), (ott*32u+r)*4u, 0); }
  }
   STAMP(14); STAMP_OUT(0,15);
  return ep;
}

DI void mlp_phase(const char* __restrict__ HC, const float* __restrict__ ssq, const u16* __restrict__ w1, const u16* __restrict__ w3, const u16* __restrict__ w2,
                  char* __restrict__ P, char* __restrict__ fl, const u32 ep, const u32 ldsb, const u32 tt, const u32 j, const u32 w, u32* dbg){
  const u32 lane=lane_id_opaque();
  const u32 fh=j&1u, f0=288u*j, tt32=4u*tt+w;
  const u32 mat=w>>1, wr=w&1u;
  __amdgpu_buffer_rsrc_t srdW =mkrsrc(mat? w3:w1,576u*9216u);
  __amdgpu_buffer_rsrc_t srdW2=mkrsrc(w2,4608u*1152u);
  __amdgpu_buffer_rsrc_t srdHC=mkrsrc(HC+(size_t)tt32*36864,36864u);
  __amdgpu_buffer_rsrc_t srdP =mkrsrc(P+((size_t)j*64+tt32)*36864,36864u);
  const u32 rot=tt&3u;
  const u32 fcb=f0+(fh?32u:0u), fhs=f0+(fh?0u:256u);
  u32 fc[5], w2o[5];
  #pragma unroll
  for(int q=0;q<4;q++){ u32 c=(q+rot)&3u; u32 f=fcb+64u*c; fc[q]=96u*wr*9216u+f*2u; w2o[q]=(f+16u*w)*1152u; }
  fc[4]=96u*wr*9216u+fhs*2u; w2o[4]=(fhs+8u*w)*1152u;
  const u32 ldsW=ldsb+mat*24576u+96u*wr*128u, ldsH=ldsb+mat*12288u+96u*wr*64u, lds2=ldsb+2u*w*1024u, lds2h=ldsb+32768u+w*1024u;
  const u32 p0=tt%9u;
  const float* ssqp=ssq+(size_t)tt32*32*16;
  const u32 h=lane>>5, blk=(lane>>4)&1u, q=(lane>>2)&3u, p=lane&3u;
  const u32 vbase=ldsb+(4u*h+q)*128u+(((2u*blk+(p>>1))^(((q>>1)&1u)<<2))*16u)+8u*(p&1u);
  const u32 vbaseH=ldsb+(4u*h+q)*64u+(2u*blk+(p>>1))*16u+8u*(p&1u);
  const u32 r8=lane>>3, sw=((lane&7u)^(((r8>>1)&1u)<<2))*16u;
  const u32 vvoff=r8*9216u+sw, vvoffH=(lane>>2)*9216u+(lane&3u)*16u, vvoff2=r8*1152u+sw;
  const char* flg=fl+FL_F3+tt*256u;
  const unsigned long long want=flagval(ep);
  asm volatile(K3_ASM_BODY
     :: "s"(srdW),"s"(srdW2),"s"(srdHC),"s"(srdP),
        "s"(fc[0]),"s"(fc[1]),"s"(fc[2]),"s"(fc[3]),"s"(fc[4]),
        "s"(w2o[0]),"s"(w2o[1]),"s"(w2o[2]),"s"(w2o[3]),"s"(w2o[4]),
        "s"(ldsW),"s"(ldsH),"s"(lds2),"s"(p0),"s"(ssqp),
        "v"(vbase),"v"(vbaseH),"v"(vvoff),"v"(vvoffH),"v"(vvoff2),"v"(lane),"s"(lds2h),"s"(dbg),"s"(flg),"s"(want)
     : K3_ASM_CLOBBERS);
}

__global__ __launch_bounds__(256) void k4_reduce(const u16* __restrict__ P, const u16* __restrict__ HC, const float* __restrict__ mean, u16* __restrict__ out){
  const u32 idx=blockIdx.x*256u+threadIdx.x;
  const u32 lane=idx&63u, blk=idx>>6, tt32=blk/36u, kap=blk%36u;
  const u32x4* Pp=(const u32x4*)P+idx;
  u32x4 v[16];
  #pragma unroll
  for(int j=0;j<16;j++) v[j]=__builtin_nontemporal_load(Pp+(size_t)j*(64*36*64));
  const u32x4 hc=((const u32x4*)HC)[idx];
  const u32 tok=tt32*32u+(lane&31u);
  const float m=mean[tok];
  float a[8];
  #pragma unroll
  for(int e=0;e<4;e++){ a[2*e]=bflo(hc[e])+m; a[2*e+1]=bfhi(hc[e])+m; }
  #pragma unroll
  for(int j=0;j<16;j++){
    #pragma unroll
    for(int e=0;e<4;e++){ a[2*e]+=bflo(v[j][e]); a[2*e+1]+=bfhi(v[j][e]); }
  }
  u32x2 o0={cvtpk(a[0],a[1]),cvtpk(a[2],a[3])}, o1={cvtpk(a[4],a[5]),cvtpk(a[6],a[7])};
  u16* op=out+(size_t)tok*576+16u*kap+4u*(lane>>5);
  *(u32x2*)op=o0; *(u32x2*)(op+8)=o1;
}

__global__ __launch_bounds__(256,1) void k_mega(const u16* __restrict__ x, const u16* __restrict__ wqkv, const u16* __restrict__ wphi, const u16* __restrict__ wo,
   const u16* __restrict__ w1, const u16* __restrict__ w3, const u16* __restrict__ w2, char* __restrict__ ws){
  extern __shared__ __attribute__((aligned(16))) char lds[];
  const u32 w=rfl(threadIdx.x>>6);
  const u32 b=blockIdx.x, xc=b&7u, lo=b>>3, i=(xc>>2)*8u+(lo>>2), j=(xc&3u)*4u+(lo&3u);
  const u32 epv=(u32)__hip_atomic_load((gu64*)ws, __ATOMIC_RELAXED, __HIP_MEMORY_SCOPE_AGENT);
  AttnWs W; W.fl=ws; W.KVP=(float*)(ws+WS_KVP); W.O=ws+WS_O; W.TP=(float*)(ws+WS_TP); W.MX=(float*)(ws+WS_MX); W.HC=ws+WS_HC; W.MH=(float*)(ws+WS_MH); W.SSQ=(float*)(ws+WS_SSQ);
  const u32 L=(u32)(size_t)lds;
  const u32 ep=attn_phases(x,wqkv,wphi,wo,w1,w3,w2,W,L,i,j,w,epv);
  publish(ws, FL_F3+i*256u+j*8u, ep);
  mlp_phase(ws+WS_HC,(const float*)(ws+WS_SSQ),w1,w3,w2,ws+WS_P,ws,ep,L,i,j,w,(u32*)(ws+WS_DBG)+(blockIdx.x*4u+w)*64u);
}

extern "C" void kernel_launch(void* const* d_in, const int* in_sizes, int n_in, void* d_out, int out_size, void* d_ws, size_t ws_size, hipStream_t stream){
  const u16* x=(const u16*)d_in[0]; const u16* wqkv=(const u16*)d_in[1]; const u16* wphi=(const u16*)d_in[2]; const u16* wo=(const u16*)d_in[3];
  const u16* w1=(const u16*)d_in[5]; const u16* w3=(const u16*)d_in[6]; const u16* w2=(const u16*)d_in[7];
  char* ws=(char*)d_ws;
  (void)hipFuncSetAttribute((const void*)k_mega, hipFuncAttributeMaxDynamicSharedMemorySize, ATT_LDS);
  k_mega<<<256,256,ATT_LDS,stream>>>(x,wqkv,wphi,wo,w1,w3,w2,ws);
  k4_reduce<<<576,256,0,stream>>>((const u16*)(ws+WS_P),(const u16*)(ws+WS_HC),(const float*)(ws+WS_MH),(u16*)d_out);
}
```
